```python
import torch

def _softmax(x, axis=-1):
    return torch.softmax(x, dim=axis)
def _gelu(x):
    return torch.nn.functional.gelu(x, approximate="tanh")
def _silu(x):
    return torch.nn.functional.silu(x)
def _sigmoid(x):
    return torch.sigmoid(x)
def _layernorm(x, axis=-1, eps=1e-5):
    m = x.mean(dim=axis, keepdim=True)
    v = x.var(dim=axis, keepdim=True, unbiased=False)
    return (x - m) / torch.sqrt(v + eps)
def _scatter_add(tgt, idx, src, axis=0):
    return tgt.clone().index_add_(axis, idx, src)
def _segment_sum(src, seg, n):
    out = torch.zeros((n,) + src.shape[1:], dtype=src.dtype)
    return out.index_add_(0, seg, src)
def _pad(x, pw):
    flat = tuple(v for pair in reversed(pw) for v in pair)
    return torch.nn.functional.pad(x, flat)

kernel_name = 'gated_mix__grouped_kv_mqa__grouped_kv_mqa__prompt'

def setup_inputs():

    _w = (lambda a: (lambda t: t.to(torch.bfloat16) if t.is_floating_point() else t)(torch.as_tensor(a)))
    def _A_setup():
        B, S, D, H = 1, 2048, 640, 32
        Dh = D // H
        return {
            "x": _w((lambda *s: torch.randn(*s, dtype=torch.bfloat16) * 0.05)(B, S, D)),
            "wq": _w((lambda *s: torch.randn(*s, dtype=torch.bfloat16) * 0.8)(D, H * Dh)),
            "wk": _w((lambda *s: torch.randn(*s, dtype=torch.bfloat16) * 0.8)(D, Dh)),
            "wv": _w((lambda *s: torch.randn(*s, dtype=torch.bfloat16) * 0.05)(D, Dh)),
            "wo": _w((lambda *s: torch.randn(*s, dtype=torch.bfloat16) * 0.05)(H * Dh, D)),
        }
    _A_in = _A_setup()

    def _B_setup():
        B, S, D, H = 1, 2048, 640, 32
        Dh = D // H
        return {
            "x": _w((lambda *s: torch.randn(*s, dtype=torch.bfloat16) * 0.05)(B, S, D)),
            "wq": _w((lambda *s: torch.randn(*s, dtype=torch.bfloat16) * 0.8)(D, H * Dh)),
            "wk": _w((lambda *s: torch.randn(*s, dtype=torch.bfloat16) * 0.8)(D, Dh)),
            "wv": _w((lambda *s: torch.randn(*s, dtype=torch.bfloat16) * 0.05)(D, Dh)),
            "wo": _w((lambda *s: torch.randn(*s, dtype=torch.bfloat16) * 0.05)(H * Dh, D)),
        }
    _B_in = _B_setup()

    return {
        "A_x": _A_in["x"],
        "A_wq": _A_in["wq"],
        "A_wk": _A_in["wk"],
        "A_wv": _A_in["wv"],
        "A_wo": _A_in["wo"],
        "B_x": _B_in["x"],
        "B_wq": _B_in["wq"],
        "B_wk": _B_in["wk"],
        "B_wv": _B_in["wv"],
        "B_wo": _B_in["wo"],
        "gate_w": _w((lambda *s: torch.randn(*s, dtype=torch.bfloat16) * 0.05)(640) * 16.0),
    }

def reference(A_x, A_wq, A_wk, A_wv, A_wo, B_x, B_wq, B_wk, B_wv, B_wo, gate_w):
    def _A_ref(x, wq, wk, wv, wo):
        B, S, D = x.shape
        H = 32
        Dh = D // H
        q = (lambda x, *d: x.permute(*d))((x @ wq).reshape(B, S, H, Dh), 0, 2, 1, 3)
        k = x @ wk
        v = x @ wv
        scores = torch.einsum("bhqd,bkd->bhqk", q, k) * (Dh ** -0.5)
        p = _softmax(scores, axis=-1)
        o = torch.einsum("bhqk,bkd->bhqd", p, v)
        return (lambda x, *d: x.permute(*d))(o, 0, 2, 1, 3).reshape(B, S, H * Dh) @ wo

    def _B_ref(x, wq, wk, wv, wo):
        B, S, D = x.shape
        H = 32
        Dh = D // H
        q = (lambda x, *d: x.permute(*d))((x @ wq).reshape(B, S, H, Dh), 0, 2, 1, 3)
        k = x @ wk
        v = x @ wv
        scores = torch.einsum("bhqd,bkd->bhqk", q, k) * (Dh ** -0.5)
        p = _softmax(scores, axis=-1)
        o = torch.einsum("bhqk,bkd->bhqd", p, v)
        return (lambda x, *d: x.permute(*d))(o, 0, 2, 1, 3).reshape(B, S, H * Dh) @ wo

    x = A_x
    g = _sigmoid((x * gate_w).sum(-1, keepdims=True))
    a = _A_ref(x, A_wq, A_wk, A_wv, A_wo)
    b = _B_ref(x, B_wq, B_wk, B_wv, B_wo)
    return g * a + (1 - g) * b
```

```cpp
#include <hip/hip_runtime.h>
#include <hip/hip_bf16.h>
#include <stdint.h>

#define KERNEL_ASM \
    \
  "s_load_dwordx4 s[12:15], s[0:1], 0x0\n\t" \
  "s_and_b32 s4, s2, 1\n\t" \
  "s_bfe_u32 s8, s2, 0x10004\n\t" \
  "s_lshl_b32 s50, s4, 3\n\t" \
  "s_lshl4_add_u32 s51, s8, s50\n\t" \
  "s_load_dwordx2 s[16:17], s[0:1], s51 offset:0x40\n\t" \
  "s_load_dwordx2 s[18:19], s[0:1], s50 offset:0x30\n\t" \
  "s_load_dwordx8 s[20:27], s[0:1], 0x10\n\t" \
    \
  "97:\n\t" \
  "s_getpc_b64 s[58:59]\n\t" \
  "s_lshr_b32 s52, s2, 3\n\t" \
  "v_lshrrev_b32 v253, 6, v0\n\t" \
  "v_bfe_u32 v254, v0, 5, 1\n\t" \
  "v_lshl_add_u32 v253, v253, 5, s52\n\t" \
  "v_lshl_add_u32 v253, v254, 8, v253\n\t" \
  "v_lshlrev_b32 v253, 7, v253\n\t" \
  "v_min_u32 v253, 99f-97b-8, v253\n\t" \
  "global_load_dword v251, v253, s[58:59]\n\t" \
  "v_readfirstlane_b32 s3, v0\n\t" \
  "v_and_b32 v1, 63, v0\n\t" \
  "v_lshlrev_b32 v2, 4, v1\n\t" \
  "s_lshr_b32 s3, s3, 6\n\t" \
  "s_bfe_u32 s7, s2, 0x10003\n\t" \
  "s_bfe_u32 s52, s2, 0x20001\n\t" \
  "s_lshr_b32 s53, s2, 5\n\t" \
  "s_lshl3_add_u32 s5, s52, s53\n\t" \
  "s_and_b32 s9, s5, 3\n\t" \
  "s_mul_i32 s9, s9, 10\n\t" \
  "s_lshr_b32 s9, s9, 2\n\t" \
  "s_mul_i32 s52, s5, 81920\n\t" \
  "v_lshrrev_b32 v8, 3, v1\n\t" \
  "v_mul_u32_u24 v8, 1280, v8\n\t" \
  "v_lshrrev_b32 v9, 4, v1\n\t" \
  "v_xor_b32 v9, v9, v1\n\t" \
  "v_and_b32 v9, 7, v9\n\t" \
  "v_lshl_add_u32 v8, v9, 4, v8\n\t" \
  "s_cmp_gt_u32 s3, 4\n\t" \
  "s_cbranch_scc1 100f\n\t" \
    \
  "s_mul_i32 s53, s7, 40960\n\t" \
  "s_lshl_b32 s50, s3, 8\n\t" \
  "s_add_u32 s54, s50, s53\n\t" \
  "s_add_u32 s54, s54, s52\n\t" \
  "v_add_u32 v8, s54, v8\n\t" \
  "v_xor_b32 v9, 64, v8\n\t" \
  "v_add_u32 v9, 9216, v9\n\t" \
  "v_add_u32 v10, 18432, v8\n\t" \
  "v_add_u32 v11, 18432, v9\n\t" \
  "s_lshl_b32 s55, s3, 1\n\t" \
  "s_sub_u32 s55, s55, s9\n\t" \
  "s_add_u32 s38, s55, 10\n\t" \
  "s_min_u32 s54, s55, s38\n\t" \
  "s_lshl_b32 s55, s54, 12\n\t" \
  "s_add_u32 s55, s55, s53\n\t" \
  "s_add_u32 s55, s55, 20480\n\t" \
  "s_mov_b32 m0, s55\n\t" \
  "s_mul_i32 s56, s3, 5120\n\t" \
  "v_add_u32 v13, s56, v2\n\t" \
  "s_add_u32 s57, s56, 143360\n\t" \
  "s_setprio 2\n\t" \
  "s_cmp_eq_u32 s3, 4\n\t" \
  "s_cselect_b32 s57, 0, s57\n\t" \
  "s_cbranch_scc0 109f\n\t" \
  "s_setprio 3\n\t" \
  "109:\n\t" \
  "s_waitcnt lgkmcnt(0)\n\t" \
  "global_load_lds_dwordx4 v8, s[12:13]\n\t" \
  "global_load_lds_dwordx4 v9, s[12:13] offset:1024\n\t" \
  "global_load_lds_dwordx4 v10, s[12:13] offset:2048\n\t" \
  "global_load_lds_dwordx4 v11, s[12:13] offset:3072\n\t" \
  "s_mov_b32 m0, s57\n\t" \
  "s_cmp_eq_u32 s54, 9\n\t" \
  "global_load_lds_dwordx4 v13, s[16:17]\n\t" \
  "global_load_lds_dwordx4 v13, s[16:17] offset:1024\n\t" \
  "global_load_lds_dwordx4 v13, s[16:17] offset:2048\n\t" \
  "s_cselect_b32 s38, -9, 1\n\t" \
  "s_lshl_b32 s38, s38, 12\n\t" \
  "s_add_u32 m0, s50, 17408\n\t" \
  "v_lshl_add_u32 v12, v1, 2, s50\n\t" \
  "global_load_lds_dword v12, s[20:21]\n\t" \
  "s_add_u32 m0, s55, s38\n\t" \
  "s_add_u32 s58, s12, 128\n\t" \
  "s_addc_u32 s59, s13, 0\n\t" \
  "global_load_lds_dwordx4 v8, s[58:59]\n\t" \
  "global_load_lds_dwordx4 v9, s[58:59] offset:1024\n\t" \
  "global_load_lds_dwordx4 v10, s[58:59] offset:2048\n\t" \
  "global_load_lds_dwordx4 v11, s[58:59] offset:3072\n\t" \
  "s_add_u32 m0, s57, 3072\n\t" \
  "s_add_u32 s60, s16, 3072\n\t" \
  "s_addc_u32 s61, s17, 0\n\t" \
  "global_load_lds_dwordx4 v13, s[60:61]\n\t" \
  "global_load_lds_dwordx4 v13, s[60:61] offset:1024\n\t" \
  "s_load_dwordx2 s[64:65], s[14:15], 0x0\n\t" \
  "s_bfe_u32 s6, s2, 0x20003\n\t" \
  "s_bfe_u32 s10, s2, 0x40003\n\t" \
  "v_and_b32 v3, 31, v0\n\t" \
  "v_bfe_u32 v4, v0, 5, 1\n\t" \
  "v_lshl_add_u32 v250, v4, 4, s50\n\t" \
    \
  "v_and_b32 v8, 19, v3\n\t" \
  "v_and_b32 v9, 4, v3\n\t" \
  "v_lshl_add_u32 v8, v9, 1, v8\n\t" \
  "v_bfe_u32 v9, v3, 3, 1\n\t" \
  "v_lshl_add_u32 v8, v9, 2, v8\n\t" \
  "s_cmp_eq_u32 s8, 0\n\t" \
  "s_cselect_b64 vcc, -1, 0\n\t" \
  "v_cndmask_b32 v8, v8, v3, vcc\n\t" \
  "v_bfe_u32 v9, v8, 1, 3\n\t" \
  "v_xor_b32 v9, v9, v4\n\t" \
  "v_lshlrev_b32 v9, 4, v9\n\t" \
  "v_lshl_add_u32 v228, v8, 7, v9\n\t" \
  "v_add_u32 v228, s55, v228\n\t" \
  "v_xor_b32 v229, 32, v228\n\t" \
  "v_xor_b32 v230, 64, v228\n\t" \
  "v_xor_b32 v231, 0x60, v228\n\t" \
  "v_add_u32 v252, s38, v228\n\t" \
  "v_add_u32 v253, s38, v229\n\t" \
  "v_add_u32 v254, s38, v230\n\t" \
  "v_add_u32 v255, s38, v231\n\t" \
  "v_bfe_u32 v5, v1, 2, 2\n\t" \
  "v_lshl_add_u32 v5, v4, 3, v5\n\t" \
  "v_and_b32 v9, 3, v1\n\t" \
  "v_lshlrev_b32 v9, 3, v9\n\t" \
  "v_and_b32 v10, 16, v1\n\t" \
  "v_cmp_eq_u32 s[36:37], 0, v10\n\t" \
  "v_mul_u32_u24 v232, 40, v5\n\t" \
  "s_nop 0\n\t" \
  "v_cndmask_b32 v9, 32, v9, s[36:37]\n\t" \
  "v_add3_u32 v232, v232, v9, s57\n\t" \
  "v_min_u32 v9, 19, v3\n\t" \
  "v_mad_u32_u24 v9, v4, 20, v9\n\t" \
  "v_cndmask_b32 v9, v9, v1, vcc\n\t" \
  "v_lshlrev_b32 v233, 4, v9\n\t" \
  "s_cmp_eq_u32 s3, 4\n\t" \
  "s_cbranch_scc1 110f\n\t" \
  "v_mov_b64 v[72:73], 0\n\t" \
  "v_mov_b64 v[74:75], 0\n\t" \
  "v_mov_b64 v[76:77], 0\n\t" \
  "v_mov_b64 v[78:79], 0\n\t" \
  "v_and_b32 v10, 16, v1\n\t" \
  "v_cmp_ne_u32 vcc, 0, v10\n\t" \
  "v_mov_b32 v8, 0x0c0a0200\n\t" \
  "v_mov_b32 v10, 0x3a083412\n\t" \
  "v_cndmask_b32 v8, v8, v10, vcc\n\t" \
  "v_mov_b32 v9, 0x0604100e\n\t" \
  "v_mov_b32 v10, 0x38363e3c\n\t" \
  "v_cndmask_b32 v9, v9, v10, vcc\n\t" \
  "v_and_b32 v10, 14, v1\n\t" \
  "v_lshlrev_b32 v10, 2, v10\n\t" \
  "v_lshrrev_b64 v[8:9], v10, v[8:9]\n\t" \
  "v_and_b32 v8, 0xff, v8\n\t" \
  "v_cmp_gt_u32 vcc, 20, v8\n\t" \
  "v_and_b32 v10, 1, v1\n\t" \
  "v_add_u32 v8, v8, v10\n\t" \
  "v_cndmask_b32_e64 v10, -12, 20, vcc\n\t" \
  "v_cmp_lt_u32 vcc, 31, v1\n\t" \
  "s_nop 1\n\t" \
  "v_cndmask_b32 v10, 0, v10, vcc\n\t" \
  "v_add_u32 v110, v8, v10\n\t" \
  "v_mfma_f32_32x32x16_bf16 v[112:127], v[72:75], v[72:75], 0\n\t" \
  "v_lshlrev_b32 v110, 4, v110\n\t" \
  "v_lshlrev_b32 v111, 3, v1\n\t" \
  "v_and_b32 v8, 27, v1\n\t" \
  "v_cmp_eq_u32 vcc, 18, v8\n\t" \
  "v_mov_b32 v9, 0x3f803f80\n\t" \
  "v_mov_b32 v144, 0xdddddddd\n\t" \
  "v_cndmask_b32 v88, 0, v9, vcc\n\t" \
  "v_mov_b32 v89, v88\n\t" \
  "v_mfma_f32_32x32x16_bf16 v[128:143], v[72:75], v[72:75], 0\n\t" \
  "v_mov_b64 v[90:91], v[88:89]\n\t" \
  "v_mov_b64 v[92:93], v[88:89]\n\t" \
  "v_mov_b64 v[94:95], v[88:89]\n\t" \
  "v_mov_b64 v[96:97], v[88:89]\n\t" \
  "v_mov_b64 v[98:99], v[88:89]\n\t" \
  "s_mov_b32 s34, 0x0033ffff\n\t" \
  "s_mov_b32 s35, 0x0033ffff\n\t" \
  "s_mov_b32 s28, s14\n\t" \
  "s_and_b32 s29, s15, 0xffff\n\t" \
  "s_mov_b32 s30, 7274496\n\t" \
  "s_mov_b32 s31, 0x20000\n\t" \
  "s_cmp_lt_u32 s3, 4\n\t" \
  "s_cselect_b32 s41, 1, 0\n\t" \
  "s_lshl_b32 s54, s4, 5\n\t" \
  "s_add_u32 s54, s54, s5\n\t" \
  "s_and_b32 s53, s3, 1\n\t" \
  "s_lshl1_add_u32 s54, s54, s53\n\t" \
  "s_lshl_b32 s54, s54, 7\n\t" \
  "s_add_u32 s54, s54, 16384\n\t" \
  "v_lshl_add_u32 v145, v3, 2, s54\n\t" \
  "s_mul_i32 s54, s4, 163840\n\t" \
  "s_add_u32 s54, s54, 65536\n\t" \
  "s_add_u32 s48, s14, s54\n\t" \
  "s_addc_u32 s49, s15, 0\n\t" \
  "s_and_b32 s50, s3, 1\n\t" \
  "s_mul_i32 s50, s50, 40960\n\t" \
  "s_add_u32 s50, s50, 20480\n\t" \
  "v_lshrrev_b32 v6, 4, v1\n\t" \
  "v_bfe_u32 v9, v1, 1, 3\n\t" \
  "v_xor_b32 v9, v9, v6\n\t" \
  "v_lshlrev_b32 v9, 4, v9\n\t" \
  "v_and_b32 v10, 15, v1\n\t" \
  "v_lshl_add_u32 v244, v10, 7, v9\n\t" \
  "v_add_u32 v244, s50, v244\n\t" \
  "v_xor_b32 v245, 64, v244\n\t" \
  "s_lshr_b32 s50, s3, 1\n\t" \
  "s_mul_i32 s50, s50, 160\n\t" \
  "v_bfe_u32 v5, v1, 2, 2\n\t" \
  "v_lshl_add_u32 v5, v6, 3, v5\n\t" \
  "v_mul_u32_u24 v5, 320, v5\n\t" \
  "v_add_u32 v5, 102400, v5\n\t" \
  "v_and_b32 v6, 16, v1\n\t" \
  "v_lshl_add_u32 v6, v6, 1, s50\n\t" \
  "v_and_b32 v9, 1, v1\n\t" \
  "v_bfe_u32 v10, v1, 1, 1\n\t" \
  "v_lshl_add_u32 v9, v9, 1, v10\n\t" \
  "v_lshl_add_u32 v8, v9, 3, v6\n\t" \
  "v_add_u32 v246, v5, v8\n\t" \
  "v_add_u32 v8, 120, v8\n\t" \
  "v_subrev_u32 v11, 320, v8\n\t" \
  "v_min_u32 v8, v8, v11\n\t" \
  "v_add_u32 v247, v5, v8\n\t" \
  "v_mul_u32_u24 v9, 40, v9\n\t" \
  "v_add3_u32 v8, v9, v6, 32\n\t" \
  "v_subrev_u32 v11, 320, v8\n\t" \
  "v_min_u32 v8, v8, v11\n\t" \
  "v_add_u32 v248, v5, v8\n\t" \
  "s_mul_i32 s50, s3, 3072\n\t" \
  "v_add_u32 v9, s50, v233\n\t" \
  "s_cmp_eq_u32 s8, 0\n\t" \
  "s_cbranch_scc1 101f\n\t" \
  "s_waitcnt vmcnt(6) lgkmcnt(0)\n\t" \
  "ds_read_b128 v[148:151], v228 offset:0\n\t" \
  "ds_read_b64_tr_b16 v[180:181], v232 offset:0\n\t" \
  "ds_read_b64_tr_b16 v[182:183], v232 offset:160\n\t" \
  "ds_read_b128 v[152:155], v229 offset:0\n\t" \
  "ds_read_b64_tr_b16 v[184:185], v232 offset:640\n\t" \
  "ds_read_b64_tr_b16 v[186:187], v232 offset:800\n\t" \
  "ds_read_b128 v[156:159], v230 offset:0\n\t" \
  "ds_read_b64_tr_b16 v[188:189], v232 offset:1280\n\t" \
  "ds_read_b64_tr_b16 v[190:191], v232 offset:1440\n\t" \
  "ds_read_b128 v[160:163], v231 offset:0\n\t" \
  "ds_read_b64_tr_b16 v[192:193], v232 offset:1920\n\t" \
  "ds_read_b64_tr_b16 v[194:195], v232 offset:2080\n\t" \
  "s_add_u32 s64, s64, 1\n\t" \
  "s_addc_u32 s65, s65, 0\n\t" \
  "s_waitcnt lgkmcnt(9)\n\t" \
  "v_mfma_f32_32x32x16_bf16 v[212:227], v[148:151], v[180:183], 0\n\t" \
  "s_waitcnt lgkmcnt(6)\n\t" \
  "v_mfma_f32_32x32x16_bf16 v[212:227], v[152:155], v[184:187], v[212:227]\n\t" \
  "s_waitcnt lgkmcnt(3)\n\t" \
  "v_mfma_f32_32x32x16_bf16 v[212:227], v[156:159], v[188:191], v[212:227]\n\t" \
  "s_waitcnt lgkmcnt(0)\n\t" \
  "v_mfma_f32_32x32x16_bf16 v[212:227], v[160:163], v[192:195], v[212:227]\n\t" \
  "s_waitcnt vmcnt(0)\n\t" \
  "ds_read_b128 v[164:167], v252 offset:0\n\t" \
  "ds_read_b64_tr_b16 v[196:197], v232 offset:2560\n\t" \
  "ds_read_b64_tr_b16 v[198:199], v232 offset:2720\n\t" \
  "ds_read_b128 v[168:171], v253 offset:0\n\t" \
  "ds_read_b64_tr_b16 v[200:201], v232 offset:3200\n\t" \
  "ds_read_b64_tr_b16 v[202:203], v232 offset:3360\n\t" \
  "ds_read_b128 v[172:175], v254 offset:0\n\t" \
  "ds_read_b64_tr_b16 v[204:205], v232 offset:3840\n\t" \
  "ds_read_b64_tr_b16 v[206:207], v232 offset:4000\n\t" \
  "ds_read_b128 v[176:179], v255 offset:0\n\t" \
  "ds_read_b64_tr_b16 v[208:209], v232 offset:4480\n\t" \
  "ds_read_b64_tr_b16 v[210:211], v232 offset:4640\n\t" \
  "s_waitcnt lgkmcnt(9)\n\t" \
  "v_mfma_f32_32x32x16_bf16 v[212:227], v[164:167], v[196:199], v[212:227]\n\t" \
  "s_waitcnt lgkmcnt(6)\n\t" \
  "v_mfma_f32_32x32x16_bf16 v[212:227], v[168:171], v[200:203], v[212:227]\n\t" \
  "s_waitcnt lgkmcnt(3)\n\t" \
  "v_mfma_f32_32x32x16_bf16 v[212:227], v[172:175], v[204:207], v[212:227]\n\t" \
  "s_waitcnt lgkmcnt(0)\n\t" \
  "v_mfma_f32_32x32x16_bf16 v[212:227], v[176:179], v[208:211], v[212:227]\n\t" \
  "s_nop 9\n\t" \
  "s_mov_b32 exec_lo, 0xfffff\n\t" \
  "s_mov_b32 exec_hi, 0xfffff\n\t" \
  "ds_write_b128 v9, v[212:215] offset:5120\n\t" \
  "ds_write_b128 v9, v[216:219] offset:5760\n\t" \
  "ds_write_b128 v9, v[220:223] offset:6400\n\t" \
  "ds_write_b128 v9, v[224:227] offset:7040\n\t" \
  "s_mov_b64 exec, -1\n\t" \
  "s_branch 102f\n\t" \
  "101:\n\t" \
  "s_waitcnt vmcnt(6) lgkmcnt(0)\n\t" \
  "ds_read_b128 v[148:151], v228 offset:0\n\t" \
  "ds_read_b64_tr_b16 v[180:181], v232 offset:0\n\t" \
  "ds_read_b64_tr_b16 v[182:183], v232 offset:160\n\t" \
  "ds_read_b128 v[152:155], v229 offset:0\n\t" \
  "ds_read_b64_tr_b16 v[184:185], v232 offset:640\n\t" \
  "ds_read_b64_tr_b16 v[186:187], v232 offset:800\n\t" \
  "ds_read_b128 v[156:159], v230 offset:0\n\t" \
  "ds_read_b64_tr_b16 v[188:189], v232 offset:1280\n\t" \
  "ds_read_b64_tr_b16 v[190:191], v232 offset:1440\n\t" \
  "ds_read_b128 v[160:163], v231 offset:0\n\t" \
  "ds_read_b64_tr_b16 v[192:193], v232 offset:1920\n\t" \
  "ds_read_b64_tr_b16 v[194:195], v232 offset:2080\n\t" \
  "s_mov_b32 exec_lo, 0x100000\n\t" \
  "s_mov_b32 exec_hi, 0x100000\n\t" \
  "ds_read_b128 v[180:183], v250 offset:17408\n\t" \
  "ds_read_b128 v[184:187], v250 offset:17440\n\t" \
  "ds_read_b128 v[188:191], v250 offset:17472\n\t" \
  "ds_read_b128 v[192:195], v250 offset:17504\n\t" \
  "s_mov_b64 exec, -1\n\t" \
  "s_add_u32 s64, s64, 1\n\t" \
  "s_addc_u32 s65, s65, 0\n\t" \
  "s_waitcnt lgkmcnt(3)\n\t" \
  "v_mfma_f32_32x32x16_bf16 v[212:227], v[180:183], v[148:151], 0\n\t" \
  "s_waitcnt lgkmcnt(2)\n\t" \
  "v_mfma_f32_32x32x16_bf16 v[212:227], v[184:187], v[152:155], v[212:227]\n\t" \
  "s_waitcnt lgkmcnt(1)\n\t" \
  "v_mfma_f32_32x32x16_bf16 v[212:227], v[188:191], v[156:159], v[212:227]\n\t" \
  "s_waitcnt lgkmcnt(0)\n\t" \
  "v_mfma_f32_32x32x16_bf16 v[212:227], v[192:195], v[160:163], v[212:227]\n\t" \
  "s_waitcnt vmcnt(0)\n\t" \
  "ds_read_b128 v[164:167], v252 offset:0\n\t" \
  "ds_read_b64_tr_b16 v[196:197], v232 offset:2560\n\t" \
  "ds_read_b64_tr_b16 v[198:199], v232 offset:2720\n\t" \
  "ds_read_b128 v[168:171], v253 offset:0\n\t" \
  "ds_read_b64_tr_b16 v[200:201], v232 offset:3200\n\t" \
  "ds_read_b64_tr_b16 v[202:203], v232 offset:3360\n\t" \
  "ds_read_b128 v[172:175], v254 offset:0\n\t" \
  "ds_read_b64_tr_b16 v[204:205], v232 offset:3840\n\t" \
  "ds_read_b64_tr_b16 v[206:207], v232 offset:4000\n\t" \
  "ds_read_b128 v[176:179], v255 offset:0\n\t" \
  "ds_read_b64_tr_b16 v[208:209], v232 offset:4480\n\t" \
  "ds_read_b64_tr_b16 v[210:211], v232 offset:4640\n\t" \
  "s_mov_b32 exec_lo, 0x100000\n\t" \
  "s_mov_b32 exec_hi, 0x100000\n\t" \
  "ds_read_b128 v[196:199], v250 offset:17536\n\t" \
  "ds_read_b128 v[200:203], v250 offset:17568\n\t" \
  "ds_read_b128 v[204:207], v250 offset:17600\n\t" \
  "ds_read_b128 v[208:211], v250 offset:17632\n\t" \
  "s_mov_b64 exec, -1\n\t" \
  "s_waitcnt lgkmcnt(3)\n\t" \
  "v_mfma_f32_32x32x16_bf16 v[212:227], v[196:199], v[164:167], v[212:227]\n\t" \
  "s_waitcnt lgkmcnt(2)\n\t" \
  "v_mfma_f32_32x32x16_bf16 v[212:227], v[200:203], v[168:171], v[212:227]\n\t" \
  "s_waitcnt lgkmcnt(1)\n\t" \
  "v_mfma_f32_32x32x16_bf16 v[212:227], v[204:207], v[172:175], v[212:227]\n\t" \
  "s_waitcnt lgkmcnt(0)\n\t" \
  "v_mfma_f32_32x32x16_bf16 v[212:227], v[208:211], v[176:179], v[212:227]\n\t" \
  "s_nop 11\n\t" \
  "ds_write_b128 v9, v[212:215] offset:5120\n\t" \
  "ds_write_b128 v9, v[216:219] offset:6144\n\t" \
  "ds_write_b128 v9, v[220:223] offset:7168\n\t" \
  "102:\n\t" \
    \
    \
  "s_waitcnt lgkmcnt(0)\n\t" \
  "s_barrier\n\t" \
  "ds_read_b128 v[16:19], v244 offset:0\n\t" \
  "ds_read_b128 v[20:23], v244 offset:2048\n\t" \
  "ds_read_b64_tr_b16 v[24:25], v248 offset:0\n\t" \
  "ds_read_b64_tr_b16 v[26:27], v248 offset:1280\n\t" \
  "ds_read_b64_tr_b16 v[28:29], v246 offset:0\n\t" \
  "ds_read_b64_tr_b16 v[30:31], v246 offset:1280\n\t" \
  "ds_read_b64_tr_b16 v[32:33], v246 offset:40\n\t" \
  "ds_read_b64_tr_b16 v[34:35], v246 offset:1320\n\t" \
  "ds_read_b64_tr_b16 v[36:37], v246 offset:80\n\t" \
  "ds_read_b64_tr_b16 v[38:39], v246 offset:1360\n\t" \
  "ds_read_b64_tr_b16 v[40:41], v247 offset:0\n\t" \
  "ds_read_b64_tr_b16 v[42:43], v247 offset:1280\n\t" \
  "ds_read_b128 v[188:191], v245 offset:0\n\t" \
  "ds_read_b128 v[192:195], v245 offset:2048\n\t" \
  "ds_read_b64_tr_b16 v[196:197], v248 offset:10240\n\t" \
  "ds_read_b64_tr_b16 v[198:199], v248 offset:11520\n\t" \
  "ds_read_b64_tr_b16 v[200:201], v246 offset:10240\n\t" \
  "ds_read_b64_tr_b16 v[202:203], v246 offset:11520\n\t" \
  "ds_read_b64_tr_b16 v[204:205], v246 offset:10280\n\t" \
  "ds_read_b64_tr_b16 v[206:207], v246 offset:11560\n\t" \
  "ds_read_b64_tr_b16 v[208:209], v246 offset:10320\n\t" \
  "ds_read_b64_tr_b16 v[210:211], v246 offset:11600\n\t" \
  "ds_read_b64_tr_b16 v[212:213], v247 offset:10240\n\t" \
  "ds_read_b64_tr_b16 v[214:215], v247 offset:11520\n\t" \
    \
  "s_waitcnt lgkmcnt(0)\n\t" \
  "s_barrier\n\t" \
  "ds_read_b128 v[44:47], v244 offset:4096\n\t" \
  "ds_read_b128 v[48:51], v244 offset:6144\n\t" \
  "v_mfma_f32_16x16x32_bf16 v[148:151], v[24:27], v[16:19], 0\n\t" \
  "ds_read_b64_tr_b16 v[52:53], v248 offset:20480\n\t" \
  "ds_read_b64_tr_b16 v[54:55], v248 offset:21760\n\t" \
  "v_mfma_f32_16x16x32_bf16 v[152:155], v[24:27], v[20:23], 0\n\t" \
  "ds_read_b64_tr_b16 v[56:57], v246 offset:20480\n\t" \
  "ds_read_b64_tr_b16 v[58:59], v246 offset:21760\n\t" \
  "v_mfma_f32_16x16x32_bf16 v[156:159], v[28:31], v[16:19], 0\n\t" \
  "ds_read_b64_tr_b16 v[60:61], v246 offset:20520\n\t" \
  "ds_read_b64_tr_b16 v[62:63], v246 offset:21800\n\t" \
  "v_mfma_f32_16x16x32_bf16 v[160:163], v[28:31], v[20:23], 0\n\t" \
  "ds_read_b64_tr_b16 v[64:65], v246 offset:20560\n\t" \
  "ds_read_b64_tr_b16 v[66:67], v246 offset:21840\n\t" \
  "v_mfma_f32_16x16x32_bf16 v[164:167], v[32:35], v[16:19], 0\n\t" \
  "ds_read_b64_tr_b16 v[68:69], v247 offset:20480\n\t" \
  "ds_read_b64_tr_b16 v[70:71], v247 offset:21760\n\t" \
  "v_mfma_f32_16x16x32_bf16 v[168:171], v[32:35], v[20:23], 0\n\t" \
  "ds_read_b128 v[216:219], v245 offset:4096\n\t" \
  "ds_read_b128 v[220:223], v245 offset:6144\n\t" \
  "v_mfma_f32_16x16x32_bf16 v[172:175], v[36:39], v[16:19], 0\n\t" \
  "ds_read_b64_tr_b16 v[224:225], v248 offset:30720\n\t" \
  "ds_read_b64_tr_b16 v[226:227], v248 offset:32000\n\t" \
  "v_mfma_f32_16x16x32_bf16 v[176:179], v[36:39], v[20:23], 0\n\t" \
  "ds_read_b64_tr_b16 v[228:229], v246 offset:30720\n\t" \
  "ds_read_b64_tr_b16 v[230:231], v246 offset:32000\n\t" \
  "v_mfma_f32_16x16x32_bf16 v[180:183], v[40:43], v[16:19], 0\n\t" \
  "ds_read_b64_tr_b16 v[232:233], v246 offset:30760\n\t" \
  "ds_read_b64_tr_b16 v[234:235], v246 offset:32040\n\t" \
  "v_mfma_f32_16x16x32_bf16 v[184:187], v[40:43], v[20:23], 0\n\t" \
  "ds_read_b64_tr_b16 v[236:237], v246 offset:30800\n\t" \
  "ds_read_b64_tr_b16 v[238:239], v246 offset:32080\n\t" \
  "v_mfma_f32_16x16x32_bf16 v[148:151], v[196:199], v[188:191], v[148:151]\n\t" \
  "ds_read_b64_tr_b16 v[240:241], v247 offset:30720\n\t" \
  "ds_read_b64_tr_b16 v[242:243], v247 offset:32000\n\t" \
  "v_mfma_f32_16x16x32_bf16 v[152:155], v[196:199], v[192:195], v[152:155]\n\t" \
  "v_mfma_f32_16x16x32_bf16 v[156:159], v[200:203], v[188:191], v[156:159]\n\t" \
  "v_mfma_f32_16x16x32_bf16 v[160:163], v[200:203], v[192:195], v[160:163]\n\t" \
  "v_mfma_f32_16x16x32_bf16 v[164:167], v[204:207], v[188:191], v[164:167]\n\t" \
  "v_mfma_f32_16x16x32_bf16 v[168:171], v[204:207], v[192:195], v[168:171]\n\t" \
  "v_mfma_f32_16x16x32_bf16 v[172:175], v[208:211], v[188:191], v[172:175]\n\t" \
  "v_mfma_f32_16x16x32_bf16 v[176:179], v[208:211], v[192:195], v[176:179]\n\t" \
  "v_mfma_f32_16x16x32_bf16 v[180:183], v[212:215], v[188:191], v[180:183]\n\t" \
  "v_mfma_f32_16x16x32_bf16 v[184:187], v[212:215], v[192:195], v[184:187]\n\t" \
    \
  "s_waitcnt lgkmcnt(0)\n\t" \
  "s_barrier\n\t" \
  "ds_read_b128 v[16:19], v244 offset:8192\n\t" \
  "ds_read_b128 v[20:23], v244 offset:10240\n\t" \
  "v_mfma_f32_16x16x32_bf16 v[148:151], v[52:55], v[44:47], v[148:151]\n\t" \
  "ds_read_b64_tr_b16 v[24:25], v248 offset:40960\n\t" \
  "ds_read_b64_tr_b16 v[26:27], v248 offset:42240\n\t" \
  "v_mfma_f32_16x16x32_bf16 v[152:155], v[52:55], v[48:51], v[152:155]\n\t" \
  "ds_read_b64_tr_b16 v[28:29], v246 offset:40960\n\t" \
  "ds_read_b64_tr_b16 v[30:31], v246 offset:42240\n\t" \
  "v_mfma_f32_16x16x32_bf16 v[156:159], v[56:59], v[44:47], v[156:159]\n\t" \
  "ds_read_b64_tr_b16 v[32:33], v246 offset:41000\n\t" \
  "ds_read_b64_tr_b16 v[34:35], v246 offset:42280\n\t" \
  "v_mfma_f32_16x16x32_bf16 v[160:163], v[56:59], v[48:51], v[160:163]\n\t" \
  "ds_read_b64_tr_b16 v[36:37], v246 offset:41040\n\t" \
  "ds_read_b64_tr_b16 v[38:39], v246 offset:42320\n\t" \
  "v_mfma_f32_16x16x32_bf16 v[164:167], v[60:63], v[44:47], v[164:167]\n\t" \
  "ds_read_b64_tr_b16 v[40:41], v247 offset:40960\n\t" \
  "ds_read_b64_tr_b16 v[42:43], v247 offset:42240\n\t" \
  "v_mfma_f32_16x16x32_bf16 v[168:171], v[60:63], v[48:51], v[168:171]\n\t" \
  "ds_read_b128 v[188:191], v245 offset:8192\n\t" \
  "ds_read_b128 v[192:195], v245 offset:10240\n\t" \
  "v_mfma_f32_16x16x32_bf16 v[172:175], v[64:67], v[44:47], v[172:175]\n\t" \
  "ds_read_b64_tr_b16 v[196:197], v248 offset:51200\n\t" \
  "ds_read_b64_tr_b16 v[198:199], v248 offset:52480\n\t" \
  "v_mfma_f32_16x16x32_bf16 v[176:179], v[64:67], v[48:51], v[176:179]\n\t" \
  "ds_read_b64_tr_b16 v[200:201], v246 offset:51200\n\t" \
  "ds_read_b64_tr_b16 v[202:203], v246 offset:52480\n\t" \
  "v_mfma_f32_16x16x32_bf16 v[180:183], v[68:71], v[44:47], v[180:183]\n\t" \
  "ds_read_b64_tr_b16 v[204:205], v246 offset:51240\n\t" \
  "ds_read_b64_tr_b16 v[206:207], v246 offset:52520\n\t" \
  "v_mfma_f32_16x16x32_bf16 v[184:187], v[68:71], v[48:51], v[184:187]\n\t" \
  "ds_read_b64_tr_b16 v[208:209], v246 offset:51280\n\t" \
  "ds_read_b64_tr_b16 v[210:211], v246 offset:52560\n\t" \
  "v_mfma_f32_16x16x32_bf16 v[148:151], v[224:227], v[216:219], v[148:151]\n\t" \
  "ds_read_b64_tr_b16 v[212:213], v247 offset:51200\n\t" \
  "ds_read_b64_tr_b16 v[214:215], v247 offset:52480\n\t" \
  "v_mfma_f32_16x16x32_bf16 v[152:155], v[224:227], v[220:223], v[152:155]\n\t" \
  "v_mfma_f32_16x16x32_bf16 v[156:159], v[228:231], v[216:219], v[156:159]\n\t" \
  "v_mfma_f32_16x16x32_bf16 v[160:163], v[228:231], v[220:223], v[160:163]\n\t" \
  "v_mfma_f32_16x16x32_bf16 v[164:167], v[232:235], v[216:219], v[164:167]\n\t" \
  "v_mfma_f32_16x16x32_bf16 v[168:171], v[232:235], v[220:223], v[168:171]\n\t" \
  "v_mfma_f32_16x16x32_bf16 v[172:175], v[236:239], v[216:219], v[172:175]\n\t" \
  "v_mfma_f32_16x16x32_bf16 v[176:179], v[236:239], v[220:223], v[176:179]\n\t" \
  "v_mfma_f32_16x16x32_bf16 v[180:183], v[240:243], v[216:219], v[180:183]\n\t" \
  "v_mfma_f32_16x16x32_bf16 v[184:187], v[240:243], v[220:223], v[184:187]\n\t" \
    \
  "s_waitcnt lgkmcnt(0)\n\t" \
  "s_barrier\n\t" \
  "ds_read_b128 v[44:47], v244 offset:12288\n\t" \
  "ds_read_b128 v[48:51], v244 offset:14336\n\t" \
  "v_mfma_f32_16x16x32_bf16 v[148:151], v[24:27], v[16:19], v[148:151]\n\t" \
  "ds_read_b64_tr_b16 v[52:53], v248 offset:0\n\t" \
  "ds_read_b64_tr_b16 v[54:55], v248 offset:1280\n\t" \
  "v_mfma_f32_16x16x32_bf16 v[152:155], v[24:27], v[20:23], v[152:155]\n\t" \
  "ds_read_b64_tr_b16 v[56:57], v246 offset:0\n\t" \
  "ds_read_b64_tr_b16 v[58:59], v246 offset:1280\n\t" \
  "v_mfma_f32_16x16x32_bf16 v[156:159], v[28:31], v[16:19], v[156:159]\n\t" \
  "ds_read_b64_tr_b16 v[60:61], v246 offset:40\n\t" \
  "ds_read_b64_tr_b16 v[62:63], v246 offset:1320\n\t" \
  "v_mfma_f32_16x16x32_bf16 v[160:163], v[28:31], v[20:23], v[160:163]\n\t" \
  "ds_read_b64_tr_b16 v[64:65], v246 offset:80\n\t" \
  "ds_read_b64_tr_b16 v[66:67], v246 offset:1360\n\t" \
  "v_mfma_f32_16x16x32_bf16 v[164:167], v[32:35], v[16:19], v[164:167]\n\t" \
  "ds_read_b64_tr_b16 v[68:69], v247 offset:0\n\t" \
  "ds_read_b64_tr_b16 v[70:71], v247 offset:1280\n\t" \
  "v_mfma_f32_16x16x32_bf16 v[168:171], v[32:35], v[20:23], v[168:171]\n\t" \
  "ds_read_b128 v[216:219], v245 offset:12288\n\t" \
  "ds_read_b128 v[220:223], v245 offset:14336\n\t" \
  "v_mfma_f32_16x16x32_bf16 v[172:175], v[36:39], v[16:19], v[172:175]\n\t" \
  "ds_read_b64_tr_b16 v[224:225], v248 offset:10240\n\t" \
  "ds_read_b64_tr_b16 v[226:227], v248 offset:11520\n\t" \
  "v_mfma_f32_16x16x32_bf16 v[176:179], v[36:39], v[20:23], v[176:179]\n\t" \
  "ds_read_b64_tr_b16 v[228:229], v246 offset:10240\n\t" \
  "ds_read_b64_tr_b16 v[230:231], v246 offset:11520\n\t" \
  "v_mfma_f32_16x16x32_bf16 v[180:183], v[40:43], v[16:19], v[180:183]\n\t" \
  "ds_read_b64_tr_b16 v[232:233], v246 offset:10280\n\t" \
  "ds_read_b64_tr_b16 v[234:235], v246 offset:11560\n\t" \
  "v_mfma_f32_16x16x32_bf16 v[184:187], v[40:43], v[20:23], v[184:187]\n\t" \
  "ds_read_b64_tr_b16 v[236:237], v246 offset:10320\n\t" \
  "ds_read_b64_tr_b16 v[238:239], v246 offset:11600\n\t" \
  "v_mfma_f32_16x16x32_bf16 v[148:151], v[196:199], v[188:191], v[148:151]\n\t" \
  "ds_read_b64_tr_b16 v[240:241], v247 offset:10240\n\t" \
  "ds_read_b64_tr_b16 v[242:243], v247 offset:11520\n\t" \
  "v_mfma_f32_16x16x32_bf16 v[152:155], v[196:199], v[192:195], v[152:155]\n\t" \
  "v_mfma_f32_16x16x32_bf16 v[156:159], v[200:203], v[188:191], v[156:159]\n\t" \
  "v_mfma_f32_16x16x32_bf16 v[160:163], v[200:203], v[192:195], v[160:163]\n\t" \
  "v_mfma_f32_16x16x32_bf16 v[164:167], v[204:207], v[188:191], v[164:167]\n\t" \
  "v_mfma_f32_16x16x32_bf16 v[168:171], v[204:207], v[192:195], v[168:171]\n\t" \
  "v_mfma_f32_16x16x32_bf16 v[172:175], v[208:211], v[188:191], v[172:175]\n\t" \
  "v_mfma_f32_16x16x32_bf16 v[176:179], v[208:211], v[192:195], v[176:179]\n\t" \
  "v_mfma_f32_16x16x32_bf16 v[180:183], v[212:215], v[188:191], v[180:183]\n\t" \
  "v_mfma_f32_16x16x32_bf16 v[184:187], v[212:215], v[192:195], v[184:187]\n\t" \
    \
  "s_waitcnt lgkmcnt(0)\n\t" \
  "s_barrier\n\t" \
  "ds_read_b128 v[16:19], v244 offset:16384\n\t" \
  "ds_read_b128 v[20:23], v244 offset:18432\n\t" \
  "v_mfma_f32_16x16x32_bf16 v[148:151], v[52:55], v[44:47], v[148:151]\n\t" \
  "ds_read_b64_tr_b16 v[24:25], v248 offset:20480\n\t" \
  "ds_read_b64_tr_b16 v[26:27], v248 offset:21760\n\t" \
  "v_mfma_f32_16x16x32_bf16 v[152:155], v[52:55], v[48:51], v[152:155]\n\t" \
  "ds_read_b64_tr_b16 v[28:29], v246 offset:20480\n\t" \
  "ds_read_b64_tr_b16 v[30:31], v246 offset:21760\n\t" \
  "v_mfma_f32_16x16x32_bf16 v[156:159], v[56:59], v[44:47], v[156:159]\n\t" \
  "ds_read_b64_tr_b16 v[32:33], v246 offset:20520\n\t" \
  "ds_read_b64_tr_b16 v[34:35], v246 offset:21800\n\t" \
  "v_mfma_f32_16x16x32_bf16 v[160:163], v[56:59], v[48:51], v[160:163]\n\t" \
  "ds_read_b64_tr_b16 v[36:37], v246 offset:20560\n\t" \
  "ds_read_b64_tr_b16 v[38:39], v246 offset:21840\n\t" \
  "v_mfma_f32_16x16x32_bf16 v[164:167], v[60:63], v[44:47], v[164:167]\n\t" \
  "ds_read_b64_tr_b16 v[40:41], v247 offset:20480\n\t" \
  "ds_read_b64_tr_b16 v[42:43], v247 offset:21760\n\t" \
  "v_mfma_f32_16x16x32_bf16 v[168:171], v[60:63], v[48:51], v[168:171]\n\t" \
  "ds_read_b128 v[188:191], v245 offset:16384\n\t" \
  "ds_read_b128 v[192:195], v245 offset:18432\n\t" \
  "v_mfma_f32_16x16x32_bf16 v[172:175], v[64:67], v[44:47], v[172:175]\n\t" \
  "ds_read_b64_tr_b16 v[196:197], v248 offset:30720\n\t" \
  "ds_read_b64_tr_b16 v[198:199], v248 offset:32000\n\t" \
  "v_mfma_f32_16x16x32_bf16 v[176:179], v[64:67], v[48:51], v[176:179]\n\t" \
  "ds_read_b64_tr_b16 v[200:201], v246 offset:30720\n\t" \
  "ds_read_b64_tr_b16 v[202:203], v246 offset:32000\n\t" \
  "v_mfma_f32_16x16x32_bf16 v[180:183], v[68:71], v[44:47], v[180:183]\n\t" \
  "ds_read_b64_tr_b16 v[204:205], v246 offset:30760\n\t" \
  "ds_read_b64_tr_b16 v[206:207], v246 offset:32040\n\t" \
  "v_mfma_f32_16x16x32_bf16 v[184:187], v[68:71], v[48:51], v[184:187]\n\t" \
  "ds_read_b64_tr_b16 v[208:209], v246 offset:30800\n\t" \
  "ds_read_b64_tr_b16 v[210:211], v246 offset:32080\n\t" \
  "v_mfma_f32_16x16x32_bf16 v[148:151], v[224:227], v[216:219], v[148:151]\n\t" \
  "ds_read_b64_tr_b16 v[212:213], v247 offset:30720\n\t" \
  "ds_read_b64_tr_b16 v[214:215], v247 offset:32000\n\t" \
  "v_mfma_f32_16x16x32_bf16 v[152:155], v[224:227], v[220:223], v[152:155]\n\t" \
  "v_mfma_f32_16x16x32_bf16 v[156:159], v[228:231], v[216:219], v[156:159]\n\t" \
  "v_mfma_f32_16x16x32_bf16 v[160:163], v[228:231], v[220:223], v[160:163]\n\t" \
  "v_mfma_f32_16x16x32_bf16 v[164:167], v[232:235], v[216:219], v[164:167]\n\t" \
  "v_mfma_f32_16x16x32_bf16 v[168:171], v[232:235], v[220:223], v[168:171]\n\t" \
  "v_mfma_f32_16x16x32_bf16 v[172:175], v[236:239], v[216:219], v[172:175]\n\t" \
  "v_mfma_f32_16x16x32_bf16 v[176:179], v[236:239], v[220:223], v[176:179]\n\t" \
  "v_mfma_f32_16x16x32_bf16 v[180:183], v[240:243], v[216:219], v[180:183]\n\t" \
  "v_mfma_f32_16x16x32_bf16 v[184:187], v[240:243], v[220:223], v[184:187]\n\t" \
    \
  "s_waitcnt lgkmcnt(0)\n\t" \
  "s_barrier\n\t" \
  "ds_read_b128 v[44:47], v244 offset:20480\n\t" \
  "ds_read_b128 v[48:51], v244 offset:22528\n\t" \
  "v_mfma_f32_16x16x32_bf16 v[148:151], v[24:27], v[16:19], v[148:151]\n\t" \
  "ds_read_b64_tr_b16 v[52:53], v248 offset:40960\n\t" \
  "ds_read_b64_tr_b16 v[54:55], v248 offset:42240\n\t" \
  "v_mfma_f32_16x16x32_bf16 v[152:155], v[24:27], v[20:23], v[152:155]\n\t" \
  "ds_read_b64_tr_b16 v[56:57], v246 offset:40960\n\t" \
  "ds_read_b64_tr_b16 v[58:59], v246 offset:42240\n\t" \
  "v_mfma_f32_16x16x32_bf16 v[156:159], v[28:31], v[16:19], v[156:159]\n\t" \
  "ds_read_b64_tr_b16 v[60:61], v246 offset:41000\n\t" \
  "ds_read_b64_tr_b16 v[62:63], v246 offset:42280\n\t" \
  "v_mfma_f32_16x16x32_bf16 v[160:163], v[28:31], v[20:23], v[160:163]\n\t" \
  "ds_read_b64_tr_b16 v[64:65], v246 offset:41040\n\t" \
  "ds_read_b64_tr_b16 v[66:67], v246 offset:42320\n\t" \
  "v_mfma_f32_16x16x32_bf16 v[164:167], v[32:35], v[16:19], v[164:167]\n\t" \
  "ds_read_b64_tr_b16 v[68:69], v247 offset:40960\n\t" \
  "ds_read_b64_tr_b16 v[70:71], v247 offset:42240\n\t" \
  "v_mfma_f32_16x16x32_bf16 v[168:171], v[32:35], v[20:23], v[168:171]\n\t" \
  "ds_read_b128 v[216:219], v245 offset:20480\n\t" \
  "ds_read_b128 v[220:223], v245 offset:22528\n\t" \
  "v_mfma_f32_16x16x32_bf16 v[172:175], v[36:39], v[16:19], v[172:175]\n\t" \
  "ds_read_b64_tr_b16 v[224:225], v248 offset:51200\n\t" \
  "ds_read_b64_tr_b16 v[226:227], v248 offset:52480\n\t" \
  "v_mfma_f32_16x16x32_bf16 v[176:179], v[36:39], v[20:23], v[176:179]\n\t" \
  "ds_read_b64_tr_b16 v[228:229], v246 offset:51200\n\t" \
  "ds_read_b64_tr_b16 v[230:231], v246 offset:52480\n\t" \
  "v_mfma_f32_16x16x32_bf16 v[180:183], v[40:43], v[16:19], v[180:183]\n\t" \
  "ds_read_b64_tr_b16 v[232:233], v246 offset:51240\n\t" \
  "ds_read_b64_tr_b16 v[234:235], v246 offset:52520\n\t" \
  "v_mfma_f32_16x16x32_bf16 v[184:187], v[40:43], v[20:23], v[184:187]\n\t" \
  "ds_read_b64_tr_b16 v[236:237], v246 offset:51280\n\t" \
  "ds_read_b64_tr_b16 v[238:239], v246 offset:52560\n\t" \
  "v_mfma_f32_16x16x32_bf16 v[148:151], v[196:199], v[188:191], v[148:151]\n\t" \
  "ds_read_b64_tr_b16 v[240:241], v247 offset:51200\n\t" \
  "ds_read_b64_tr_b16 v[242:243], v247 offset:52480\n\t" \
  "v_mfma_f32_16x16x32_bf16 v[152:155], v[196:199], v[192:195], v[152:155]\n\t" \
  "v_mfma_f32_16x16x32_bf16 v[156:159], v[200:203], v[188:191], v[156:159]\n\t" \
  "v_mfma_f32_16x16x32_bf16 v[160:163], v[200:203], v[192:195], v[160:163]\n\t" \
  "v_mfma_f32_16x16x32_bf16 v[164:167], v[204:207], v[188:191], v[164:167]\n\t" \
  "v_mfma_f32_16x16x32_bf16 v[168:171], v[204:207], v[192:195], v[168:171]\n\t" \
  "v_mfma_f32_16x16x32_bf16 v[172:175], v[208:211], v[188:191], v[172:175]\n\t" \
  "v_mfma_f32_16x16x32_bf16 v[176:179], v[208:211], v[192:195], v[176:179]\n\t" \
  "v_mfma_f32_16x16x32_bf16 v[180:183], v[212:215], v[188:191], v[180:183]\n\t" \
  "v_mfma_f32_16x16x32_bf16 v[184:187], v[212:215], v[192:195], v[184:187]\n\t" \
    \
  "s_waitcnt lgkmcnt(0)\n\t" \
  "s_barrier\n\t" \
  "ds_read_b128 v[16:19], v244 offset:24576\n\t" \
  "ds_read_b128 v[20:23], v244 offset:26624\n\t" \
  "v_mfma_f32_16x16x32_bf16 v[148:151], v[52:55], v[44:47], v[148:151]\n\t" \
  "ds_read_b64_tr_b16 v[24:25], v248 offset:0\n\t" \
  "ds_read_b64_tr_b16 v[26:27], v248 offset:1280\n\t" \
  "v_mfma_f32_16x16x32_bf16 v[152:155], v[52:55], v[48:51], v[152:155]\n\t" \
  "ds_read_b64_tr_b16 v[28:29], v246 offset:0\n\t" \
  "ds_read_b64_tr_b16 v[30:31], v246 offset:1280\n\t" \
  "v_mfma_f32_16x16x32_bf16 v[156:159], v[56:59], v[44:47], v[156:159]\n\t" \
  "ds_read_b64_tr_b16 v[32:33], v246 offset:40\n\t" \
  "ds_read_b64_tr_b16 v[34:35], v246 offset:1320\n\t" \
  "v_mfma_f32_16x16x32_bf16 v[160:163], v[56:59], v[48:51], v[160:163]\n\t" \
  "ds_read_b64_tr_b16 v[36:37], v246 offset:80\n\t" \
  "ds_read_b64_tr_b16 v[38:39], v246 offset:1360\n\t" \
  "v_mfma_f32_16x16x32_bf16 v[164:167], v[60:63], v[44:47], v[164:167]\n\t" \
  "ds_read_b64_tr_b16 v[40:41], v247 offset:0\n\t" \
  "ds_read_b64_tr_b16 v[42:43], v247 offset:1280\n\t" \
  "v_mfma_f32_16x16x32_bf16 v[168:171], v[60:63], v[48:51], v[168:171]\n\t" \
  "ds_read_b128 v[188:191], v245 offset:24576\n\t" \
  "ds_read_b128 v[192:195], v245 offset:26624\n\t" \
  "v_mfma_f32_16x16x32_bf16 v[172:175], v[64:67], v[44:47], v[172:175]\n\t" \
  "ds_read_b64_tr_b16 v[196:197], v248 offset:10240\n\t" \
  "ds_read_b64_tr_b16 v[198:199], v248 offset:11520\n\t" \
  "v_mfma_f32_16x16x32_bf16 v[176:179], v[64:67], v[48:51], v[176:179]\n\t" \
  "ds_read_b64_tr_b16 v[200:201], v246 offset:10240\n\t" \
  "ds_read_b64_tr_b16 v[202:203], v246 offset:11520\n\t" \
  "v_mfma_f32_16x16x32_bf16 v[180:183], v[68:71], v[44:47], v[180:183]\n\t" \
  "ds_read_b64_tr_b16 v[204:205], v246 offset:10280\n\t" \
  "ds_read_b64_tr_b16 v[206:207], v246 offset:11560\n\t" \
  "v_mfma_f32_16x16x32_bf16 v[184:187], v[68:71], v[48:51], v[184:187]\n\t" \
  "ds_read_b64_tr_b16 v[208:209], v246 offset:10320\n\t" \
  "ds_read_b64_tr_b16 v[210:211], v246 offset:11600\n\t" \
  "v_mfma_f32_16x16x32_bf16 v[148:151], v[224:227], v[216:219], v[148:151]\n\t" \
  "ds_read_b64_tr_b16 v[212:213], v247 offset:10240\n\t" \
  "ds_read_b64_tr_b16 v[214:215], v247 offset:11520\n\t" \
  "v_mfma_f32_16x16x32_bf16 v[152:155], v[224:227], v[220:223], v[152:155]\n\t" \
  "v_mfma_f32_16x16x32_bf16 v[156:159], v[228:231], v[216:219], v[156:159]\n\t" \
  "v_mfma_f32_16x16x32_bf16 v[160:163], v[228:231], v[220:223], v[160:163]\n\t" \
  "v_mfma_f32_16x16x32_bf16 v[164:167], v[232:235], v[216:219], v[164:167]\n\t" \
  "v_mfma_f32_16x16x32_bf16 v[168:171], v[232:235], v[220:223], v[168:171]\n\t" \
  "v_mfma_f32_16x16x32_bf16 v[172:175], v[236:239], v[216:219], v[172:175]\n\t" \
  "v_mfma_f32_16x16x32_bf16 v[176:179], v[236:239], v[220:223], v[176:179]\n\t" \
  "v_mfma_f32_16x16x32_bf16 v[180:183], v[240:243], v[216:219], v[180:183]\n\t" \
  "v_mfma_f32_16x16x32_bf16 v[184:187], v[240:243], v[220:223], v[184:187]\n\t" \
    \
  "s_waitcnt lgkmcnt(0)\n\t" \
  "s_barrier\n\t" \
  "ds_read_b128 v[44:47], v244 offset:28672\n\t" \
  "ds_read_b128 v[48:51], v244 offset:30720\n\t" \
  "v_mfma_f32_16x16x32_bf16 v[148:151], v[24:27], v[16:19], v[148:151]\n\t" \
  "ds_read_b64_tr_b16 v[52:53], v248 offset:20480\n\t" \
  "ds_read_b64_tr_b16 v[54:55], v248 offset:21760\n\t" \
  "v_mfma_f32_16x16x32_bf16 v[152:155], v[24:27], v[20:23], v[152:155]\n\t" \
  "ds_read_b64_tr_b16 v[56:57], v246 offset:20480\n\t" \
  "ds_read_b64_tr_b16 v[58:59], v246 offset:21760\n\t" \
  "v_mfma_f32_16x16x32_bf16 v[156:159], v[28:31], v[16:19], v[156:159]\n\t" \
  "ds_read_b64_tr_b16 v[60:61], v246 offset:20520\n\t" \
  "ds_read_b64_tr_b16 v[62:63], v246 offset:21800\n\t" \
  "v_mfma_f32_16x16x32_bf16 v[160:163], v[28:31], v[20:23], v[160:163]\n\t" \
  "ds_read_b64_tr_b16 v[64:65], v246 offset:20560\n\t" \
  "ds_read_b64_tr_b16 v[66:67], v246 offset:21840\n\t" \
  "v_mfma_f32_16x16x32_bf16 v[164:167], v[32:35], v[16:19], v[164:167]\n\t" \
  "ds_read_b64_tr_b16 v[68:69], v247 offset:20480\n\t" \
  "ds_read_b64_tr_b16 v[70:71], v247 offset:21760\n\t" \
  "v_mfma_f32_16x16x32_bf16 v[168:171], v[32:35], v[20:23], v[168:171]\n\t" \
  "ds_read_b128 v[216:219], v245 offset:28672\n\t" \
  "ds_read_b128 v[220:223], v245 offset:30720\n\t" \
  "v_mfma_f32_16x16x32_bf16 v[172:175], v[36:39], v[16:19], v[172:175]\n\t" \
  "ds_read_b64_tr_b16 v[224:225], v248 offset:30720\n\t" \
  "ds_read_b64_tr_b16 v[226:227], v248 offset:32000\n\t" \
  "v_mfma_f32_16x16x32_bf16 v[176:179], v[36:39], v[20:23], v[176:179]\n\t" \
  "ds_read_b64_tr_b16 v[228:229], v246 offset:30720\n\t" \
  "ds_read_b64_tr_b16 v[230:231], v246 offset:32000\n\t" \
  "v_mfma_f32_16x16x32_bf16 v[180:183], v[40:43], v[16:19], v[180:183]\n\t" \
  "ds_read_b64_tr_b16 v[232:233], v246 offset:30760\n\t" \
  "ds_read_b64_tr_b16 v[234:235], v246 offset:32040\n\t" \
  "v_mfma_f32_16x16x32_bf16 v[184:187], v[40:43], v[20:23], v[184:187]\n\t" \
  "ds_read_b64_tr_b16 v[236:237], v246 offset:30800\n\t" \
  "ds_read_b64_tr_b16 v[238:239], v246 offset:32080\n\t" \
  "v_mfma_f32_16x16x32_bf16 v[148:151], v[196:199], v[188:191], v[148:151]\n\t" \
  "ds_read_b64_tr_b16 v[240:241], v247 offset:30720\n\t" \
  "ds_read_b64_tr_b16 v[242:243], v247 offset:32000\n\t" \
  "v_mfma_f32_16x16x32_bf16 v[152:155], v[196:199], v[192:195], v[152:155]\n\t" \
  "v_mfma_f32_16x16x32_bf16 v[156:159], v[200:203], v[188:191], v[156:159]\n\t" \
  "v_mfma_f32_16x16x32_bf16 v[160:163], v[200:203], v[192:195], v[160:163]\n\t" \
  "v_mfma_f32_16x16x32_bf16 v[164:167], v[204:207], v[188:191], v[164:167]\n\t" \
  "v_mfma_f32_16x16x32_bf16 v[168:171], v[204:207], v[192:195], v[168:171]\n\t" \
  "v_mfma_f32_16x16x32_bf16 v[172:175], v[208:211], v[188:191], v[172:175]\n\t" \
  "v_mfma_f32_16x16x32_bf16 v[176:179], v[208:211], v[192:195], v[176:179]\n\t" \
  "v_mfma_f32_16x16x32_bf16 v[180:183], v[212:215], v[188:191], v[180:183]\n\t" \
  "v_mfma_f32_16x16x32_bf16 v[184:187], v[212:215], v[192:195], v[184:187]\n\t" \
    \
  "s_waitcnt lgkmcnt(0)\n\t" \
  "s_barrier\n\t" \
  "ds_read_b128 v[16:19], v244 offset:32768\n\t" \
  "ds_read_b128 v[20:23], v244 offset:34816\n\t" \
  "v_mfma_f32_16x16x32_bf16 v[148:151], v[52:55], v[44:47], v[148:151]\n\t" \
  "ds_read_b64_tr_b16 v[24:25], v248 offset:40960\n\t" \
  "ds_read_b64_tr_b16 v[26:27], v248 offset:42240\n\t" \
  "v_mfma_f32_16x16x32_bf16 v[152:155], v[52:55], v[48:51], v[152:155]\n\t" \
  "ds_read_b64_tr_b16 v[28:29], v246 offset:40960\n\t" \
  "ds_read_b64_tr_b16 v[30:31], v246 offset:42240\n\t" \
  "v_mfma_f32_16x16x32_bf16 v[156:159], v[56:59], v[44:47], v[156:159]\n\t" \
  "ds_read_b64_tr_b16 v[32:33], v246 offset:41000\n\t" \
  "ds_read_b64_tr_b16 v[34:35], v246 offset:42280\n\t" \
  "v_mfma_f32_16x16x32_bf16 v[160:163], v[56:59], v[48:51], v[160:163]\n\t" \
  "ds_read_b64_tr_b16 v[36:37], v246 offset:41040\n\t" \
  "ds_read_b64_tr_b16 v[38:39], v246 offset:42320\n\t" \
  "v_mfma_f32_16x16x32_bf16 v[164:167], v[60:63], v[44:47], v[164:167]\n\t" \
  "ds_read_b64_tr_b16 v[40:41], v247 offset:40960\n\t" \
  "ds_read_b64_tr_b16 v[42:43], v247 offset:42240\n\t" \
  "v_mfma_f32_16x16x32_bf16 v[168:171], v[60:63], v[48:51], v[168:171]\n\t" \
  "ds_read_b128 v[188:191], v245 offset:32768\n\t" \
  "ds_read_b128 v[192:195], v245 offset:34816\n\t" \
  "v_mfma_f32_16x16x32_bf16 v[172:175], v[64:67], v[44:47], v[172:175]\n\t" \
  "ds_read_b64_tr_b16 v[196:197], v248 offset:51200\n\t" \
  "ds_read_b64_tr_b16 v[198:199], v248 offset:52480\n\t" \
  "v_mfma_f32_16x16x32_bf16 v[176:179], v[64:67], v[48:51], v[176:179]\n\t" \
  "ds_read_b64_tr_b16 v[200:201], v246 offset:51200\n\t" \
  "ds_read_b64_tr_b16 v[202:203], v246 offset:52480\n\t" \
  "v_mfma_f32_16x16x32_bf16 v[180:183], v[68:71], v[44:47], v[180:183]\n\t" \
  "ds_read_b64_tr_b16 v[204:205], v246 offset:51240\n\t" \
  "ds_read_b64_tr_b16 v[206:207], v246 offset:52520\n\t" \
  "v_mfma_f32_16x16x32_bf16 v[184:187], v[68:71], v[48:51], v[184:187]\n\t" \
  "ds_read_b64_tr_b16 v[208:209], v246 offset:51280\n\t" \
  "ds_read_b64_tr_b16 v[210:211], v246 offset:52560\n\t" \
  "v_mfma_f32_16x16x32_bf16 v[148:151], v[224:227], v[216:219], v[148:151]\n\t" \
  "ds_read_b64_tr_b16 v[212:213], v247 offset:51200\n\t" \
  "ds_read_b64_tr_b16 v[214:215], v247 offset:52480\n\t" \
  "v_mfma_f32_16x16x32_bf16 v[152:155], v[224:227], v[220:223], v[152:155]\n\t" \
  "v_mfma_f32_16x16x32_bf16 v[156:159], v[228:231], v[216:219], v[156:159]\n\t" \
  "v_mfma_f32_16x16x32_bf16 v[160:163], v[228:231], v[220:223], v[160:163]\n\t" \
  "v_mfma_f32_16x16x32_bf16 v[164:167], v[232:235], v[216:219], v[164:167]\n\t" \
  "v_mfma_f32_16x16x32_bf16 v[168:171], v[232:235], v[220:223], v[168:171]\n\t" \
  "v_mfma_f32_16x16x32_bf16 v[172:175], v[236:239], v[216:219], v[172:175]\n\t" \
  "v_mfma_f32_16x16x32_bf16 v[176:179], v[236:239], v[220:223], v[176:179]\n\t" \
  "v_mfma_f32_16x16x32_bf16 v[180:183], v[240:243], v[216:219], v[180:183]\n\t" \
  "v_mfma_f32_16x16x32_bf16 v[184:187], v[240:243], v[220:223], v[184:187]\n\t" \
  "s_waitcnt lgkmcnt(14)\n\t" \
  "v_mfma_f32_16x16x32_bf16 v[148:151], v[24:27], v[16:19], v[148:151]\n\t" \
  "v_mfma_f32_16x16x32_bf16 v[152:155], v[24:27], v[20:23], v[152:155]\n\t" \
  "v_mfma_f32_16x16x32_bf16 v[156:159], v[28:31], v[16:19], v[156:159]\n\t" \
  "v_mfma_f32_16x16x32_bf16 v[160:163], v[28:31], v[20:23], v[160:163]\n\t" \
  "v_mfma_f32_16x16x32_bf16 v[164:167], v[32:35], v[16:19], v[164:167]\n\t" \
  "v_mfma_f32_16x16x32_bf16 v[168:171], v[32:35], v[20:23], v[168:171]\n\t" \
  "v_mfma_f32_16x16x32_bf16 v[172:175], v[36:39], v[16:19], v[172:175]\n\t" \
  "v_mfma_f32_16x16x32_bf16 v[176:179], v[36:39], v[20:23], v[176:179]\n\t" \
  "s_waitcnt lgkmcnt(12)\n\t" \
  "v_mfma_f32_16x16x32_bf16 v[180:183], v[40:43], v[16:19], v[180:183]\n\t" \
  "v_mfma_f32_16x16x32_bf16 v[184:187], v[40:43], v[20:23], v[184:187]\n\t" \
    \
  "s_waitcnt lgkmcnt(0)\n\t" \
  "s_barrier\n\t" \
  "ds_read_b128 v[44:47], v244 offset:36864\n\t" \
  "ds_read_b128 v[48:51], v244 offset:38912\n\t" \
  "v_mfma_f32_16x16x32_bf16 v[148:151], v[196:199], v[188:191], v[148:151]\n\t" \
  "ds_read_b64_tr_b16 v[52:53], v248 offset:0\n\t" \
  "ds_read_b64_tr_b16 v[54:55], v248 offset:1280\n\t" \
  "v_mfma_f32_16x16x32_bf16 v[152:155], v[196:199], v[192:195], v[152:155]\n\t" \
  "ds_read_b64_tr_b16 v[56:57], v246 offset:0\n\t" \
  "ds_read_b64_tr_b16 v[58:59], v246 offset:1280\n\t" \
  "v_mfma_f32_16x16x32_bf16 v[156:159], v[200:203], v[188:191], v[156:159]\n\t" \
  "ds_read_b64_tr_b16 v[60:61], v246 offset:40\n\t" \
  "ds_read_b64_tr_b16 v[62:63], v246 offset:1320\n\t" \
  "v_mfma_f32_16x16x32_bf16 v[160:163], v[200:203], v[192:195], v[160:163]\n\t" \
  "ds_read_b64_tr_b16 v[64:65], v246 offset:80\n\t" \
  "ds_read_b64_tr_b16 v[66:67], v246 offset:1360\n\t" \
  "v_mfma_f32_16x16x32_bf16 v[164:167], v[204:207], v[188:191], v[164:167]\n\t" \
  "ds_read_b64_tr_b16 v[68:69], v247 offset:0\n\t" \
  "ds_read_b64_tr_b16 v[70:71], v247 offset:1280\n\t" \
  "v_mfma_f32_16x16x32_bf16 v[168:171], v[204:207], v[192:195], v[168:171]\n\t" \
  "ds_read_b128 v[216:219], v245 offset:36864\n\t" \
  "ds_read_b128 v[220:223], v245 offset:38912\n\t" \
  "v_mfma_f32_16x16x32_bf16 v[172:175], v[208:211], v[188:191], v[172:175]\n\t" \
  "ds_read_b64_tr_b16 v[224:225], v248 offset:10240\n\t" \
  "ds_read_b64_tr_b16 v[226:227], v248 offset:11520\n\t" \
  "v_mfma_f32_16x16x32_bf16 v[176:179], v[208:211], v[192:195], v[176:179]\n\t" \
  "ds_read_b64_tr_b16 v[228:229], v246 offset:10240\n\t" \
  "ds_read_b64_tr_b16 v[230:231], v246 offset:11520\n\t" \
  "v_mfma_f32_16x16x32_bf16 v[180:183], v[212:215], v[188:191], v[180:183]\n\t" \
  "ds_read_b64_tr_b16 v[232:233], v246 offset:10280\n\t" \
  "ds_read_b64_tr_b16 v[234:235], v246 offset:11560\n\t" \
  "v_mfma_f32_16x16x32_bf16 v[184:187], v[212:215], v[192:195], v[184:187]\n\t" \
  "ds_read_b64_tr_b16 v[236:237], v246 offset:10320\n\t" \
  "ds_read_b64_tr_b16 v[238:239], v246 offset:11600\n\t" \
  "s_waitcnt lgkmcnt(14)\n\t" \
  "v_mfma_f32_16x16x32_bf16 v[148:151], v[52:55], v[44:47], v[148:151]\n\t" \
  "ds_read_b64_tr_b16 v[240:241], v247 offset:10240\n\t" \
  "ds_read_b64_tr_b16 v[242:243], v247 offset:11520\n\t" \
  "v_mfma_f32_16x16x32_bf16 v[152:155], v[52:55], v[48:51], v[152:155]\n\t" \
  "v_mfma_f32_16x16x32_bf16 v[156:159], v[56:59], v[44:47], v[156:159]\n\t" \
  "v_mfma_f32_16x16x32_bf16 v[160:163], v[56:59], v[48:51], v[160:163]\n\t" \
  "v_mfma_f32_16x16x32_bf16 v[164:167], v[60:63], v[44:47], v[164:167]\n\t" \
  "v_mfma_f32_16x16x32_bf16 v[168:171], v[60:63], v[48:51], v[168:171]\n\t" \
  "s_waitcnt lgkmcnt(14)\n\t" \
  "v_mfma_f32_16x16x32_bf16 v[172:175], v[64:67], v[44:47], v[172:175]\n\t" \
  "v_mfma_f32_16x16x32_bf16 v[176:179], v[64:67], v[48:51], v[176:179]\n\t" \
  "s_waitcnt lgkmcnt(12)\n\t" \
  "v_mfma_f32_16x16x32_bf16 v[180:183], v[68:71], v[44:47], v[180:183]\n\t" \
  "v_mfma_f32_16x16x32_bf16 v[184:187], v[68:71], v[48:51], v[184:187]\n\t" \
    \
  "s_waitcnt lgkmcnt(0)\n\t" \
  "v_mfma_f32_16x16x32_bf16 v[148:151], v[224:227], v[216:219], v[148:151]\n\t" \
  "v_mfma_f32_16x16x32_bf16 v[152:155], v[224:227], v[220:223], v[152:155]\n\t" \
  "s_barrier\n\t" \
  "v_mfma_f32_16x16x32_bf16 v[156:159], v[228:231], v[216:219], v[156:159]\n\t" \
  "ds_read2st64_b64 v[84:87], v111 offset0:4 offset1:5\n\t" \
  "v_mfma_f32_16x16x32_bf16 v[160:163], v[228:231], v[220:223], v[160:163]\n\t" \
  "ds_read_b128 v[80:83], v110 offset:0\n\t" \
  "v_mfma_f32_16x16x32_bf16 v[164:167], v[232:235], v[216:219], v[164:167]\n\t" \
  "s_mov_b64 exec, s[34:35]\n\t" \
  "ds_read_b128 v[92:95], v110 offset:3072\n\t" \
  "s_mov_b64 exec, -1\n\t" \
  "v_mfma_f32_16x16x32_bf16 v[168:171], v[232:235], v[220:223], v[168:171]\n\t" \
  "v_cvt_pk_bf16_f32 v108, v148, v149\n\t" \
  "v_cvt_pk_bf16_f32 v109, v150, v151\n\t" \
  "v_mfma_f32_16x16x32_bf16 v[172:175], v[236:239], v[216:219], v[172:175]\n\t" \
  "v_cvt_pk_bf16_f32 v8, v152, v153\n\t" \
  "v_cvt_pk_bf16_f32 v9, v154, v155\n\t" \
  "v_mfma_f32_16x16x32_bf16 v[176:179], v[236:239], v[220:223], v[176:179]\n\t" \
  "s_add_u32 s50, s3, 4\n\t" \
  "s_mul_i32 s50, s50, 10240\n\t" \
  "v_mfma_f32_16x16x32_bf16 v[180:183], v[240:243], v[216:219], v[180:183]\n\t" \
  "v_permlane16_swap_b32 v108, v8\n\t" \
  "v_permlane16_swap_b32 v109, v9\n\t" \
  "v_mfma_f32_16x16x32_bf16 v[184:187], v[240:243], v[220:223], v[184:187]\n\t" \
  "v_add_u32 v6, s50, v2\n\t" \
  "s_waitcnt lgkmcnt(0)\n\t" \
  "v_mfma_f32_32x32x4_2b_bf16 v[16:47], v[84:85], v[108:109], 4.0 cbsz:1 abid:0\n\t" \
  "v_cvt_pk_bf16_f32 v100, v156, v157\n\t" \
  "v_cvt_pk_bf16_f32 v101, v158, v159\n\t" \
  "v_cvt_pk_bf16_f32 v102, v160, v161\n\t" \
  "v_cvt_pk_bf16_f32 v103, v162, v163\n\t" \
  "s_nop 0\n\t" \
  "v_permlane16_swap_b32 v100, v102\n\t" \
  "v_permlane16_swap_b32 v101, v103\n\t" \
  "v_cvt_pk_bf16_f32 v104, v164, v165\n\t" \
  "v_cvt_pk_bf16_f32 v105, v166, v167\n\t" \
  "v_cvt_pk_bf16_f32 v106, v168, v169\n\t" \
  "v_cvt_pk_bf16_f32 v107, v170, v171\n\t" \
  "s_nop 0\n\t" \
  "v_permlane16_swap_b32 v104, v106\n\t" \
  "v_permlane16_swap_b32 v105, v107\n\t" \
  "ds_write_b64 v6, v[8:9] offset:2048\n\t" \
  "v_cvt_pk_bf16_f32 v12, v172, v173\n\t" \
  "v_cvt_pk_bf16_f32 v13, v174, v175\n\t" \
  "v_cvt_pk_bf16_f32 v14, v176, v177\n\t" \
  "v_cvt_pk_bf16_f32 v15, v178, v179\n\t" \
  "v_mfma_f32_32x32x16_bf16 v[16:31], v[80:83], v[100:103], v[16:31]\n\t" \
  "s_nop 0\n\t" \
  "v_permlane16_swap_b32 v12, v14\n\t" \
  "v_permlane16_swap_b32 v13, v15\n\t" \
  "v_mfma_f32_32x32x16_bf16 v[32:47], v[80:83], v[104:107], v[32:47]\n\t" \
  "ds_read_b128 v[80:83], v110 offset:1024\n\t" \
  "ds_write_b128 v6, v[12:15] offset:0\n\t" \
  "v_cvt_pk_bf16_f32 v148, v180, v181\n\t" \
  "v_cvt_pk_bf16_f32 v149, v182, v183\n\t" \
  "v_cvt_pk_bf16_f32 v150, v184, v185\n\t" \
  "v_cvt_pk_bf16_f32 v151, v186, v187\n\t" \
  "s_nop 0\n\t" \
  "v_permlane16_swap_b32 v148, v150\n\t" \
  "v_permlane16_swap_b32 v149, v151\n\t" \
  "ds_write_b128 v6, v[148:151] offset:1024\n\t" \
  "s_setprio 1\n\t" \
    \
  "1:\n\t" \
    \
  "v_exp_f32 v16, v16\n\t" \
  "v_exp_f32 v17, v17\n\t" \
  "v_smfmac_f32_32x32x32_bf16 v[128:143], v[88:91], v[72:79], v144\n\t" \
  "s_mov_b64 exec, s[34:35]\n\t" \
  "ds_read_b128 v[88:91], v110 offset:3712\n\t" \
  "ds_read_b128 v[96:99], v110 offset:6400\n\t" \
  "s_mov_b64 exec, -1\n\t" \
  "v_exp_f32 v18, v18\n\t" \
  "v_exp_f32 v19, v19\n\t" \
  "v_exp_f32 v20, v20\n\t" \
  "v_exp_f32 v21, v21\n\t" \
  "v_mfma_f32_32x32x4_2b_bf16 v[48:79], v[84:85], v[108:109], 4.0 cbsz:1 abid:1\n\t" \
  "v_exp_f32 v22, v22\n\t" \
  "v_exp_f32 v23, v23\n\t" \
  "v_exp_f32 v24, v24\n\t" \
  "v_exp_f32 v25, v25\n\t" \
  "v_exp_f32 v26, v26\n\t" \
  "v_exp_f32 v27, v27\n\t" \
  "v_exp_f32 v28, v28\n\t" \
  "v_exp_f32 v29, v29\n\t" \
  "v_smfmac_f32_32x32x32_bf16 v[112:127], v[92:95], v[16:23], v144\n\t" \
  "s_waitcnt lgkmcnt(0)\n\t" \
  "v_exp_f32 v30, v30\n\t" \
  "v_exp_f32 v31, v31\n\t" \
  "v_exp_f32 v32, v32\n\t" \
  "v_exp_f32 v33, v33\n\t" \
  "v_smfmac_f32_32x32x32_bf16 v[112:127], v[88:91], v[24:31], v144\n\t" \
  "v_exp_f32 v34, v34\n\t" \
  "v_exp_f32 v35, v35\n\t" \
  "v_exp_f32 v36, v36\n\t" \
  "v_exp_f32 v37, v37\n\t" \
  "v_mfma_f32_32x32x16_bf16 v[48:63], v[80:83], v[100:103], v[48:63]\n\t" \
  "v_exp_f32 v38, v38\n\t" \
  "v_exp_f32 v39, v39\n\t" \
  "v_exp_f32 v40, v40\n\t" \
  "v_exp_f32 v41, v41\n\t" \
  "v_mfma_f32_32x32x16_bf16 v[64:79], v[80:83], v[104:107], v[64:79]\n\t" \
  "ds_read_b128 v[80:83], v110 offset:4352\n\t" \
  "v_exp_f32 v42, v42\n\t" \
  "v_exp_f32 v43, v43\n\t" \
  "v_exp_f32 v44, v44\n\t" \
  "v_exp_f32 v45, v45\n\t" \
  "v_smfmac_f32_32x32x32_bf16 v[128:143], v[92:95], v[32:39], v144\n\t" \
  "v_exp_f32 v46, v46\n\t" \
  "v_exp_f32 v47, v47\n\t" \
    \
  "v_exp_f32 v48, v48\n\t" \
  "v_exp_f32 v49, v49\n\t" \
  "v_smfmac_f32_32x32x32_bf16 v[128:143], v[88:91], v[40:47], v144\n\t" \
  "s_mov_b64 exec, s[34:35]\n\t" \
  "ds_read_b128 v[88:91], v110 offset:7040\n\t" \
  "ds_read_b128 v[92:95], v110 offset:7680\n\t" \
  "s_mov_b64 exec, -1\n\t" \
  "v_exp_f32 v50, v50\n\t" \
  "v_exp_f32 v51, v51\n\t" \
  "v_exp_f32 v52, v52\n\t" \
  "v_exp_f32 v53, v53\n\t" \
  "v_mfma_f32_32x32x4_2b_bf16 v[16:47], v[86:87], v[108:109], 4.0 cbsz:1 abid:0\n\t" \
  "v_exp_f32 v54, v54\n\t" \
  "v_exp_f32 v55, v55\n\t" \
  "v_exp_f32 v56, v56\n\t" \
  "v_exp_f32 v57, v57\n\t" \
  "v_exp_f32 v58, v58\n\t" \
  "v_exp_f32 v59, v59\n\t" \
  "v_exp_f32 v60, v60\n\t" \
  "v_exp_f32 v61, v61\n\t" \
  "v_smfmac_f32_32x32x32_bf16 v[112:127], v[96:99], v[48:55], v144\n\t" \
  "s_waitcnt lgkmcnt(0)\n\t" \
  "v_exp_f32 v62, v62\n\t" \
  "v_exp_f32 v63, v63\n\t" \
  "v_exp_f32 v64, v64\n\t" \
  "v_exp_f32 v65, v65\n\t" \
  "v_smfmac_f32_32x32x32_bf16 v[112:127], v[88:91], v[56:63], v144\n\t" \
  "v_exp_f32 v66, v66\n\t" \
  "v_exp_f32 v67, v67\n\t" \
  "v_exp_f32 v68, v68\n\t" \
  "v_exp_f32 v69, v69\n\t" \
  "v_mfma_f32_32x32x16_bf16 v[16:31], v[80:83], v[100:103], v[16:31]\n\t" \
  "v_exp_f32 v70, v70\n\t" \
  "v_exp_f32 v71, v71\n\t" \
  "v_exp_f32 v72, v72\n\t" \
  "v_exp_f32 v73, v73\n\t" \
  "v_mfma_f32_32x32x16_bf16 v[32:47], v[80:83], v[104:107], v[32:47]\n\t" \
  "ds_read_b128 v[80:83], v110 offset:5376\n\t" \
  "v_exp_f32 v74, v74\n\t" \
  "v_exp_f32 v75, v75\n\t" \
  "v_exp_f32 v76, v76\n\t" \
  "v_exp_f32 v77, v77\n\t" \
  "v_smfmac_f32_32x32x32_bf16 v[128:143], v[96:99], v[64:71], v144\n\t" \
  "v_exp_f32 v78, v78\n\t" \
  "v_exp_f32 v79, v79\n\t" \
    \
  "v_exp_f32 v16, v16\n\t" \
  "v_exp_f32 v17, v17\n\t" \
  "v_smfmac_f32_32x32x32_bf16 v[128:143], v[88:91], v[72:79], v144\n\t" \
  "s_mov_b64 exec, s[34:35]\n\t" \
  "ds_read_b128 v[88:91], v110 offset:8320\n\t" \
  "ds_read_b128 v[96:99], v110 offset:8960\n\t" \
  "s_mov_b64 exec, -1\n\t" \
  "v_exp_f32 v18, v18\n\t" \
  "v_exp_f32 v19, v19\n\t" \
  "v_exp_f32 v20, v20\n\t" \
  "v_exp_f32 v21, v21\n\t" \
  "v_mfma_f32_32x32x4_2b_bf16 v[48:79], v[86:87], v[108:109], 4.0 cbsz:1 abid:1\n\t" \
  "ds_read2st64_b64 v[84:87], v111 offset0:24 offset1:25\n\t" \
  "v_exp_f32 v22, v22\n\t" \
  "v_exp_f32 v23, v23\n\t" \
  "v_exp_f32 v24, v24\n\t" \
  "v_exp_f32 v25, v25\n\t" \
  "v_exp_f32 v26, v26\n\t" \
  "v_exp_f32 v27, v27\n\t" \
  "v_exp_f32 v28, v28\n\t" \
  "v_exp_f32 v29, v29\n\t" \
  "v_smfmac_f32_32x32x32_bf16 v[112:127], v[92:95], v[16:23], v144\n\t" \
  "s_waitcnt lgkmcnt(0)\n\t" \
  "v_exp_f32 v30, v30\n\t" \
  "v_exp_f32 v31, v31\n\t" \
  "v_exp_f32 v32, v32\n\t" \
  "v_exp_f32 v33, v33\n\t" \
  "v_smfmac_f32_32x32x32_bf16 v[112:127], v[88:91], v[24:31], v144\n\t" \
  "v_exp_f32 v34, v34\n\t" \
  "v_exp_f32 v35, v35\n\t" \
  "v_exp_f32 v36, v36\n\t" \
  "v_exp_f32 v37, v37\n\t" \
  "v_mfma_f32_32x32x16_bf16 v[48:63], v[80:83], v[100:103], v[48:63]\n\t" \
  "v_exp_f32 v38, v38\n\t" \
  "v_exp_f32 v39, v39\n\t" \
  "v_exp_f32 v40, v40\n\t" \
  "v_exp_f32 v41, v41\n\t" \
  "v_mfma_f32_32x32x16_bf16 v[64:79], v[80:83], v[104:107], v[64:79]\n\t" \
  "ds_read_b128 v[80:83], v110 offset:10240\n\t" \
  "v_exp_f32 v42, v42\n\t" \
  "v_exp_f32 v43, v43\n\t" \
  "v_exp_f32 v44, v44\n\t" \
  "v_exp_f32 v45, v45\n\t" \
  "v_smfmac_f32_32x32x32_bf16 v[128:143], v[92:95], v[32:39], v144\n\t" \
  "v_exp_f32 v46, v46\n\t" \
  "v_exp_f32 v47, v47\n\t" \
    \
  "v_exp_f32 v48, v48\n\t" \
  "v_exp_f32 v49, v49\n\t" \
  "v_smfmac_f32_32x32x32_bf16 v[128:143], v[88:91], v[40:47], v144\n\t" \
  "s_mov_b64 exec, s[34:35]\n\t" \
  "ds_read_b128 v[88:91], v110 offset:9600\n\t" \
  "ds_read_b128 v[92:95], v110 offset:13312\n\t" \
  "s_mov_b64 exec, -1\n\t" \
  "v_exp_f32 v50, v50\n\t" \
  "v_exp_f32 v51, v51\n\t" \
  "v_exp_f32 v52, v52\n\t" \
  "v_exp_f32 v53, v53\n\t" \
  "v_mfma_f32_32x32x4_2b_bf16 v[16:47], v[84:85], v[108:109], 4.0 cbsz:1 abid:0\n\t" \
  "v_exp_f32 v54, v54\n\t" \
  "v_exp_f32 v55, v55\n\t" \
  "v_exp_f32 v56, v56\n\t" \
  "v_exp_f32 v57, v57\n\t" \
  "v_exp_f32 v58, v58\n\t" \
  "v_exp_f32 v59, v59\n\t" \
  "v_exp_f32 v60, v60\n\t" \
  "v_exp_f32 v61, v61\n\t" \
  "v_smfmac_f32_32x32x32_bf16 v[112:127], v[96:99], v[48:55], v144\n\t" \
  "s_waitcnt lgkmcnt(0)\n\t" \
  "v_exp_f32 v62, v62\n\t" \
  "v_exp_f32 v63, v63\n\t" \
  "v_exp_f32 v64, v64\n\t" \
  "v_exp_f32 v65, v65\n\t" \
  "v_smfmac_f32_32x32x32_bf16 v[112:127], v[88:91], v[56:63], v144\n\t" \
  "v_exp_f32 v66, v66\n\t" \
  "v_exp_f32 v67, v67\n\t" \
  "v_exp_f32 v68, v68\n\t" \
  "v_exp_f32 v69, v69\n\t" \
  "v_mfma_f32_32x32x16_bf16 v[16:31], v[80:83], v[100:103], v[16:31]\n\t" \
  "v_exp_f32 v70, v70\n\t" \
  "v_exp_f32 v71, v71\n\t" \
  "v_exp_f32 v72, v72\n\t" \
  "v_exp_f32 v73, v73\n\t" \
  "v_mfma_f32_32x32x16_bf16 v[32:47], v[80:83], v[104:107], v[32:47]\n\t" \
  "ds_read_b128 v[80:83], v110 offset:11264\n\t" \
  "v_exp_f32 v74, v74\n\t" \
  "v_exp_f32 v75, v75\n\t" \
  "v_exp_f32 v76, v76\n\t" \
  "v_exp_f32 v77, v77\n\t" \
  "v_smfmac_f32_32x32x32_bf16 v[128:143], v[96:99], v[64:71], v144\n\t" \
  "v_exp_f32 v78, v78\n\t" \
  "v_exp_f32 v79, v79\n\t" \
  "s_barrier\n\t" \
    \
  "v_exp_f32 v16, v16\n\t" \
  "v_exp_f32 v17, v17\n\t" \
  "v_smfmac_f32_32x32x32_bf16 v[128:143], v[88:91], v[72:79], v144\n\t" \
  "s_mov_b64 exec, s[34:35]\n\t" \
  "ds_read_b128 v[88:91], v110 offset:13952\n\t" \
  "ds_read_b128 v[96:99], v110 offset:16640\n\t" \
  "s_mov_b64 exec, -1\n\t" \
  "v_exp_f32 v18, v18\n\t" \
  "v_exp_f32 v19, v19\n\t" \
  "v_exp_f32 v20, v20\n\t" \
  "v_exp_f32 v21, v21\n\t" \
  "v_mfma_f32_32x32x4_2b_bf16 v[48:79], v[84:85], v[108:109], 4.0 cbsz:1 abid:1\n\t" \
  "v_exp_f32 v22, v22\n\t" \
  "v_exp_f32 v23, v23\n\t" \
  "v_exp_f32 v24, v24\n\t" \
  "v_exp_f32 v25, v25\n\t" \
  "v_exp_f32 v26, v26\n\t" \
  "v_exp_f32 v27, v27\n\t" \
  "v_exp_f32 v28, v28\n\t" \
  "v_exp_f32 v29, v29\n\t" \
  "v_smfmac_f32_32x32x32_bf16 v[112:127], v[92:95], v[16:23], v144\n\t" \
  "s_waitcnt lgkmcnt(0)\n\t" \
  "v_exp_f32 v30, v30\n\t" \
  "v_exp_f32 v31, v31\n\t" \
  "v_exp_f32 v32, v32\n\t" \
  "v_exp_f32 v33, v33\n\t" \
  "v_smfmac_f32_32x32x32_bf16 v[112:127], v[88:91], v[24:31], v144\n\t" \
  "v_exp_f32 v34, v34\n\t" \
  "v_exp_f32 v35, v35\n\t" \
  "v_exp_f32 v36, v36\n\t" \
  "v_exp_f32 v37, v37\n\t" \
  "v_mfma_f32_32x32x16_bf16 v[48:63], v[80:83], v[100:103], v[48:63]\n\t" \
  "v_exp_f32 v38, v38\n\t" \
  "v_exp_f32 v39, v39\n\t" \
  "v_exp_f32 v40, v40\n\t" \
  "v_exp_f32 v41, v41\n\t" \
  "v_mfma_f32_32x32x16_bf16 v[64:79], v[80:83], v[104:107], v[64:79]\n\t" \
  "ds_read_b128 v[80:83], v110 offset:14592\n\t" \
  "v_exp_f32 v42, v42\n\t" \
  "v_exp_f32 v43, v43\n\t" \
  "v_exp_f32 v44, v44\n\t" \
  "v_exp_f32 v45, v45\n\t" \
  "v_smfmac_f32_32x32x32_bf16 v[128:143], v[92:95], v[32:39], v144\n\t" \
  "v_exp_f32 v46, v46\n\t" \
  "v_exp_f32 v47, v47\n\t" \
    \
  "v_exp_f32 v48, v48\n\t" \
  "v_exp_f32 v49, v49\n\t" \
  "v_smfmac_f32_32x32x32_bf16 v[128:143], v[88:91], v[40:47], v144\n\t" \
  "s_mov_b64 exec, s[34:35]\n\t" \
  "ds_read_b128 v[88:91], v110 offset:17280\n\t" \
  "ds_read_b128 v[92:95], v110 offset:17920\n\t" \
  "s_mov_b64 exec, -1\n\t" \
  "v_exp_f32 v50, v50\n\t" \
  "v_exp_f32 v51, v51\n\t" \
  "v_exp_f32 v52, v52\n\t" \
  "v_exp_f32 v53, v53\n\t" \
  "v_mfma_f32_32x32x4_2b_bf16 v[16:47], v[86:87], v[108:109], 4.0 cbsz:1 abid:0\n\t" \
  "v_exp_f32 v54, v54\n\t" \
  "v_exp_f32 v55, v55\n\t" \
  "v_exp_f32 v56, v56\n\t" \
  "v_exp_f32 v57, v57\n\t" \
  "v_exp_f32 v58, v58\n\t" \
  "v_exp_f32 v59, v59\n\t" \
  "v_exp_f32 v60, v60\n\t" \
  "v_exp_f32 v61, v61\n\t" \
  "v_smfmac_f32_32x32x32_bf16 v[112:127], v[96:99], v[48:55], v144\n\t" \
  "s_waitcnt lgkmcnt(0)\n\t" \
  "v_exp_f32 v62, v62\n\t" \
  "v_exp_f32 v63, v63\n\t" \
  "v_exp_f32 v64, v64\n\t" \
  "v_exp_f32 v65, v65\n\t" \
  "v_smfmac_f32_32x32x32_bf16 v[112:127], v[88:91], v[56:63], v144\n\t" \
  "v_exp_f32 v66, v66\n\t" \
  "v_exp_f32 v67, v67\n\t" \
  "v_exp_f32 v68, v68\n\t" \
  "v_exp_f32 v69, v69\n\t" \
  "v_mfma_f32_32x32x16_bf16 v[16:31], v[80:83], v[100:103], v[16:31]\n\t" \
  "v_exp_f32 v70, v70\n\t" \
  "v_exp_f32 v71, v71\n\t" \
  "v_exp_f32 v72, v72\n\t" \
  "v_exp_f32 v73, v73\n\t" \
  "v_mfma_f32_32x32x16_bf16 v[32:47], v[80:83], v[104:107], v[32:47]\n\t" \
  "ds_read_b128 v[80:83], v110 offset:15616\n\t" \
  "v_exp_f32 v74, v74\n\t" \
  "v_exp_f32 v75, v75\n\t" \
  "v_exp_f32 v76, v76\n\t" \
  "v_exp_f32 v77, v77\n\t" \
  "v_smfmac_f32_32x32x32_bf16 v[128:143], v[96:99], v[64:71], v144\n\t" \
  "v_exp_f32 v78, v78\n\t" \
  "v_exp_f32 v79, v79\n\t" \
    \
  "v_exp_f32 v16, v16\n\t" \
  "v_exp_f32 v17, v17\n\t" \
  "v_smfmac_f32_32x32x32_bf16 v[128:143], v[88:91], v[72:79], v144\n\t" \
  "s_mov_b64 exec, s[34:35]\n\t" \
  "ds_read_b128 v[88:91], v110 offset:18560\n\t" \
  "ds_read_b128 v[96:99], v110 offset:19200\n\t" \
  "s_mov_b64 exec, -1\n\t" \
  "v_exp_f32 v18, v18\n\t" \
  "v_exp_f32 v19, v19\n\t" \
  "v_exp_f32 v20, v20\n\t" \
  "v_exp_f32 v21, v21\n\t" \
  "v_mfma_f32_32x32x4_2b_bf16 v[48:79], v[86:87], v[108:109], 4.0 cbsz:1 abid:1\n\t" \
  "ds_read2st64_b64 v[84:87], v111 offset0:44 offset1:45\n\t" \
  "v_exp_f32 v22, v22\n\t" \
  "v_exp_f32 v23, v23\n\t" \
  "v_exp_f32 v24, v24\n\t" \
  "v_exp_f32 v25, v25\n\t" \
  "v_exp_f32 v26, v26\n\t" \
  "v_exp_f32 v27, v27\n\t" \
  "v_exp_f32 v28, v28\n\t" \
  "v_exp_f32 v29, v29\n\t" \
  "v_smfmac_f32_32x32x32_bf16 v[112:127], v[92:95], v[16:23], v144\n\t" \
  "s_waitcnt lgkmcnt(0)\n\t" \
  "v_exp_f32 v30, v30\n\t" \
  "v_exp_f32 v31, v31\n\t" \
  "v_exp_f32 v32, v32\n\t" \
  "v_exp_f32 v33, v33\n\t" \
  "v_smfmac_f32_32x32x32_bf16 v[112:127], v[88:91], v[24:31], v144\n\t" \
  "v_exp_f32 v34, v34\n\t" \
  "v_exp_f32 v35, v35\n\t" \
  "v_exp_f32 v36, v36\n\t" \
  "v_exp_f32 v37, v37\n\t" \
  "v_mfma_f32_32x32x16_bf16 v[48:63], v[80:83], v[100:103], v[48:63]\n\t" \
  "v_exp_f32 v38, v38\n\t" \
  "v_exp_f32 v39, v39\n\t" \
  "v_exp_f32 v40, v40\n\t" \
  "v_exp_f32 v41, v41\n\t" \
  "v_mfma_f32_32x32x16_bf16 v[64:79], v[80:83], v[104:107], v[64:79]\n\t" \
  "ds_read_b128 v[80:83], v110 offset:20480\n\t" \
  "v_exp_f32 v42, v42\n\t" \
  "v_exp_f32 v43, v43\n\t" \
  "v_exp_f32 v44, v44\n\t" \
  "v_exp_f32 v45, v45\n\t" \
  "v_smfmac_f32_32x32x32_bf16 v[128:143], v[92:95], v[32:39], v144\n\t" \
  "v_exp_f32 v46, v46\n\t" \
  "v_exp_f32 v47, v47\n\t" \
    \
  "v_exp_f32 v48, v48\n\t" \
  "v_exp_f32 v49, v49\n\t" \
  "v_smfmac_f32_32x32x32_bf16 v[128:143], v[88:91], v[40:47], v144\n\t" \
  "s_mov_b64 exec, s[34:35]\n\t" \
  "ds_read_b128 v[88:91], v110 offset:19840\n\t" \
  "ds_read_b128 v[92:95], v110 offset:23552\n\t" \
  "s_mov_b64 exec, -1\n\t" \
  "v_exp_f32 v50, v50\n\t" \
  "v_exp_f32 v51, v51\n\t" \
  "v_exp_f32 v52, v52\n\t" \
  "v_exp_f32 v53, v53\n\t" \
  "v_mfma_f32_32x32x4_2b_bf16 v[16:47], v[84:85], v[108:109], 4.0 cbsz:1 abid:0\n\t" \
  "v_exp_f32 v54, v54\n\t" \
  "v_exp_f32 v55, v55\n\t" \
  "v_exp_f32 v56, v56\n\t" \
  "v_exp_f32 v57, v57\n\t" \
  "v_exp_f32 v58, v58\n\t" \
  "v_exp_f32 v59, v59\n\t" \
  "v_exp_f32 v60, v60\n\t" \
  "v_exp_f32 v61, v61\n\t" \
  "v_smfmac_f32_32x32x32_bf16 v[112:127], v[96:99], v[48:55], v144\n\t" \
  "s_waitcnt lgkmcnt(0)\n\t" \
  "v_exp_f32 v62, v62\n\t" \
  "v_exp_f32 v63, v63\n\t" \
  "v_exp_f32 v64, v64\n\t" \
  "v_exp_f32 v65, v65\n\t" \
  "v_smfmac_f32_32x32x32_bf16 v[112:127], v[88:91], v[56:63], v144\n\t" \
  "v_exp_f32 v66, v66\n\t" \
  "v_exp_f32 v67, v67\n\t" \
  "v_exp_f32 v68, v68\n\t" \
  "v_exp_f32 v69, v69\n\t" \
  "v_mfma_f32_32x32x16_bf16 v[16:31], v[80:83], v[100:103], v[16:31]\n\t" \
  "v_exp_f32 v70, v70\n\t" \
  "v_exp_f32 v71, v71\n\t" \
  "v_exp_f32 v72, v72\n\t" \
  "v_exp_f32 v73, v73\n\t" \
  "v_mfma_f32_32x32x16_bf16 v[32:47], v[80:83], v[104:107], v[32:47]\n\t" \
  "ds_read_b128 v[80:83], v110 offset:21504\n\t" \
  "v_exp_f32 v74, v74\n\t" \
  "v_exp_f32 v75, v75\n\t" \
  "v_exp_f32 v76, v76\n\t" \
  "v_exp_f32 v77, v77\n\t" \
  "v_smfmac_f32_32x32x32_bf16 v[128:143], v[96:99], v[64:71], v144\n\t" \
  "buffer_load_dword v145, v145, s[28:31], 0 offen sc1\n\t" \
  "v_exp_f32 v78, v78\n\t" \
  "v_exp_f32 v79, v79\n\t" \
  "2:\n\t" \
    \
  "v_exp_f32 v16, v16\n\t" \
  "v_exp_f32 v17, v17\n\t" \
  "v_smfmac_f32_32x32x32_bf16 v[128:143], v[88:91], v[72:79], v144\n\t" \
  "s_mov_b64 exec, s[34:35]\n\t" \
  "ds_read_b128 v[88:91], v110 offset:24192\n\t" \
  "ds_read_b128 v[96:99], v110 offset:26880\n\t" \
  "s_mov_b64 exec, -1\n\t" \
  "v_exp_f32 v18, v18\n\t" \
  "v_exp_f32 v19, v19\n\t" \
  "v_exp_f32 v20, v20\n\t" \
  "v_exp_f32 v21, v21\n\t" \
  "v_mfma_f32_32x32x4_2b_bf16 v[48:79], v[84:85], v[108:109], 4.0 cbsz:1 abid:1\n\t" \
  "v_exp_f32 v22, v22\n\t" \
  "v_exp_f32 v23, v23\n\t" \
  "v_exp_f32 v24, v24\n\t" \
  "v_exp_f32 v25, v25\n\t" \
  "v_exp_f32 v26, v26\n\t" \
  "v_exp_f32 v27, v27\n\t" \
  "v_exp_f32 v28, v28\n\t" \
  "v_exp_f32 v29, v29\n\t" \
  "v_smfmac_f32_32x32x32_bf16 v[112:127], v[92:95], v[16:23], v144\n\t" \
  "s_waitcnt lgkmcnt(0)\n\t" \
  "v_exp_f32 v30, v30\n\t" \
  "v_exp_f32 v31, v31\n\t" \
  "v_exp_f32 v32, v32\n\t" \
  "v_exp_f32 v33, v33\n\t" \
  "v_smfmac_f32_32x32x32_bf16 v[112:127], v[88:91], v[24:31], v144\n\t" \
  "v_exp_f32 v34, v34\n\t" \
  "v_exp_f32 v35, v35\n\t" \
  "v_exp_f32 v36, v36\n\t" \
  "v_exp_f32 v37, v37\n\t" \
  "v_mfma_f32_32x32x16_bf16 v[48:63], v[80:83], v[100:103], v[48:63]\n\t" \
  "v_exp_f32 v38, v38\n\t" \
  "v_exp_f32 v39, v39\n\t" \
  "v_exp_f32 v40, v40\n\t" \
  "v_exp_f32 v41, v41\n\t" \
  "v_mfma_f32_32x32x16_bf16 v[64:79], v[80:83], v[104:107], v[64:79]\n\t" \
  "ds_read_b128 v[80:83], v110 offset:24832\n\t" \
  "v_exp_f32 v42, v42\n\t" \
  "v_exp_f32 v43, v43\n\t" \
  "v_exp_f32 v44, v44\n\t" \
  "v_exp_f32 v45, v45\n\t" \
  "v_smfmac_f32_32x32x32_bf16 v[128:143], v[92:95], v[32:39], v144\n\t" \
  "v_exp_f32 v46, v46\n\t" \
  "v_exp_f32 v47, v47\n\t" \
    \
  "v_exp_f32 v48, v48\n\t" \
  "v_exp_f32 v49, v49\n\t" \
  "v_smfmac_f32_32x32x32_bf16 v[128:143], v[88:91], v[40:47], v144\n\t" \
  "s_mov_b64 exec, s[34:35]\n\t" \
  "ds_read_b128 v[88:91], v110 offset:27520\n\t" \
  "ds_read_b128 v[92:95], v110 offset:28160\n\t" \
  "s_mov_b64 exec, -1\n\t" \
  "v_exp_f32 v50, v50\n\t" \
  "v_exp_f32 v51, v51\n\t" \
  "v_exp_f32 v52, v52\n\t" \
  "v_exp_f32 v53, v53\n\t" \
  "v_mfma_f32_32x32x4_2b_bf16 v[16:47], v[86:87], v[108:109], 4.0 cbsz:1 abid:0\n\t" \
  "v_exp_f32 v54, v54\n\t" \
  "v_exp_f32 v55, v55\n\t" \
  "v_exp_f32 v56, v56\n\t" \
  "v_exp_f32 v57, v57\n\t" \
  "v_exp_f32 v58, v58\n\t" \
  "v_exp_f32 v59, v59\n\t" \
  "v_exp_f32 v60, v60\n\t" \
  "v_exp_f32 v61, v61\n\t" \
  "v_smfmac_f32_32x32x32_bf16 v[112:127], v[96:99], v[48:55], v144\n\t" \
  "s_waitcnt lgkmcnt(0)\n\t" \
  "v_exp_f32 v62, v62\n\t" \
  "v_exp_f32 v63, v63\n\t" \
  "v_exp_f32 v64, v64\n\t" \
  "v_exp_f32 v65, v65\n\t" \
  "v_smfmac_f32_32x32x32_bf16 v[112:127], v[88:91], v[56:63], v144\n\t" \
  "v_exp_f32 v66, v66\n\t" \
  "v_exp_f32 v67, v67\n\t" \
  "v_exp_f32 v68, v68\n\t" \
  "v_exp_f32 v69, v69\n\t" \
  "v_mfma_f32_32x32x16_bf16 v[16:31], v[80:83], v[100:103], v[16:31]\n\t" \
  "v_exp_f32 v70, v70\n\t" \
  "v_exp_f32 v71, v71\n\t" \
  "v_exp_f32 v72, v72\n\t" \
  "v_exp_f32 v73, v73\n\t" \
  "v_mfma_f32_32x32x16_bf16 v[32:47], v[80:83], v[104:107], v[32:47]\n\t" \
  "ds_read_b128 v[80:83], v110 offset:25856\n\t" \
  "v_exp_f32 v74, v74\n\t" \
  "v_exp_f32 v75, v75\n\t" \
  "v_exp_f32 v76, v76\n\t" \
  "v_exp_f32 v77, v77\n\t" \
  "v_smfmac_f32_32x32x32_bf16 v[128:143], v[96:99], v[64:71], v144\n\t" \
  "v_exp_f32 v78, v78\n\t" \
  "v_exp_f32 v79, v79\n\t" \
    \
  "v_exp_f32 v16, v16\n\t" \
  "v_exp_f32 v17, v17\n\t" \
  "v_smfmac_f32_32x32x32_bf16 v[128:143], v[88:91], v[72:79], v144\n\t" \
  "s_mov_b64 exec, s[34:35]\n\t" \
  "ds_read_b128 v[88:91], v110 offset:28800\n\t" \
  "ds_read_b128 v[96:99], v110 offset:29440\n\t" \
  "s_mov_b64 exec, -1\n\t" \
  "v_exp_f32 v18, v18\n\t" \
  "v_exp_f32 v19, v19\n\t" \
  "v_exp_f32 v20, v20\n\t" \
  "v_exp_f32 v21, v21\n\t" \
  "v_mfma_f32_32x32x4_2b_bf16 v[48:79], v[86:87], v[108:109], 4.0 cbsz:1 abid:1\n\t" \
  "ds_read2st64_b64 v[84:87], v111 offset0:64 offset1:65\n\t" \
  "v_exp_f32 v22, v22\n\t" \
  "v_exp_f32 v23, v23\n\t" \
  "v_exp_f32 v24, v24\n\t" \
  "v_exp_f32 v25, v25\n\t" \
  "v_exp_f32 v26, v26\n\t" \
  "v_exp_f32 v27, v27\n\t" \
  "v_exp_f32 v28, v28\n\t" \
  "v_exp_f32 v29, v29\n\t" \
  "v_smfmac_f32_32x32x32_bf16 v[112:127], v[92:95], v[16:23], v144\n\t" \
  "s_waitcnt lgkmcnt(0)\n\t" \
  "v_exp_f32 v30, v30\n\t" \
  "v_exp_f32 v31, v31\n\t" \
  "v_exp_f32 v32, v32\n\t" \
  "v_exp_f32 v33, v33\n\t" \
  "v_smfmac_f32_32x32x32_bf16 v[112:127], v[88:91], v[24:31], v144\n\t" \
  "v_exp_f32 v34, v34\n\t" \
  "v_exp_f32 v35, v35\n\t" \
  "v_exp_f32 v36, v36\n\t" \
  "v_exp_f32 v37, v37\n\t" \
  "v_mfma_f32_32x32x16_bf16 v[48:63], v[80:83], v[100:103], v[48:63]\n\t" \
  "v_exp_f32 v38, v38\n\t" \
  "v_exp_f32 v39, v39\n\t" \
  "v_exp_f32 v40, v40\n\t" \
  "v_exp_f32 v41, v41\n\t" \
  "v_mfma_f32_32x32x16_bf16 v[64:79], v[80:83], v[104:107], v[64:79]\n\t" \
  "ds_read_b128 v[80:83], v110 offset:30720\n\t" \
  "v_exp_f32 v42, v42\n\t" \
  "v_exp_f32 v43, v43\n\t" \
  "v_exp_f32 v44, v44\n\t" \
  "v_exp_f32 v45, v45\n\t" \
  "v_smfmac_f32_32x32x32_bf16 v[128:143], v[92:95], v[32:39], v144\n\t" \
  "v_exp_f32 v46, v46\n\t" \
  "v_exp_f32 v47, v47\n\t" \
    \
  "v_exp_f32 v48, v48\n\t" \
  "v_exp_f32 v49, v49\n\t" \
  "v_smfmac_f32_32x32x32_bf16 v[128:143], v[88:91], v[40:47], v144\n\t" \
  "s_mov_b64 exec, s[34:35]\n\t" \
  "ds_read_b128 v[88:91], v110 offset:30080\n\t" \
  "ds_read_b128 v[92:95], v110 offset:33792\n\t" \
  "s_mov_b64 exec, -1\n\t" \
  "v_exp_f32 v50, v50\n\t" \
  "v_exp_f32 v51, v51\n\t" \
  "v_exp_f32 v52, v52\n\t" \
  "v_exp_f32 v53, v53\n\t" \
  "v_mfma_f32_32x32x4_2b_bf16 v[16:47], v[84:85], v[108:109], 4.0 cbsz:1 abid:0\n\t" \
  "v_exp_f32 v54, v54\n\t" \
  "v_exp_f32 v55, v55\n\t" \
  "v_exp_f32 v56, v56\n\t" \
  "v_exp_f32 v57, v57\n\t" \
  "v_exp_f32 v58, v58\n\t" \
  "v_exp_f32 v59, v59\n\t" \
  "v_exp_f32 v60, v60\n\t" \
  "v_exp_f32 v61, v61\n\t" \
  "v_smfmac_f32_32x32x32_bf16 v[112:127], v[96:99], v[48:55], v144\n\t" \
  "s_waitcnt lgkmcnt(0)\n\t" \
  "v_exp_f32 v62, v62\n\t" \
  "v_exp_f32 v63, v63\n\t" \
  "v_exp_f32 v64, v64\n\t" \
  "v_exp_f32 v65, v65\n\t" \
  "v_smfmac_f32_32x32x32_bf16 v[112:127], v[88:91], v[56:63], v144\n\t" \
  "v_exp_f32 v66, v66\n\t" \
  "v_exp_f32 v67, v67\n\t" \
  "v_exp_f32 v68, v68\n\t" \
  "v_exp_f32 v69, v69\n\t" \
  "v_mfma_f32_32x32x16_bf16 v[16:31], v[80:83], v[100:103], v[16:31]\n\t" \
  "v_exp_f32 v70, v70\n\t" \
  "v_exp_f32 v71, v71\n\t" \
  "v_exp_f32 v72, v72\n\t" \
  "v_exp_f32 v73, v73\n\t" \
  "v_mfma_f32_32x32x16_bf16 v[32:47], v[80:83], v[104:107], v[32:47]\n\t" \
  "ds_read_b128 v[80:83], v110 offset:31744\n\t" \
  "v_exp_f32 v74, v74\n\t" \
  "v_exp_f32 v75, v75\n\t" \
  "v_exp_f32 v76, v76\n\t" \
  "v_exp_f32 v77, v77\n\t" \
  "v_smfmac_f32_32x32x32_bf16 v[128:143], v[96:99], v[64:71], v144\n\t" \
  "s_cmp_eq_u32 s41, 0\n\t" \
  "v_exp_f32 v78, v78\n\t" \
  "v_exp_f32 v79, v79\n\t" \
  "s_cbranch_scc0 202f\n\t" \
  "s_cbranch_scc1 302f\n\t" \
  "402:\n\t" \
    \
  "v_exp_f32 v16, v16\n\t" \
  "v_exp_f32 v17, v17\n\t" \
  "v_smfmac_f32_32x32x32_bf16 v[128:143], v[88:91], v[72:79], v144\n\t" \
  "s_mov_b64 exec, s[34:35]\n\t" \
  "ds_read_b128 v[88:91], v110 offset:34432\n\t" \
  "ds_read_b128 v[96:99], v110 offset:37120\n\t" \
  "s_mov_b64 exec, -1\n\t" \
  "v_exp_f32 v18, v18\n\t" \
  "v_exp_f32 v19, v19\n\t" \
  "v_exp_f32 v20, v20\n\t" \
  "v_exp_f32 v21, v21\n\t" \
  "v_mfma_f32_32x32x4_2b_bf16 v[48:79], v[84:85], v[108:109], 4.0 cbsz:1 abid:1\n\t" \
  "v_exp_f32 v22, v22\n\t" \
  "v_exp_f32 v23, v23\n\t" \
  "v_exp_f32 v24, v24\n\t" \
  "v_exp_f32 v25, v25\n\t" \
  "v_exp_f32 v26, v26\n\t" \
  "v_exp_f32 v27, v27\n\t" \
  "v_exp_f32 v28, v28\n\t" \
  "v_exp_f32 v29, v29\n\t" \
  "v_smfmac_f32_32x32x32_bf16 v[112:127], v[92:95], v[16:23], v144\n\t" \
  "s_waitcnt lgkmcnt(0)\n\t" \
  "v_exp_f32 v30, v30\n\t" \
  "v_exp_f32 v31, v31\n\t" \
  "v_exp_f32 v32, v32\n\t" \
  "v_exp_f32 v33, v33\n\t" \
  "v_smfmac_f32_32x32x32_bf16 v[112:127], v[88:91], v[24:31], v144\n\t" \
  "v_exp_f32 v34, v34\n\t" \
  "v_exp_f32 v35, v35\n\t" \
  "v_exp_f32 v36, v36\n\t" \
  "v_exp_f32 v37, v37\n\t" \
  "v_mfma_f32_32x32x16_bf16 v[48:63], v[80:83], v[100:103], v[48:63]\n\t" \
  "v_exp_f32 v38, v38\n\t" \
  "v_exp_f32 v39, v39\n\t" \
  "v_exp_f32 v40, v40\n\t" \
  "v_exp_f32 v41, v41\n\t" \
  "v_mfma_f32_32x32x16_bf16 v[64:79], v[80:83], v[104:107], v[64:79]\n\t" \
  "ds_read_b128 v[80:83], v110 offset:35072\n\t" \
  "v_exp_f32 v42, v42\n\t" \
  "v_exp_f32 v43, v43\n\t" \
  "v_exp_f32 v44, v44\n\t" \
  "v_exp_f32 v45, v45\n\t" \
  "v_smfmac_f32_32x32x32_bf16 v[128:143], v[92:95], v[32:39], v144\n\t" \
  "v_exp_f32 v46, v46\n\t" \
  "v_exp_f32 v47, v47\n\t" \
    \
  "v_exp_f32 v48, v48\n\t" \
  "v_exp_f32 v49, v49\n\t" \
  "v_smfmac_f32_32x32x32_bf16 v[128:143], v[88:91], v[40:47], v144\n\t" \
  "s_mov_b64 exec, s[34:35]\n\t" \
  "ds_read_b128 v[88:91], v110 offset:37760\n\t" \
  "ds_read_b128 v[92:95], v110 offset:38400\n\t" \
  "s_mov_b64 exec, -1\n\t" \
  "v_exp_f32 v50, v50\n\t" \
  "v_exp_f32 v51, v51\n\t" \
  "v_exp_f32 v52, v52\n\t" \
  "v_exp_f32 v53, v53\n\t" \
  "v_mfma_f32_32x32x4_2b_bf16 v[16:47], v[86:87], v[108:109], 4.0 cbsz:1 abid:0\n\t" \
  "v_exp_f32 v54, v54\n\t" \
  "v_exp_f32 v55, v55\n\t" \
  "v_exp_f32 v56, v56\n\t" \
  "v_exp_f32 v57, v57\n\t" \
  "v_exp_f32 v58, v58\n\t" \
  "v_exp_f32 v59, v59\n\t" \
  "v_exp_f32 v60, v60\n\t" \
  "v_exp_f32 v61, v61\n\t" \
  "v_smfmac_f32_32x32x32_bf16 v[112:127], v[96:99], v[48:55], v144\n\t" \
  "s_waitcnt lgkmcnt(0)\n\t" \
  "v_exp_f32 v62, v62\n\t" \
  "v_exp_f32 v63, v63\n\t" \
  "v_exp_f32 v64, v64\n\t" \
  "v_exp_f32 v65, v65\n\t" \
  "v_smfmac_f32_32x32x32_bf16 v[112:127], v[88:91], v[56:63], v144\n\t" \
  "v_exp_f32 v66, v66\n\t" \
  "v_exp_f32 v67, v67\n\t" \
  "v_exp_f32 v68, v68\n\t" \
  "v_exp_f32 v69, v69\n\t" \
  "v_mfma_f32_32x32x16_bf16 v[16:31], v[80:83], v[100:103], v[16:31]\n\t" \
  "v_exp_f32 v70, v70\n\t" \
  "v_exp_f32 v71, v71\n\t" \
  "v_exp_f32 v72, v72\n\t" \
  "v_exp_f32 v73, v73\n\t" \
  "v_mfma_f32_32x32x16_bf16 v[32:47], v[80:83], v[104:107], v[32:47]\n\t" \
  "ds_read_b128 v[80:83], v110 offset:36096\n\t" \
  "v_exp_f32 v74, v74\n\t" \
  "v_exp_f32 v75, v75\n\t" \
  "v_exp_f32 v76, v76\n\t" \
  "v_exp_f32 v77, v77\n\t" \
  "v_smfmac_f32_32x32x32_bf16 v[128:143], v[96:99], v[64:71], v144\n\t" \
  "v_exp_f32 v78, v78\n\t" \
  "v_exp_f32 v79, v79\n\t" \
    \
  "v_exp_f32 v16, v16\n\t" \
  "v_exp_f32 v17, v17\n\t" \
  "v_smfmac_f32_32x32x32_bf16 v[128:143], v[88:91], v[72:79], v144\n\t" \
  "s_mov_b64 exec, s[34:35]\n\t" \
  "ds_read_b128 v[88:91], v110 offset:39040\n\t" \
  "ds_read_b128 v[96:99], v110 offset:39680\n\t" \
  "s_mov_b64 exec, -1\n\t" \
  "v_exp_f32 v18, v18\n\t" \
  "v_exp_f32 v19, v19\n\t" \
  "v_exp_f32 v20, v20\n\t" \
  "v_exp_f32 v21, v21\n\t" \
  "v_mfma_f32_32x32x4_2b_bf16 v[48:79], v[86:87], v[108:109], 4.0 cbsz:1 abid:1\n\t" \
  "ds_read2st64_b64 v[84:87], v111 offset0:84 offset1:85\n\t" \
  "v_exp_f32 v22, v22\n\t" \
  "v_exp_f32 v23, v23\n\t" \
  "v_exp_f32 v24, v24\n\t" \
  "v_exp_f32 v25, v25\n\t" \
  "v_exp_f32 v26, v26\n\t" \
  "v_exp_f32 v27, v27\n\t" \
  "v_exp_f32 v28, v28\n\t" \
  "v_exp_f32 v29, v29\n\t" \
  "v_smfmac_f32_32x32x32_bf16 v[112:127], v[92:95], v[16:23], v144\n\t" \
  "s_waitcnt lgkmcnt(0)\n\t" \
  "v_exp_f32 v30, v30\n\t" \
  "v_exp_f32 v31, v31\n\t" \
  "v_exp_f32 v32, v32\n\t" \
  "v_exp_f32 v33, v33\n\t" \
  "v_smfmac_f32_32x32x32_bf16 v[112:127], v[88:91], v[24:31], v144\n\t" \
  "v_exp_f32 v34, v34\n\t" \
  "v_exp_f32 v35, v35\n\t" \
  "v_exp_f32 v36, v36\n\t" \
  "v_exp_f32 v37, v37\n\t" \
  "v_mfma_f32_32x32x16_bf16 v[48:63], v[80:83], v[100:103], v[48:63]\n\t" \
  "v_exp_f32 v38, v38\n\t" \
  "v_exp_f32 v39, v39\n\t" \
  "v_exp_f32 v40, v40\n\t" \
  "v_exp_f32 v41, v41\n\t" \
  "v_mfma_f32_32x32x16_bf16 v[64:79], v[80:83], v[104:107], v[64:79]\n\t" \
  "ds_read_b128 v[80:83], v110 offset:40960\n\t" \
  "v_exp_f32 v42, v42\n\t" \
  "v_exp_f32 v43, v43\n\t" \
  "v_exp_f32 v44, v44\n\t" \
  "v_exp_f32 v45, v45\n\t" \
  "v_smfmac_f32_32x32x32_bf16 v[128:143], v[92:95], v[32:39], v144\n\t" \
  "v_exp_f32 v46, v46\n\t" \
  "v_exp_f32 v47, v47\n\t" \
    \
  "v_exp_f32 v48, v48\n\t" \
  "v_exp_f32 v49, v49\n\t" \
  "v_smfmac_f32_32x32x32_bf16 v[128:143], v[88:91], v[40:47], v144\n\t" \
  "s_mov_b64 exec, s[34:35]\n\t" \
  "ds_read_b128 v[88:91], v110 offset:40320\n\t" \
  "ds_read_b128 v[92:95], v110 offset:44032\n\t" \
  "s_mov_b64 exec, -1\n\t" \
  "v_exp_f32 v50, v50\n\t" \
  "v_exp_f32 v51, v51\n\t" \
  "v_exp_f32 v52, v52\n\t" \
  "v_exp_f32 v53, v53\n\t" \
  "v_mfma_f32_32x32x4_2b_bf16 v[16:47], v[84:85], v[108:109], 4.0 cbsz:1 abid:0\n\t" \
  "v_exp_f32 v54, v54\n\t" \
  "v_exp_f32 v55, v55\n\t" \
  "v_exp_f32 v56, v56\n\t" \
  "v_exp_f32 v57, v57\n\t" \
  "v_exp_f32 v58, v58\n\t" \
  "v_exp_f32 v59, v59\n\t" \
  "v_exp_f32 v60, v60\n\t" \
  "v_exp_f32 v61, v61\n\t" \
  "v_smfmac_f32_32x32x32_bf16 v[112:127], v[96:99], v[48:55], v144\n\t" \
  "s_waitcnt lgkmcnt(0)\n\t" \
  "v_exp_f32 v62, v62\n\t" \
  "v_exp_f32 v63, v63\n\t" \
  "v_exp_f32 v64, v64\n\t" \
  "v_exp_f32 v65, v65\n\t" \
  "v_smfmac_f32_32x32x32_bf16 v[112:127], v[88:91], v[56:63], v144\n\t" \
  "v_exp_f32 v66, v66\n\t" \
  "v_exp_f32 v67, v67\n\t" \
  "v_exp_f32 v68, v68\n\t" \
  "v_exp_f32 v69, v69\n\t" \
  "v_mfma_f32_32x32x16_bf16 v[16:31], v[80:83], v[100:103], v[16:31]\n\t" \
  "v_exp_f32 v70, v70\n\t" \
  "v_exp_f32 v71, v71\n\t" \
  "v_exp_f32 v72, v72\n\t" \
  "v_exp_f32 v73, v73\n\t" \
  "v_mfma_f32_32x32x16_bf16 v[32:47], v[80:83], v[104:107], v[32:47]\n\t" \
  "ds_read_b128 v[80:83], v110 offset:41984\n\t" \
  "v_exp_f32 v74, v74\n\t" \
  "v_exp_f32 v75, v75\n\t" \
  "v_exp_f32 v76, v76\n\t" \
  "v_exp_f32 v77, v77\n\t" \
  "v_smfmac_f32_32x32x32_bf16 v[128:143], v[96:99], v[64:71], v144\n\t" \
  "s_cmp_eq_u32 s41, 0\n\t" \
  "v_exp_f32 v78, v78\n\t" \
  "v_exp_f32 v79, v79\n\t" \
  "s_cbranch_scc0 203f\n\t" \
  "403:\n\t" \
    \
  "v_exp_f32 v16, v16\n\t" \
  "v_exp_f32 v17, v17\n\t" \
  "v_smfmac_f32_32x32x32_bf16 v[128:143], v[88:91], v[72:79], v144\n\t" \
  "s_mov_b64 exec, s[34:35]\n\t" \
  "ds_read_b128 v[88:91], v110 offset:44672\n\t" \
  "ds_read_b128 v[96:99], v110 offset:47360\n\t" \
  "s_mov_b64 exec, -1\n\t" \
  "v_exp_f32 v18, v18\n\t" \
  "v_exp_f32 v19, v19\n\t" \
  "v_exp_f32 v20, v20\n\t" \
  "v_exp_f32 v21, v21\n\t" \
  "v_mfma_f32_32x32x4_2b_bf16 v[48:79], v[84:85], v[108:109], 4.0 cbsz:1 abid:1\n\t" \
  "v_exp_f32 v22, v22\n\t" \
  "v_exp_f32 v23, v23\n\t" \
  "v_exp_f32 v24, v24\n\t" \
  "v_exp_f32 v25, v25\n\t" \
  "v_exp_f32 v26, v26\n\t" \
  "v_exp_f32 v27, v27\n\t" \
  "v_exp_f32 v28, v28\n\t" \
  "v_exp_f32 v29, v29\n\t" \
  "v_smfmac_f32_32x32x32_bf16 v[112:127], v[92:95], v[16:23], v144\n\t" \
  "s_waitcnt lgkmcnt(0)\n\t" \
  "v_exp_f32 v30, v30\n\t" \
  "v_exp_f32 v31, v31\n\t" \
  "v_exp_f32 v32, v32\n\t" \
  "v_exp_f32 v33, v33\n\t" \
  "v_smfmac_f32_32x32x32_bf16 v[112:127], v[88:91], v[24:31], v144\n\t" \
  "v_exp_f32 v34, v34\n\t" \
  "v_exp_f32 v35, v35\n\t" \
  "v_exp_f32 v36, v36\n\t" \
  "v_exp_f32 v37, v37\n\t" \
  "v_mfma_f32_32x32x16_bf16 v[48:63], v[80:83], v[100:103], v[48:63]\n\t" \
  "v_exp_f32 v38, v38\n\t" \
  "v_exp_f32 v39, v39\n\t" \
  "v_exp_f32 v40, v40\n\t" \
  "v_exp_f32 v41, v41\n\t" \
  "v_mfma_f32_32x32x16_bf16 v[64:79], v[80:83], v[104:107], v[64:79]\n\t" \
  "ds_read_b128 v[80:83], v110 offset:45312\n\t" \
  "v_exp_f32 v42, v42\n\t" \
  "v_exp_f32 v43, v43\n\t" \
  "v_exp_f32 v44, v44\n\t" \
  "v_exp_f32 v45, v45\n\t" \
  "v_smfmac_f32_32x32x32_bf16 v[128:143], v[92:95], v[32:39], v144\n\t" \
  "v_exp_f32 v46, v46\n\t" \
  "v_exp_f32 v47, v47\n\t" \
    \
  "v_exp_f32 v48, v48\n\t" \
  "v_exp_f32 v49, v49\n\t" \
  "v_smfmac_f32_32x32x32_bf16 v[128:143], v[88:91], v[40:47], v144\n\t" \
  "s_mov_b64 exec, s[34:35]\n\t" \
  "ds_read_b128 v[88:91], v110 offset:48000\n\t" \
  "ds_read_b128 v[92:95], v110 offset:48640\n\t" \
  "s_mov_b64 exec, -1\n\t" \
  "v_exp_f32 v50, v50\n\t" \
  "v_exp_f32 v51, v51\n\t" \
  "v_exp_f32 v52, v52\n\t" \
  "v_exp_f32 v53, v53\n\t" \
  "v_mfma_f32_32x32x4_2b_bf16 v[16:47], v[86:87], v[108:109], 4.0 cbsz:1 abid:0\n\t" \
  "v_exp_f32 v54, v54\n\t" \
  "v_exp_f32 v55, v55\n\t" \
  "v_exp_f32 v56, v56\n\t" \
  "v_exp_f32 v57, v57\n\t" \
  "v_exp_f32 v58, v58\n\t" \
  "v_exp_f32 v59, v59\n\t" \
  "v_exp_f32 v60, v60\n\t" \
  "v_exp_f32 v61, v61\n\t" \
  "v_smfmac_f32_32x32x32_bf16 v[112:127], v[96:99], v[48:55], v144\n\t" \
  "s_waitcnt lgkmcnt(0)\n\t" \
  "v_exp_f32 v62, v62\n\t" \
  "v_exp_f32 v63, v63\n\t" \
  "v_exp_f32 v64, v64\n\t" \
  "v_exp_f32 v65, v65\n\t" \
  "v_smfmac_f32_32x32x32_bf16 v[112:127], v[88:91], v[56:63], v144\n\t" \
  "v_exp_f32 v66, v66\n\t" \
  "v_exp_f32 v67, v67\n\t" \
  "v_exp_f32 v68, v68\n\t" \
  "v_exp_f32 v69, v69\n\t" \
  "v_mfma_f32_32x32x16_bf16 v[16:31], v[80:83], v[100:103], v[16:31]\n\t" \
  "v_exp_f32 v70, v70\n\t" \
  "v_exp_f32 v71, v71\n\t" \
  "v_exp_f32 v72, v72\n\t" \
  "v_exp_f32 v73, v73\n\t" \
  "v_mfma_f32_32x32x16_bf16 v[32:47], v[80:83], v[104:107], v[32:47]\n\t" \
  "ds_read_b128 v[80:83], v110 offset:46336\n\t" \
  "v_exp_f32 v74, v74\n\t" \
  "v_exp_f32 v75, v75\n\t" \
  "v_exp_f32 v76, v76\n\t" \
  "v_exp_f32 v77, v77\n\t" \
  "v_smfmac_f32_32x32x32_bf16 v[128:143], v[96:99], v[64:71], v144\n\t" \
  "v_exp_f32 v78, v78\n\t" \
  "v_exp_f32 v79, v79\n\t" \
    \
  "v_exp_f32 v16, v16\n\t" \
  "v_exp_f32 v17, v17\n\t" \
  "v_smfmac_f32_32x32x32_bf16 v[128:143], v[88:91], v[72:79], v144\n\t" \
  "s_mov_b64 exec, s[34:35]\n\t" \
  "ds_read_b128 v[88:91], v110 offset:49280\n\t" \
  "ds_read_b128 v[96:99], v110 offset:49920\n\t" \
  "s_mov_b64 exec, -1\n\t" \
  "v_exp_f32 v18, v18\n\t" \
  "v_exp_f32 v19, v19\n\t" \
  "v_exp_f32 v20, v20\n\t" \
  "v_exp_f32 v21, v21\n\t" \
  "v_mfma_f32_32x32x4_2b_bf16 v[48:79], v[86:87], v[108:109], 4.0 cbsz:1 abid:1\n\t" \
  "ds_read2st64_b64 v[84:87], v111 offset0:104 offset1:105\n\t" \
  "v_exp_f32 v22, v22\n\t" \
  "v_exp_f32 v23, v23\n\t" \
  "v_exp_f32 v24, v24\n\t" \
  "v_exp_f32 v25, v25\n\t" \
  "v_exp_f32 v26, v26\n\t" \
  "v_exp_f32 v27, v27\n\t" \
  "v_exp_f32 v28, v28\n\t" \
  "v_exp_f32 v29, v29\n\t" \
  "v_smfmac_f32_32x32x32_bf16 v[112:127], v[92:95], v[16:23], v144\n\t" \
  "s_waitcnt lgkmcnt(0)\n\t" \
  "v_exp_f32 v30, v30\n\t" \
  "v_exp_f32 v31, v31\n\t" \
  "v_exp_f32 v32, v32\n\t" \
  "v_exp_f32 v33, v33\n\t" \
  "v_smfmac_f32_32x32x32_bf16 v[112:127], v[88:91], v[24:31], v144\n\t" \
  "v_exp_f32 v34, v34\n\t" \
  "v_exp_f32 v35, v35\n\t" \
  "v_exp_f32 v36, v36\n\t" \
  "v_exp_f32 v37, v37\n\t" \
  "v_mfma_f32_32x32x16_bf16 v[48:63], v[80:83], v[100:103], v[48:63]\n\t" \
  "v_exp_f32 v38, v38\n\t" \
  "v_exp_f32 v39, v39\n\t" \
  "v_exp_f32 v40, v40\n\t" \
  "v_exp_f32 v41, v41\n\t" \
  "v_mfma_f32_32x32x16_bf16 v[64:79], v[80:83], v[104:107], v[64:79]\n\t" \
  "ds_read_b128 v[80:83], v110 offset:51200\n\t" \
  "v_exp_f32 v42, v42\n\t" \
  "v_exp_f32 v43, v43\n\t" \
  "v_exp_f32 v44, v44\n\t" \
  "v_exp_f32 v45, v45\n\t" \
  "v_smfmac_f32_32x32x32_bf16 v[128:143], v[92:95], v[32:39], v144\n\t" \
  "v_exp_f32 v46, v46\n\t" \
  "v_exp_f32 v47, v47\n\t" \
    \
  "v_exp_f32 v48, v48\n\t" \
  "v_exp_f32 v49, v49\n\t" \
  "v_smfmac_f32_32x32x32_bf16 v[128:143], v[88:91], v[40:47], v144\n\t" \
  "s_mov_b64 exec, s[34:35]\n\t" \
  "ds_read_b128 v[88:91], v110 offset:50560\n\t" \
  "ds_read_b128 v[92:95], v110 offset:54272\n\t" \
  "s_mov_b64 exec, -1\n\t" \
  "v_exp_f32 v50, v50\n\t" \
  "v_exp_f32 v51, v51\n\t" \
  "v_exp_f32 v52, v52\n\t" \
  "v_exp_f32 v53, v53\n\t" \
  "v_mfma_f32_32x32x4_2b_bf16 v[16:47], v[84:85], v[108:109], 4.0 cbsz:1 abid:0\n\t" \
  "v_exp_f32 v54, v54\n\t" \
  "v_exp_f32 v55, v55\n\t" \
  "v_exp_f32 v56, v56\n\t" \
  "v_exp_f32 v57, v57\n\t" \
  "v_exp_f32 v58, v58\n\t" \
  "v_exp_f32 v59, v59\n\t" \
  "v_exp_f32 v60, v60\n\t" \
  "v_exp_f32 v61, v61\n\t" \
  "v_smfmac_f32_32x32x32_bf16 v[112:127], v[96:99], v[48:55], v144\n\t" \
  "s_waitcnt lgkmcnt(0)\n\t" \
  "v_exp_f32 v62, v62\n\t" \
  "v_exp_f32 v63, v63\n\t" \
  "v_exp_f32 v64, v64\n\t" \
  "v_exp_f32 v65, v65\n\t" \
  "v_smfmac_f32_32x32x32_bf16 v[112:127], v[88:91], v[56:63], v144\n\t" \
  "v_exp_f32 v66, v66\n\t" \
  "v_exp_f32 v67, v67\n\t" \
  "v_exp_f32 v68, v68\n\t" \
  "v_exp_f32 v69, v69\n\t" \
  "v_mfma_f32_32x32x16_bf16 v[16:31], v[80:83], v[100:103], v[16:31]\n\t" \
  "v_exp_f32 v70, v70\n\t" \
  "v_exp_f32 v71, v71\n\t" \
  "v_exp_f32 v72, v72\n\t" \
  "v_exp_f32 v73, v73\n\t" \
  "v_mfma_f32_32x32x16_bf16 v[32:47], v[80:83], v[104:107], v[32:47]\n\t" \
  "ds_read_b128 v[80:83], v110 offset:52224\n\t" \
  "v_exp_f32 v74, v74\n\t" \
  "v_exp_f32 v75, v75\n\t" \
  "v_exp_f32 v76, v76\n\t" \
  "v_exp_f32 v77, v77\n\t" \
  "v_smfmac_f32_32x32x32_bf16 v[128:143], v[96:99], v[64:71], v144\n\t" \
  "s_cmp_eq_u32 s41, 0\n\t" \
  "v_exp_f32 v78, v78\n\t" \
  "v_exp_f32 v79, v79\n\t" \
  "v_add_u32 v110, 51200, v110\n\t" \
  "v_add_u32 v111, 51200, v111\n\t" \
  "s_cbranch_scc1 304f\n\t" \
  "404:\n\t" \
    \
  "v_exp_f32 v16, v16\n\t" \
  "v_exp_f32 v17, v17\n\t" \
  "v_smfmac_f32_32x32x32_bf16 v[128:143], v[88:91], v[72:79], v144\n\t" \
  "s_mov_b64 exec, s[34:35]\n\t" \
  "ds_read_b128 v[88:91], v110 offset:3712\n\t" \
  "ds_read_b128 v[96:99], v110 offset:6400\n\t" \
  "s_mov_b64 exec, -1\n\t" \
  "v_exp_f32 v18, v18\n\t" \
  "v_exp_f32 v19, v19\n\t" \
  "v_exp_f32 v20, v20\n\t" \
  "v_exp_f32 v21, v21\n\t" \
  "v_mfma_f32_32x32x4_2b_bf16 v[48:79], v[84:85], v[108:109], 4.0 cbsz:1 abid:1\n\t" \
  "v_exp_f32 v22, v22\n\t" \
  "v_exp_f32 v23, v23\n\t" \
  "v_exp_f32 v24, v24\n\t" \
  "v_exp_f32 v25, v25\n\t" \
  "v_exp_f32 v26, v26\n\t" \
  "v_exp_f32 v27, v27\n\t" \
  "v_exp_f32 v28, v28\n\t" \
  "v_exp_f32 v29, v29\n\t" \
  "v_smfmac_f32_32x32x32_bf16 v[112:127], v[92:95], v[16:23], v144\n\t" \
  "s_waitcnt lgkmcnt(0)\n\t" \
  "v_exp_f32 v30, v30\n\t" \
  "v_exp_f32 v31, v31\n\t" \
  "v_exp_f32 v32, v32\n\t" \
  "v_exp_f32 v33, v33\n\t" \
  "v_smfmac_f32_32x32x32_bf16 v[112:127], v[88:91], v[24:31], v144\n\t" \
  "v_exp_f32 v34, v34\n\t" \
  "v_exp_f32 v35, v35\n\t" \
  "v_exp_f32 v36, v36\n\t" \
  "v_exp_f32 v37, v37\n\t" \
  "v_mfma_f32_32x32x16_bf16 v[48:63], v[80:83], v[100:103], v[48:63]\n\t" \
  "v_exp_f32 v38, v38\n\t" \
  "v_exp_f32 v39, v39\n\t" \
  "v_exp_f32 v40, v40\n\t" \
  "v_exp_f32 v41, v41\n\t" \
  "v_mfma_f32_32x32x16_bf16 v[64:79], v[80:83], v[104:107], v[64:79]\n\t" \
  "ds_read_b128 v[80:83], v110 offset:4352\n\t" \
  "v_exp_f32 v42, v42\n\t" \
  "v_exp_f32 v43, v43\n\t" \
  "v_exp_f32 v44, v44\n\t" \
  "v_exp_f32 v45, v45\n\t" \
  "v_smfmac_f32_32x32x32_bf16 v[128:143], v[92:95], v[32:39], v144\n\t" \
  "v_exp_f32 v46, v46\n\t" \
  "v_exp_f32 v47, v47\n\t" \
    \
  "v_exp_f32 v48, v48\n\t" \
  "v_exp_f32 v49, v49\n\t" \
  "v_smfmac_f32_32x32x32_bf16 v[128:143], v[88:91], v[40:47], v144\n\t" \
  "s_mov_b64 exec, s[34:35]\n\t" \
  "ds_read_b128 v[88:91], v110 offset:7040\n\t" \
  "ds_read_b128 v[92:95], v110 offset:7680\n\t" \
  "s_mov_b64 exec, -1\n\t" \
  "v_exp_f32 v50, v50\n\t" \
  "v_exp_f32 v51, v51\n\t" \
  "v_exp_f32 v52, v52\n\t" \
  "v_exp_f32 v53, v53\n\t" \
  "v_mfma_f32_32x32x4_2b_bf16 v[16:47], v[86:87], v[108:109], 4.0 cbsz:1 abid:0\n\t" \
  "v_exp_f32 v54, v54\n\t" \
  "v_exp_f32 v55, v55\n\t" \
  "v_exp_f32 v56, v56\n\t" \
  "v_exp_f32 v57, v57\n\t" \
  "v_exp_f32 v58, v58\n\t" \
  "v_exp_f32 v59, v59\n\t" \
  "v_exp_f32 v60, v60\n\t" \
  "v_exp_f32 v61, v61\n\t" \
  "v_smfmac_f32_32x32x32_bf16 v[112:127], v[96:99], v[48:55], v144\n\t" \
  "s_waitcnt lgkmcnt(0)\n\t" \
  "v_exp_f32 v62, v62\n\t" \
  "v_exp_f32 v63, v63\n\t" \
  "v_exp_f32 v64, v64\n\t" \
  "v_exp_f32 v65, v65\n\t" \
  "v_smfmac_f32_32x32x32_bf16 v[112:127], v[88:91], v[56:63], v144\n\t" \
  "v_exp_f32 v66, v66\n\t" \
  "v_exp_f32 v67, v67\n\t" \
  "v_exp_f32 v68, v68\n\t" \
  "v_exp_f32 v69, v69\n\t" \
  "v_mfma_f32_32x32x16_bf16 v[16:31], v[80:83], v[100:103], v[16:31]\n\t" \
  "v_exp_f32 v70, v70\n\t" \
  "v_exp_f32 v71, v71\n\t" \
  "v_exp_f32 v72, v72\n\t" \
  "v_exp_f32 v73, v73\n\t" \
  "v_mfma_f32_32x32x16_bf16 v[32:47], v[80:83], v[104:107], v[32:47]\n\t" \
  "ds_read_b128 v[80:83], v110 offset:5376\n\t" \
  "v_exp_f32 v74, v74\n\t" \
  "v_exp_f32 v75, v75\n\t" \
  "v_exp_f32 v76, v76\n\t" \
  "v_exp_f32 v77, v77\n\t" \
  "v_smfmac_f32_32x32x32_bf16 v[128:143], v[96:99], v[64:71], v144\n\t" \
  "v_exp_f32 v78, v78\n\t" \
  "v_exp_f32 v79, v79\n\t" \
    \
  "v_exp_f32 v16, v16\n\t" \
  "v_exp_f32 v17, v17\n\t" \
  "v_smfmac_f32_32x32x32_bf16 v[128:143], v[88:91], v[72:79], v144\n\t" \
  "s_mov_b64 exec, s[34:35]\n\t" \
  "ds_read_b128 v[88:91], v110 offset:8320\n\t" \
  "ds_read_b128 v[96:99], v110 offset:8960\n\t" \
  "s_mov_b64 exec, -1\n\t" \
  "v_exp_f32 v18, v18\n\t" \
  "v_exp_f32 v19, v19\n\t" \
  "v_exp_f32 v20, v20\n\t" \
  "v_exp_f32 v21, v21\n\t" \
  "v_mfma_f32_32x32x4_2b_bf16 v[48:79], v[86:87], v[108:109], 4.0 cbsz:1 abid:1\n\t" \
  "ds_read2st64_b64 v[84:87], v111 offset0:24 offset1:25\n\t" \
  "v_exp_f32 v22, v22\n\t" \
  "v_exp_f32 v23, v23\n\t" \
  "v_exp_f32 v24, v24\n\t" \
  "v_exp_f32 v25, v25\n\t" \
  "v_exp_f32 v26, v26\n\t" \
  "v_exp_f32 v27, v27\n\t" \
  "v_exp_f32 v28, v28\n\t" \
  "v_exp_f32 v29, v29\n\t" \
  "v_smfmac_f32_32x32x32_bf16 v[112:127], v[92:95], v[16:23], v144\n\t" \
  "s_waitcnt lgkmcnt(0)\n\t" \
  "v_exp_f32 v30, v30\n\t" \
  "v_exp_f32 v31, v31\n\t" \
  "v_exp_f32 v32, v32\n\t" \
  "v_exp_f32 v33, v33\n\t" \
  "v_smfmac_f32_32x32x32_bf16 v[112:127], v[88:91], v[24:31], v144\n\t" \
  "v_exp_f32 v34, v34\n\t" \
  "v_exp_f32 v35, v35\n\t" \
  "v_exp_f32 v36, v36\n\t" \
  "v_exp_f32 v37, v37\n\t" \
  "v_mfma_f32_32x32x16_bf16 v[48:63], v[80:83], v[100:103], v[48:63]\n\t" \
  "v_exp_f32 v38, v38\n\t" \
  "v_exp_f32 v39, v39\n\t" \
  "v_exp_f32 v40, v40\n\t" \
  "v_exp_f32 v41, v41\n\t" \
  "v_mfma_f32_32x32x16_bf16 v[64:79], v[80:83], v[104:107], v[64:79]\n\t" \
  "ds_read_b128 v[80:83], v110 offset:10240\n\t" \
  "v_exp_f32 v42, v42\n\t" \
  "v_exp_f32 v43, v43\n\t" \
  "v_exp_f32 v44, v44\n\t" \
  "v_exp_f32 v45, v45\n\t" \
  "v_smfmac_f32_32x32x32_bf16 v[128:143], v[92:95], v[32:39], v144\n\t" \
  "v_exp_f32 v46, v46\n\t" \
  "v_exp_f32 v47, v47\n\t" \
    \
  "v_exp_f32 v48, v48\n\t" \
  "v_exp_f32 v49, v49\n\t" \
  "v_smfmac_f32_32x32x32_bf16 v[128:143], v[88:91], v[40:47], v144\n\t" \
  "s_mov_b64 exec, s[34:35]\n\t" \
  "ds_read_b128 v[88:91], v110 offset:9600\n\t" \
  "ds_read_b128 v[92:95], v110 offset:13312\n\t" \
  "s_mov_b64 exec, -1\n\t" \
  "v_exp_f32 v50, v50\n\t" \
  "v_exp_f32 v51, v51\n\t" \
  "v_exp_f32 v52, v52\n\t" \
  "v_exp_f32 v53, v53\n\t" \
  "v_mfma_f32_32x32x4_2b_bf16 v[16:47], v[84:85], v[108:109], 4.0 cbsz:1 abid:0\n\t" \
  "v_exp_f32 v54, v54\n\t" \
  "v_exp_f32 v55, v55\n\t" \
  "v_exp_f32 v56, v56\n\t" \
  "v_exp_f32 v57, v57\n\t" \
  "v_exp_f32 v58, v58\n\t" \
  "v_exp_f32 v59, v59\n\t" \
  "v_exp_f32 v60, v60\n\t" \
  "v_exp_f32 v61, v61\n\t" \
  "v_smfmac_f32_32x32x32_bf16 v[112:127], v[96:99], v[48:55], v144\n\t" \
  "s_waitcnt lgkmcnt(0)\n\t" \
  "v_exp_f32 v62, v62\n\t" \
  "v_exp_f32 v63, v63\n\t" \
  "v_exp_f32 v64, v64\n\t" \
  "v_exp_f32 v65, v65\n\t" \
  "v_smfmac_f32_32x32x32_bf16 v[112:127], v[88:91], v[56:63], v144\n\t" \
  "v_exp_f32 v66, v66\n\t" \
  "v_exp_f32 v67, v67\n\t" \
  "v_exp_f32 v68, v68\n\t" \
  "v_exp_f32 v69, v69\n\t" \
  "v_mfma_f32_32x32x16_bf16 v[16:31], v[80:83], v[100:103], v[16:31]\n\t" \
  "v_exp_f32 v70, v70\n\t" \
  "v_exp_f32 v71, v71\n\t" \
  "v_exp_f32 v72, v72\n\t" \
  "v_exp_f32 v73, v73\n\t" \
  "v_mfma_f32_32x32x16_bf16 v[32:47], v[80:83], v[104:107], v[32:47]\n\t" \
  "ds_read_b128 v[80:83], v110 offset:11264\n\t" \
  "v_exp_f32 v74, v74\n\t" \
  "v_exp_f32 v75, v75\n\t" \
  "v_exp_f32 v76, v76\n\t" \
  "v_exp_f32 v77, v77\n\t" \
  "v_smfmac_f32_32x32x32_bf16 v[128:143], v[96:99], v[64:71], v144\n\t" \
  "v_exp_f32 v78, v78\n\t" \
  "v_exp_f32 v79, v79\n\t" \
    \
  "v_exp_f32 v16, v16\n\t" \
  "v_exp_f32 v17, v17\n\t" \
  "v_smfmac_f32_32x32x32_bf16 v[128:143], v[88:91], v[72:79], v144\n\t" \
  "s_mov_b64 exec, s[34:35]\n\t" \
  "ds_read_b128 v[88:91], v110 offset:13952\n\t" \
  "ds_read_b128 v[96:99], v110 offset:16640\n\t" \
  "s_mov_b64 exec, -1\n\t" \
  "v_exp_f32 v18, v18\n\t" \
  "v_exp_f32 v19, v19\n\t" \
  "v_exp_f32 v20, v20\n\t" \
  "v_exp_f32 v21, v21\n\t" \
  "v_mfma_f32_32x32x4_2b_bf16 v[48:79], v[84:85], v[108:109], 4.0 cbsz:1 abid:1\n\t" \
  "v_exp_f32 v22, v22\n\t" \
  "v_exp_f32 v23, v23\n\t" \
  "v_exp_f32 v24, v24\n\t" \
  "v_exp_f32 v25, v25\n\t" \
  "v_exp_f32 v26, v26\n\t" \
  "v_exp_f32 v27, v27\n\t" \
  "v_exp_f32 v28, v28\n\t" \
  "v_exp_f32 v29, v29\n\t" \
  "v_smfmac_f32_32x32x32_bf16 v[112:127], v[92:95], v[16:23], v144\n\t" \
  "s_waitcnt lgkmcnt(0)\n\t" \
  "v_exp_f32 v30, v30\n\t" \
  "v_exp_f32 v31, v31\n\t" \
  "v_exp_f32 v32, v32\n\t" \
  "v_exp_f32 v33, v33\n\t" \
  "v_smfmac_f32_32x32x32_bf16 v[112:127], v[88:91], v[24:31], v144\n\t" \
  "v_exp_f32 v34, v34\n\t" \
  "v_exp_f32 v35, v35\n\t" \
  "v_exp_f32 v36, v36\n\t" \
  "v_exp_f32 v37, v37\n\t" \
  "v_mfma_f32_32x32x16_bf16 v[48:63], v[80:83], v[100:103], v[48:63]\n\t" \
  "v_exp_f32 v38, v38\n\t" \
  "v_exp_f32 v39, v39\n\t" \
  "v_exp_f32 v40, v40\n\t" \
  "v_exp_f32 v41, v41\n\t" \
  "v_mfma_f32_32x32x16_bf16 v[64:79], v[80:83], v[104:107], v[64:79]\n\t" \
  "ds_read_b128 v[80:83], v110 offset:14592\n\t" \
  "v_exp_f32 v42, v42\n\t" \
  "v_exp_f32 v43, v43\n\t" \
  "v_exp_f32 v44, v44\n\t" \
  "v_exp_f32 v45, v45\n\t" \
  "v_smfmac_f32_32x32x32_bf16 v[128:143], v[92:95], v[32:39], v144\n\t" \
  "v_exp_f32 v46, v46\n\t" \
  "v_exp_f32 v47, v47\n\t" \
    \
  "v_exp_f32 v48, v48\n\t" \
  "v_exp_f32 v49, v49\n\t" \
  "v_smfmac_f32_32x32x32_bf16 v[128:143], v[88:91], v[40:47], v144\n\t" \
  "s_mov_b64 exec, s[34:35]\n\t" \
  "ds_read_b128 v[88:91], v110 offset:17280\n\t" \
  "ds_read_b128 v[92:95], v110 offset:17920\n\t" \
  "s_mov_b64 exec, -1\n\t" \
  "v_exp_f32 v50, v50\n\t" \
  "v_exp_f32 v51, v51\n\t" \
  "v_exp_f32 v52, v52\n\t" \
  "v_exp_f32 v53, v53\n\t" \
  "v_mfma_f32_32x32x4_2b_bf16 v[16:47], v[86:87], v[108:109], 4.0 cbsz:1 abid:0\n\t" \
  "v_exp_f32 v54, v54\n\t" \
  "v_exp_f32 v55, v55\n\t" \
  "v_exp_f32 v56, v56\n\t" \
  "v_exp_f32 v57, v57\n\t" \
  "v_exp_f32 v58, v58\n\t" \
  "v_exp_f32 v59, v59\n\t" \
  "v_exp_f32 v60, v60\n\t" \
  "v_exp_f32 v61, v61\n\t" \
  "v_smfmac_f32_32x32x32_bf16 v[112:127], v[96:99], v[48:55], v144\n\t" \
  "s_waitcnt lgkmcnt(0)\n\t" \
  "v_exp_f32 v62, v62\n\t" \
  "v_exp_f32 v63, v63\n\t" \
  "v_exp_f32 v64, v64\n\t" \
  "v_exp_f32 v65, v65\n\t" \
  "v_smfmac_f32_32x32x32_bf16 v[112:127], v[88:91], v[56:63], v144\n\t" \
  "v_exp_f32 v66, v66\n\t" \
  "v_exp_f32 v67, v67\n\t" \
  "v_exp_f32 v68, v68\n\t" \
  "v_exp_f32 v69, v69\n\t" \
  "v_mfma_f32_32x32x16_bf16 v[16:31], v[80:83], v[100:103], v[16:31]\n\t" \
  "v_exp_f32 v70, v70\n\t" \
  "v_exp_f32 v71, v71\n\t" \
  "v_exp_f32 v72, v72\n\t" \
  "v_exp_f32 v73, v73\n\t" \
  "v_mfma_f32_32x32x16_bf16 v[32:47], v[80:83], v[104:107], v[32:47]\n\t" \
  "ds_read_b128 v[80:83], v110 offset:15616\n\t" \
  "v_exp_f32 v74, v74\n\t" \
  "v_exp_f32 v75, v75\n\t" \
  "v_exp_f32 v76, v76\n\t" \
  "v_exp_f32 v77, v77\n\t" \
  "v_smfmac_f32_32x32x32_bf16 v[128:143], v[96:99], v[64:71], v144\n\t" \
  "v_exp_f32 v78, v78\n\t" \
  "v_exp_f32 v79, v79\n\t" \
    \
  "v_exp_f32 v16, v16\n\t" \
  "v_exp_f32 v17, v17\n\t" \
  "v_smfmac_f32_32x32x32_bf16 v[128:143], v[88:91], v[72:79], v144\n\t" \
  "s_mov_b64 exec, s[34:35]\n\t" \
  "ds_read_b128 v[88:91], v110 offset:18560\n\t" \
  "ds_read_b128 v[96:99], v110 offset:19200\n\t" \
  "s_mov_b64 exec, -1\n\t" \
  "v_exp_f32 v18, v18\n\t" \
  "v_exp_f32 v19, v19\n\t" \
  "v_exp_f32 v20, v20\n\t" \
  "v_exp_f32 v21, v21\n\t" \
  "v_mfma_f32_32x32x4_2b_bf16 v[48:79], v[86:87], v[108:109], 4.0 cbsz:1 abid:1\n\t" \
  "ds_read2st64_b64 v[84:87], v111 offset0:44 offset1:45\n\t" \
  "v_exp_f32 v22, v22\n\t" \
  "v_exp_f32 v23, v23\n\t" \
  "v_exp_f32 v24, v24\n\t" \
  "v_exp_f32 v25, v25\n\t" \
  "v_exp_f32 v26, v26\n\t" \
  "v_exp_f32 v27, v27\n\t" \
  "v_exp_f32 v28, v28\n\t" \
  "v_exp_f32 v29, v29\n\t" \
  "v_smfmac_f32_32x32x32_bf16 v[112:127], v[92:95], v[16:23], v144\n\t" \
  "s_waitcnt lgkmcnt(0)\n\t" \
  "v_exp_f32 v30, v30\n\t" \
  "v_exp_f32 v31, v31\n\t" \
  "v_exp_f32 v32, v32\n\t" \
  "v_exp_f32 v33, v33\n\t" \
  "v_smfmac_f32_32x32x32_bf16 v[112:127], v[88:91], v[24:31], v144\n\t" \
  "v_exp_f32 v34, v34\n\t" \
  "v_exp_f32 v35, v35\n\t" \
  "v_exp_f32 v36, v36\n\t" \
  "v_exp_f32 v37, v37\n\t" \
  "v_mfma_f32_32x32x16_bf16 v[48:63], v[80:83], v[100:103], v[48:63]\n\t" \
  "v_exp_f32 v38, v38\n\t" \
  "v_exp_f32 v39, v39\n\t" \
  "v_exp_f32 v40, v40\n\t" \
  "v_exp_f32 v41, v41\n\t" \
  "v_mfma_f32_32x32x16_bf16 v[64:79], v[80:83], v[104:107], v[64:79]\n\t" \
  "ds_read_b128 v[80:83], v110 offset:20480\n\t" \
  "v_exp_f32 v42, v42\n\t" \
  "v_exp_f32 v43, v43\n\t" \
  "v_exp_f32 v44, v44\n\t" \
  "v_exp_f32 v45, v45\n\t" \
  "v_smfmac_f32_32x32x32_bf16 v[128:143], v[92:95], v[32:39], v144\n\t" \
  "v_exp_f32 v46, v46\n\t" \
  "v_exp_f32 v47, v47\n\t" \
    \
  "v_exp_f32 v48, v48\n\t" \
  "v_exp_f32 v49, v49\n\t" \
  "v_smfmac_f32_32x32x32_bf16 v[128:143], v[88:91], v[40:47], v144\n\t" \
  "s_mov_b64 exec, s[34:35]\n\t" \
  "ds_read_b128 v[88:91], v110 offset:19840\n\t" \
  "ds_read_b128 v[92:95], v110 offset:23552\n\t" \
  "s_mov_b64 exec, -1\n\t" \
  "v_exp_f32 v50, v50\n\t" \
  "v_exp_f32 v51, v51\n\t" \
  "v_exp_f32 v52, v52\n\t" \
  "v_exp_f32 v53, v53\n\t" \
  "v_mfma_f32_32x32x4_2b_bf16 v[16:47], v[84:85], v[108:109], 4.0 cbsz:1 abid:0\n\t" \
  "v_exp_f32 v54, v54\n\t" \
  "v_exp_f32 v55, v55\n\t" \
  "v_exp_f32 v56, v56\n\t" \
  "v_exp_f32 v57, v57\n\t" \
  "v_exp_f32 v58, v58\n\t" \
  "v_exp_f32 v59, v59\n\t" \
  "v_exp_f32 v60, v60\n\t" \
  "v_exp_f32 v61, v61\n\t" \
  "v_smfmac_f32_32x32x32_bf16 v[112:127], v[96:99], v[48:55], v144\n\t" \
  "s_waitcnt lgkmcnt(0)\n\t" \
  "v_exp_f32 v62, v62\n\t" \
  "v_exp_f32 v63, v63\n\t" \
  "v_exp_f32 v64, v64\n\t" \
  "v_exp_f32 v65, v65\n\t" \
  "v_smfmac_f32_32x32x32_bf16 v[112:127], v[88:91], v[56:63], v144\n\t" \
  "buffer_load_dwordx2 v[4:5], v147, s[28:31], 0 offen sc1\n\t" \
  "v_exp_f32 v66, v66\n\t" \
  "v_exp_f32 v67, v67\n\t" \
  "v_exp_f32 v68, v68\n\t" \
  "v_exp_f32 v69, v69\n\t" \
  "v_mfma_f32_32x32x16_bf16 v[16:31], v[80:83], v[100:103], v[16:31]\n\t" \
  "v_exp_f32 v70, v70\n\t" \
  "v_exp_f32 v71, v71\n\t" \
  "v_exp_f32 v72, v72\n\t" \
  "v_exp_f32 v73, v73\n\t" \
  "v_mfma_f32_32x32x16_bf16 v[32:47], v[80:83], v[104:107], v[32:47]\n\t" \
  "ds_read_b128 v[80:83], v110 offset:21504\n\t" \
  "v_exp_f32 v74, v74\n\t" \
  "v_exp_f32 v75, v75\n\t" \
  "v_exp_f32 v76, v76\n\t" \
  "v_exp_f32 v77, v77\n\t" \
  "v_smfmac_f32_32x32x32_bf16 v[128:143], v[96:99], v[64:71], v144\n\t" \
  "v_exp_f32 v78, v78\n\t" \
  "v_exp_f32 v79, v79\n\t" \
    \
  "v_exp_f32 v16, v16\n\t" \
  "v_exp_f32 v17, v17\n\t" \
  "v_smfmac_f32_32x32x32_bf16 v[128:143], v[88:91], v[72:79], v144\n\t" \
  "s_mov_b64 exec, s[34:35]\n\t" \
  "ds_read_b128 v[88:91], v110 offset:24192\n\t" \
  "ds_read_b128 v[96:99], v110 offset:26880\n\t" \
  "s_mov_b64 exec, -1\n\t" \
  "v_exp_f32 v18, v18\n\t" \
  "v_exp_f32 v19, v19\n\t" \
  "v_exp_f32 v20, v20\n\t" \
  "v_exp_f32 v21, v21\n\t" \
  "v_mfma_f32_32x32x4_2b_bf16 v[48:79], v[84:85], v[108:109], 4.0 cbsz:1 abid:1\n\t" \
  "v_exp_f32 v22, v22\n\t" \
  "v_exp_f32 v23, v23\n\t" \
  "v_exp_f32 v24, v24\n\t" \
  "v_exp_f32 v25, v25\n\t" \
  "v_exp_f32 v26, v26\n\t" \
  "v_exp_f32 v27, v27\n\t" \
  "v_exp_f32 v28, v28\n\t" \
  "v_exp_f32 v29, v29\n\t" \
  "v_smfmac_f32_32x32x32_bf16 v[112:127], v[92:95], v[16:23], v144\n\t" \
  "s_waitcnt lgkmcnt(0)\n\t" \
  "v_exp_f32 v30, v30\n\t" \
  "v_exp_f32 v31, v31\n\t" \
  "v_exp_f32 v32, v32\n\t" \
  "v_exp_f32 v33, v33\n\t" \
  "v_smfmac_f32_32x32x32_bf16 v[112:127], v[88:91], v[24:31], v144\n\t" \
  "v_exp_f32 v34, v34\n\t" \
  "v_exp_f32 v35, v35\n\t" \
  "v_exp_f32 v36, v36\n\t" \
  "v_exp_f32 v37, v37\n\t" \
  "v_mfma_f32_32x32x16_bf16 v[48:63], v[80:83], v[100:103], v[48:63]\n\t" \
  "v_exp_f32 v38, v38\n\t" \
  "v_exp_f32 v39, v39\n\t" \
  "v_exp_f32 v40, v40\n\t" \
  "v_exp_f32 v41, v41\n\t" \
  "v_mfma_f32_32x32x16_bf16 v[64:79], v[80:83], v[104:107], v[64:79]\n\t" \
  "ds_read_b128 v[80:83], v110 offset:24832\n\t" \
  "v_exp_f32 v42, v42\n\t" \
  "v_exp_f32 v43, v43\n\t" \
  "v_exp_f32 v44, v44\n\t" \
  "v_exp_f32 v45, v45\n\t" \
  "v_smfmac_f32_32x32x32_bf16 v[128:143], v[92:95], v[32:39], v144\n\t" \
  "v_exp_f32 v46, v46\n\t" \
  "v_exp_f32 v47, v47\n\t" \
    \
  "v_exp_f32 v48, v48\n\t" \
  "v_exp_f32 v49, v49\n\t" \
  "v_smfmac_f32_32x32x32_bf16 v[128:143], v[88:91], v[40:47], v144\n\t" \
  "s_mov_b64 exec, s[34:35]\n\t" \
  "ds_read_b128 v[88:91], v110 offset:27520\n\t" \
  "ds_read_b128 v[92:95], v110 offset:28160\n\t" \
  "s_mov_b64 exec, -1\n\t" \
  "v_exp_f32 v50, v50\n\t" \
  "v_exp_f32 v51, v51\n\t" \
  "v_exp_f32 v52, v52\n\t" \
  "v_exp_f32 v53, v53\n\t" \
  "v_mfma_f32_32x32x4_2b_bf16 v[16:47], v[86:87], v[108:109], 4.0 cbsz:1 abid:0\n\t" \
  "v_exp_f32 v54, v54\n\t" \
  "v_exp_f32 v55, v55\n\t" \
  "v_exp_f32 v56, v56\n\t" \
  "v_exp_f32 v57, v57\n\t" \
  "v_exp_f32 v58, v58\n\t" \
  "v_exp_f32 v59, v59\n\t" \
  "v_exp_f32 v60, v60\n\t" \
  "v_exp_f32 v61, v61\n\t" \
  "v_smfmac_f32_32x32x32_bf16 v[112:127], v[96:99], v[48:55], v144\n\t" \
  "s_waitcnt lgkmcnt(0)\n\t" \
  "v_exp_f32 v62, v62\n\t" \
  "v_exp_f32 v63, v63\n\t" \
  "v_exp_f32 v64, v64\n\t" \
  "v_exp_f32 v65, v65\n\t" \
  "v_smfmac_f32_32x32x32_bf16 v[112:127], v[88:91], v[56:63], v144\n\t" \
  "v_exp_f32 v66, v66\n\t" \
  "v_exp_f32 v67, v67\n\t" \
  "v_exp_f32 v68, v68\n\t" \
  "v_exp_f32 v69, v69\n\t" \
  "v_mfma_f32_32x32x16_bf16 v[16:31], v[80:83], v[100:103], v[16:31]\n\t" \
  "v_exp_f32 v70, v70\n\t" \
  "v_exp_f32 v71, v71\n\t" \
  "v_exp_f32 v72, v72\n\t" \
  "v_exp_f32 v73, v73\n\t" \
  "v_mfma_f32_32x32x16_bf16 v[32:47], v[80:83], v[104:107], v[32:47]\n\t" \
  "ds_read_b128 v[80:83], v110 offset:25856\n\t" \
  "v_exp_f32 v74, v74\n\t" \
  "v_exp_f32 v75, v75\n\t" \
  "v_exp_f32 v76, v76\n\t" \
  "v_exp_f32 v77, v77\n\t" \
  "v_smfmac_f32_32x32x32_bf16 v[128:143], v[96:99], v[64:71], v144\n\t" \
  "v_exp_f32 v78, v78\n\t" \
  "v_exp_f32 v79, v79\n\t" \
    \
  "v_exp_f32 v16, v16\n\t" \
  "v_exp_f32 v17, v17\n\t" \
  "v_smfmac_f32_32x32x32_bf16 v[128:143], v[88:91], v[72:79], v144\n\t" \
  "s_mov_b64 exec, s[34:35]\n\t" \
  "ds_read_b128 v[88:91], v110 offset:28800\n\t" \
  "ds_read_b128 v[96:99], v110 offset:29440\n\t" \
  "s_mov_b64 exec, -1\n\t" \
  "v_exp_f32 v18, v18\n\t" \
  "v_exp_f32 v19, v19\n\t" \
  "v_exp_f32 v20, v20\n\t" \
  "v_exp_f32 v21, v21\n\t" \
  "v_mfma_f32_32x32x4_2b_bf16 v[48:79], v[86:87], v[108:109], 4.0 cbsz:1 abid:1\n\t" \
  "ds_read2st64_b64 v[84:87], v111 offset0:64 offset1:65\n\t" \
  "v_exp_f32 v22, v22\n\t" \
  "v_exp_f32 v23, v23\n\t" \
  "v_exp_f32 v24, v24\n\t" \
  "v_exp_f32 v25, v25\n\t" \
  "v_exp_f32 v26, v26\n\t" \
  "v_exp_f32 v27, v27\n\t" \
  "v_exp_f32 v28, v28\n\t" \
  "v_exp_f32 v29, v29\n\t" \
  "v_smfmac_f32_32x32x32_bf16 v[112:127], v[92:95], v[16:23], v144\n\t" \
  "s_waitcnt lgkmcnt(0)\n\t" \
  "v_exp_f32 v30, v30\n\t" \
  "v_exp_f32 v31, v31\n\t" \
  "v_exp_f32 v32, v32\n\t" \
  "v_exp_f32 v33, v33\n\t" \
  "v_smfmac_f32_32x32x32_bf16 v[112:127], v[88:91], v[24:31], v144\n\t" \
  "v_exp_f32 v34, v34\n\t" \
  "v_exp_f32 v35, v35\n\t" \
  "v_exp_f32 v36, v36\n\t" \
  "v_exp_f32 v37, v37\n\t" \
  "v_mfma_f32_32x32x16_bf16 v[48:63], v[80:83], v[100:103], v[48:63]\n\t" \
  "v_exp_f32 v38, v38\n\t" \
  "v_exp_f32 v39, v39\n\t" \
  "v_exp_f32 v40, v40\n\t" \
  "v_exp_f32 v41, v41\n\t" \
  "v_mfma_f32_32x32x16_bf16 v[64:79], v[80:83], v[104:107], v[64:79]\n\t" \
  "ds_read_b128 v[80:83], v110 offset:30720\n\t" \
  "v_exp_f32 v42, v42\n\t" \
  "v_exp_f32 v43, v43\n\t" \
  "v_exp_f32 v44, v44\n\t" \
  "v_exp_f32 v45, v45\n\t" \
  "v_smfmac_f32_32x32x32_bf16 v[128:143], v[92:95], v[32:39], v144\n\t" \
  "v_exp_f32 v46, v46\n\t" \
  "v_exp_f32 v47, v47\n\t" \
    \
  "v_exp_f32 v48, v48\n\t" \
  "v_exp_f32 v49, v49\n\t" \
  "v_smfmac_f32_32x32x32_bf16 v[128:143], v[88:91], v[40:47], v144\n\t" \
  "s_mov_b64 exec, s[34:35]\n\t" \
  "ds_read_b128 v[88:91], v110 offset:30080\n\t" \
  "ds_read_b128 v[92:95], v110 offset:33792\n\t" \
  "s_mov_b64 exec, -1\n\t" \
  "v_exp_f32 v50, v50\n\t" \
  "v_exp_f32 v51, v51\n\t" \
  "v_exp_f32 v52, v52\n\t" \
  "v_exp_f32 v53, v53\n\t" \
  "v_mfma_f32_32x32x4_2b_bf16 v[16:47], v[84:85], v[108:109], 4.0 cbsz:1 abid:0\n\t" \
  "v_exp_f32 v54, v54\n\t" \
  "v_exp_f32 v55, v55\n\t" \
  "v_exp_f32 v56, v56\n\t" \
  "v_exp_f32 v57, v57\n\t" \
  "v_exp_f32 v58, v58\n\t" \
  "v_exp_f32 v59, v59\n\t" \
  "v_exp_f32 v60, v60\n\t" \
  "v_exp_f32 v61, v61\n\t" \
  "v_smfmac_f32_32x32x32_bf16 v[112:127], v[96:99], v[48:55], v144\n\t" \
  "s_waitcnt lgkmcnt(0)\n\t" \
  "v_exp_f32 v62, v62\n\t" \
  "v_exp_f32 v63, v63\n\t" \
  "v_exp_f32 v64, v64\n\t" \
  "v_exp_f32 v65, v65\n\t" \
  "v_smfmac_f32_32x32x32_bf16 v[112:127], v[88:91], v[56:63], v144\n\t" \
  "v_exp_f32 v66, v66\n\t" \
  "v_exp_f32 v67, v67\n\t" \
  "v_exp_f32 v68, v68\n\t" \
  "v_exp_f32 v69, v69\n\t" \
  "v_mfma_f32_32x32x16_bf16 v[16:31], v[80:83], v[100:103], v[16:31]\n\t" \
  "s_waitcnt vmcnt(0)\n\t" \
  "v_cmp_ne_u64 vcc, s[64:65], v[4:5]\n\t" \
  "v_exp_f32 v70, v70\n\t" \
  "v_exp_f32 v71, v71\n\t" \
  "v_exp_f32 v72, v72\n\t" \
  "v_exp_f32 v73, v73\n\t" \
  "v_mfma_f32_32x32x16_bf16 v[32:47], v[80:83], v[104:107], v[32:47]\n\t" \
  "ds_read_b128 v[80:83], v110 offset:31744\n\t" \
  "v_exp_f32 v74, v74\n\t" \
  "v_exp_f32 v75, v75\n\t" \
  "v_exp_f32 v76, v76\n\t" \
  "v_exp_f32 v77, v77\n\t" \
  "v_smfmac_f32_32x32x32_bf16 v[128:143], v[96:99], v[64:71], v144\n\t" \
  "v_exp_f32 v78, v78\n\t" \
  "v_exp_f32 v79, v79\n\t" \
  "s_cbranch_vccnz 3f\n\t" \
  "5:\n\t" \
    \
  "v_exp_f32 v16, v16\n\t" \
  "v_exp_f32 v17, v17\n\t" \
  "v_smfmac_f32_32x32x32_bf16 v[128:143], v[88:91], v[72:79], v144\n\t" \
  "s_mov_b64 exec, s[34:35]\n\t" \
  "ds_read_b128 v[88:91], v110 offset:34432\n\t" \
  "ds_read_b128 v[96:99], v110 offset:37120\n\t" \
  "s_mov_b64 exec, -1\n\t" \
  "v_exp_f32 v18, v18\n\t" \
  "v_exp_f32 v19, v19\n\t" \
  "v_exp_f32 v20, v20\n\t" \
  "v_exp_f32 v21, v21\n\t" \
  "v_mfma_f32_32x32x4_2b_bf16 v[48:79], v[84:85], v[108:109], 4.0 cbsz:1 abid:1\n\t" \
  "v_exp_f32 v22, v22\n\t" \
  "v_exp_f32 v23, v23\n\t" \
  "v_exp_f32 v24, v24\n\t" \
  "v_exp_f32 v25, v25\n\t" \
  "v_exp_f32 v26, v26\n\t" \
  "v_exp_f32 v27, v27\n\t" \
  "v_exp_f32 v28, v28\n\t" \
  "v_exp_f32 v29, v29\n\t" \
  "v_smfmac_f32_32x32x32_bf16 v[112:127], v[92:95], v[16:23], v144\n\t" \
  "s_waitcnt lgkmcnt(0)\n\t" \
  "v_exp_f32 v30, v30\n\t" \
  "v_exp_f32 v31, v31\n\t" \
  "v_exp_f32 v32, v32\n\t" \
  "v_exp_f32 v33, v33\n\t" \
  "v_smfmac_f32_32x32x32_bf16 v[112:127], v[88:91], v[24:31], v144\n\t" \
  "s_add_u32 s39, s43, 0\n\t" \
  "buffer_load_dwordx4 v[148:151], v2, s[28:31], s39 offen offset:0 sc1\n\t" \
  "v_exp_f32 v34, v34\n\t" \
  "v_exp_f32 v35, v35\n\t" \
  "v_exp_f32 v36, v36\n\t" \
  "v_exp_f32 v37, v37\n\t" \
  "v_mfma_f32_32x32x16_bf16 v[48:63], v[80:83], v[100:103], v[48:63]\n\t" \
  "v_exp_f32 v38, v38\n\t" \
  "v_exp_f32 v39, v39\n\t" \
  "v_exp_f32 v40, v40\n\t" \
  "v_exp_f32 v41, v41\n\t" \
  "v_mfma_f32_32x32x16_bf16 v[64:79], v[80:83], v[104:107], v[64:79]\n\t" \
  "ds_read_b128 v[80:83], v110 offset:35072\n\t" \
  "v_exp_f32 v42, v42\n\t" \
  "v_exp_f32 v43, v43\n\t" \
  "v_exp_f32 v44, v44\n\t" \
  "v_exp_f32 v45, v45\n\t" \
  "v_smfmac_f32_32x32x32_bf16 v[128:143], v[92:95], v[32:39], v144\n\t" \
  "buffer_load_dwordx4 v[152:155], v2, s[28:31], s39 offen offset:1024 sc1\n\t" \
  "v_exp_f32 v46, v46\n\t" \
  "v_exp_f32 v47, v47\n\t" \
    \
  "v_exp_f32 v48, v48\n\t" \
  "v_exp_f32 v49, v49\n\t" \
  "v_smfmac_f32_32x32x32_bf16 v[128:143], v[88:91], v[40:47], v144\n\t" \
  "s_mov_b64 exec, s[34:35]\n\t" \
  "ds_read_b128 v[88:91], v110 offset:37760\n\t" \
  "ds_read_b128 v[92:95], v110 offset:38400\n\t" \
  "s_mov_b64 exec, -1\n\t" \
  "v_exp_f32 v50, v50\n\t" \
  "v_exp_f32 v51, v51\n\t" \
  "v_exp_f32 v52, v52\n\t" \
  "v_exp_f32 v53, v53\n\t" \
  "v_mfma_f32_32x32x4_2b_bf16 v[16:47], v[86:87], v[108:109], 4.0 cbsz:1 abid:0\n\t" \
  "v_exp_f32 v54, v54\n\t" \
  "v_exp_f32 v55, v55\n\t" \
  "v_exp_f32 v56, v56\n\t" \
  "v_exp_f32 v57, v57\n\t" \
  "v_exp_f32 v58, v58\n\t" \
  "v_exp_f32 v59, v59\n\t" \
  "v_exp_f32 v60, v60\n\t" \
  "v_exp_f32 v61, v61\n\t" \
  "v_smfmac_f32_32x32x32_bf16 v[112:127], v[96:99], v[48:55], v144\n\t" \
  "s_waitcnt lgkmcnt(0)\n\t" \
  "v_exp_f32 v62, v62\n\t" \
  "v_exp_f32 v63, v63\n\t" \
  "v_exp_f32 v64, v64\n\t" \
  "v_exp_f32 v65, v65\n\t" \
  "v_smfmac_f32_32x32x32_bf16 v[112:127], v[88:91], v[56:63], v144\n\t" \
  "buffer_load_dwordx4 v[156:159], v2, s[28:31], s39 offen offset:2048 sc1\n\t" \
  "v_exp_f32 v66, v66\n\t" \
  "v_exp_f32 v67, v67\n\t" \
  "v_exp_f32 v68, v68\n\t" \
  "v_exp_f32 v69, v69\n\t" \
  "v_mfma_f32_32x32x16_bf16 v[16:31], v[80:83], v[100:103], v[16:31]\n\t" \
  "v_exp_f32 v70, v70\n\t" \
  "v_exp_f32 v71, v71\n\t" \
  "v_exp_f32 v72, v72\n\t" \
  "v_exp_f32 v73, v73\n\t" \
  "v_mfma_f32_32x32x16_bf16 v[32:47], v[80:83], v[104:107], v[32:47]\n\t" \
  "ds_read_b128 v[80:83], v110 offset:36096\n\t" \
  "v_exp_f32 v74, v74\n\t" \
  "v_exp_f32 v75, v75\n\t" \
  "v_exp_f32 v76, v76\n\t" \
  "v_exp_f32 v77, v77\n\t" \
  "v_smfmac_f32_32x32x32_bf16 v[128:143], v[96:99], v[64:71], v144\n\t" \
  "buffer_load_dwordx4 v[160:163], v2, s[28:31], s39 offen offset:3072 sc1\n\t" \
  "v_exp_f32 v78, v78\n\t" \
  "v_exp_f32 v79, v79\n\t" \
    \
  "v_exp_f32 v16, v16\n\t" \
  "v_exp_f32 v17, v17\n\t" \
  "v_smfmac_f32_32x32x32_bf16 v[128:143], v[88:91], v[72:79], v144\n\t" \
  "s_mov_b64 exec, s[34:35]\n\t" \
  "ds_read_b128 v[88:91], v110 offset:39040\n\t" \
  "ds_read_b128 v[96:99], v110 offset:39680\n\t" \
  "s_mov_b64 exec, -1\n\t" \
  "v_exp_f32 v18, v18\n\t" \
  "v_exp_f32 v19, v19\n\t" \
  "v_exp_f32 v20, v20\n\t" \
  "v_exp_f32 v21, v21\n\t" \
  "v_mfma_f32_32x32x4_2b_bf16 v[48:79], v[86:87], v[108:109], 4.0 cbsz:1 abid:1\n\t" \
  "ds_read2st64_b64 v[84:87], v111 offset0:84 offset1:85\n\t" \
  "v_exp_f32 v22, v22\n\t" \
  "v_exp_f32 v23, v23\n\t" \
  "v_exp_f32 v24, v24\n\t" \
  "v_exp_f32 v25, v25\n\t" \
  "v_exp_f32 v26, v26\n\t" \
  "v_exp_f32 v27, v27\n\t" \
  "v_exp_f32 v28, v28\n\t" \
  "v_exp_f32 v29, v29\n\t" \
  "v_smfmac_f32_32x32x32_bf16 v[112:127], v[92:95], v[16:23], v144\n\t" \
  "s_waitcnt lgkmcnt(0)\n\t" \
  "v_exp_f32 v30, v30\n\t" \
  "v_exp_f32 v31, v31\n\t" \
  "v_exp_f32 v32, v32\n\t" \
  "v_exp_f32 v33, v33\n\t" \
  "v_smfmac_f32_32x32x32_bf16 v[112:127], v[88:91], v[24:31], v144\n\t" \
  "s_add_u32 s39, s43, 4096\n\t" \
  "buffer_load_dwordx4 v[164:167], v2, s[28:31], s39 offen offset:0 sc1\n\t" \
  "v_exp_f32 v34, v34\n\t" \
  "v_exp_f32 v35, v35\n\t" \
  "v_exp_f32 v36, v36\n\t" \
  "v_exp_f32 v37, v37\n\t" \
  "v_mfma_f32_32x32x16_bf16 v[48:63], v[80:83], v[100:103], v[48:63]\n\t" \
  "v_exp_f32 v38, v38\n\t" \
  "v_exp_f32 v39, v39\n\t" \
  "v_exp_f32 v40, v40\n\t" \
  "v_exp_f32 v41, v41\n\t" \
  "v_mfma_f32_32x32x16_bf16 v[64:79], v[80:83], v[104:107], v[64:79]\n\t" \
  "ds_read_b128 v[80:83], v110 offset:40960\n\t" \
  "v_exp_f32 v42, v42\n\t" \
  "v_exp_f32 v43, v43\n\t" \
  "v_exp_f32 v44, v44\n\t" \
  "v_exp_f32 v45, v45\n\t" \
  "v_smfmac_f32_32x32x32_bf16 v[128:143], v[92:95], v[32:39], v144\n\t" \
  "buffer_load_dwordx4 v[168:171], v2, s[28:31], s39 offen offset:1024 sc1\n\t" \
  "v_exp_f32 v46, v46\n\t" \
  "v_exp_f32 v47, v47\n\t" \
    \
  "v_exp_f32 v48, v48\n\t" \
  "v_exp_f32 v49, v49\n\t" \
  "v_smfmac_f32_32x32x32_bf16 v[128:143], v[88:91], v[40:47], v144\n\t" \
  "s_mov_b64 exec, s[34:35]\n\t" \
  "ds_read_b128 v[88:91], v110 offset:40320\n\t" \
  "ds_read_b128 v[92:95], v110 offset:44032\n\t" \
  "s_mov_b64 exec, -1\n\t" \
  "v_exp_f32 v50, v50\n\t" \
  "v_exp_f32 v51, v51\n\t" \
  "v_exp_f32 v52, v52\n\t" \
  "v_exp_f32 v53, v53\n\t" \
  "v_mfma_f32_32x32x4_2b_bf16 v[16:47], v[84:85], v[108:109], 4.0 cbsz:1 abid:0\n\t" \
  "v_exp_f32 v54, v54\n\t" \
  "v_exp_f32 v55, v55\n\t" \
  "v_exp_f32 v56, v56\n\t" \
  "v_exp_f32 v57, v57\n\t" \
  "v_exp_f32 v58, v58\n\t" \
  "v_exp_f32 v59, v59\n\t" \
  "v_exp_f32 v60, v60\n\t" \
  "v_exp_f32 v61, v61\n\t" \
  "v_smfmac_f32_32x32x32_bf16 v[112:127], v[96:99], v[48:55], v144\n\t" \
  "s_waitcnt lgkmcnt(0)\n\t" \
  "v_exp_f32 v62, v62\n\t" \
  "v_exp_f32 v63, v63\n\t" \
  "v_exp_f32 v64, v64\n\t" \
  "v_exp_f32 v65, v65\n\t" \
  "v_smfmac_f32_32x32x32_bf16 v[112:127], v[88:91], v[56:63], v144\n\t" \
  "buffer_load_dwordx4 v[172:175], v2, s[28:31], s39 offen offset:2048 sc1\n\t" \
  "v_exp_f32 v66, v66\n\t" \
  "v_exp_f32 v67, v67\n\t" \
  "v_exp_f32 v68, v68\n\t" \
  "v_exp_f32 v69, v69\n\t" \
  "v_mfma_f32_32x32x16_bf16 v[16:31], v[80:83], v[100:103], v[16:31]\n\t" \
  "v_exp_f32 v70, v70\n\t" \
  "v_exp_f32 v71, v71\n\t" \
  "v_exp_f32 v72, v72\n\t" \
  "v_exp_f32 v73, v73\n\t" \
  "v_mfma_f32_32x32x16_bf16 v[32:47], v[80:83], v[104:107], v[32:47]\n\t" \
  "ds_read_b128 v[80:83], v110 offset:41984\n\t" \
  "v_exp_f32 v74, v74\n\t" \
  "v_exp_f32 v75, v75\n\t" \
  "v_exp_f32 v76, v76\n\t" \
  "v_exp_f32 v77, v77\n\t" \
  "v_smfmac_f32_32x32x32_bf16 v[128:143], v[96:99], v[64:71], v144\n\t" \
  "buffer_load_dwordx4 v[176:179], v2, s[28:31], s39 offen offset:3072 sc1\n\t" \
  "v_exp_f32 v78, v78\n\t" \
  "v_exp_f32 v79, v79\n\t" \
    \
  "v_exp_f32 v16, v16\n\t" \
  "v_exp_f32 v17, v17\n\t" \
  "v_smfmac_f32_32x32x32_bf16 v[128:143], v[88:91], v[72:79], v144\n\t" \
  "s_mov_b64 exec, s[34:35]\n\t" \
  "ds_read_b128 v[88:91], v110 offset:44672\n\t" \
  "ds_read_b128 v[96:99], v110 offset:47360\n\t" \
  "s_mov_b64 exec, -1\n\t" \
  "v_exp_f32 v18, v18\n\t" \
  "v_exp_f32 v19, v19\n\t" \
  "v_exp_f32 v20, v20\n\t" \
  "v_exp_f32 v21, v21\n\t" \
  "v_mfma_f32_32x32x4_2b_bf16 v[48:79], v[84:85], v[108:109], 4.0 cbsz:1 abid:1\n\t" \
  "v_exp_f32 v22, v22\n\t" \
  "v_exp_f32 v23, v23\n\t" \
  "v_exp_f32 v24, v24\n\t" \
  "v_exp_f32 v25, v25\n\t" \
  "v_exp_f32 v26, v26\n\t" \
  "v_exp_f32 v27, v27\n\t" \
  "v_exp_f32 v28, v28\n\t" \
  "v_exp_f32 v29, v29\n\t" \
  "v_smfmac_f32_32x32x32_bf16 v[112:127], v[92:95], v[16:23], v144\n\t" \
  "s_waitcnt lgkmcnt(0)\n\t" \
  "v_exp_f32 v30, v30\n\t" \
  "v_exp_f32 v31, v31\n\t" \
  "v_exp_f32 v32, v32\n\t" \
  "v_exp_f32 v33, v33\n\t" \
  "v_smfmac_f32_32x32x32_bf16 v[112:127], v[88:91], v[24:31], v144\n\t" \
  "s_add_u32 s39, s43, 8192\n\t" \
  "buffer_load_dwordx4 v[180:183], v2, s[28:31], s39 offen offset:0 sc1\n\t" \
  "v_exp_f32 v34, v34\n\t" \
  "v_exp_f32 v35, v35\n\t" \
  "v_exp_f32 v36, v36\n\t" \
  "v_exp_f32 v37, v37\n\t" \
  "v_mfma_f32_32x32x16_bf16 v[48:63], v[80:83], v[100:103], v[48:63]\n\t" \
  "v_exp_f32 v38, v38\n\t" \
  "v_exp_f32 v39, v39\n\t" \
  "v_exp_f32 v40, v40\n\t" \
  "v_exp_f32 v41, v41\n\t" \
  "v_mfma_f32_32x32x16_bf16 v[64:79], v[80:83], v[104:107], v[64:79]\n\t" \
  "ds_read_b128 v[80:83], v110 offset:45312\n\t" \
  "v_exp_f32 v42, v42\n\t" \
  "v_exp_f32 v43, v43\n\t" \
  "v_exp_f32 v44, v44\n\t" \
  "v_exp_f32 v45, v45\n\t" \
  "v_smfmac_f32_32x32x32_bf16 v[128:143], v[92:95], v[32:39], v144\n\t" \
  "buffer_load_dwordx4 v[184:187], v2, s[28:31], s39 offen offset:1024 sc1\n\t" \
  "v_exp_f32 v46, v46\n\t" \
  "v_exp_f32 v47, v47\n\t" \
    \
  "v_exp_f32 v48, v48\n\t" \
  "v_exp_f32 v49, v49\n\t" \
  "v_smfmac_f32_32x32x32_bf16 v[128:143], v[88:91], v[40:47], v144\n\t" \
  "s_mov_b64 exec, s[34:35]\n\t" \
  "ds_read_b128 v[88:91], v110 offset:48000\n\t" \
  "ds_read_b128 v[92:95], v110 offset:48640\n\t" \
  "s_mov_b64 exec, -1\n\t" \
  "v_exp_f32 v50, v50\n\t" \
  "v_exp_f32 v51, v51\n\t" \
  "v_exp_f32 v52, v52\n\t" \
  "v_exp_f32 v53, v53\n\t" \
  "v_mfma_f32_32x32x4_2b_bf16 v[16:47], v[86:87], v[108:109], 4.0 cbsz:1 abid:0\n\t" \
  "v_exp_f32 v54, v54\n\t" \
  "v_exp_f32 v55, v55\n\t" \
  "v_exp_f32 v56, v56\n\t" \
  "v_exp_f32 v57, v57\n\t" \
  "v_exp_f32 v58, v58\n\t" \
  "v_exp_f32 v59, v59\n\t" \
  "v_exp_f32 v60, v60\n\t" \
  "v_exp_f32 v61, v61\n\t" \
  "v_smfmac_f32_32x32x32_bf16 v[112:127], v[96:99], v[48:55], v144\n\t" \
  "s_waitcnt lgkmcnt(0)\n\t" \
  "v_exp_f32 v62, v62\n\t" \
  "v_exp_f32 v63, v63\n\t" \
  "v_exp_f32 v64, v64\n\t" \
  "v_exp_f32 v65, v65\n\t" \
  "v_smfmac_f32_32x32x32_bf16 v[112:127], v[88:91], v[56:63], v144\n\t" \
  "buffer_load_dwordx4 v[188:191], v2, s[28:31], s39 offen offset:2048 sc1\n\t" \
  "v_exp_f32 v66, v66\n\t" \
  "v_exp_f32 v67, v67\n\t" \
  "v_exp_f32 v68, v68\n\t" \
  "v_exp_f32 v69, v69\n\t" \
  "v_mfma_f32_32x32x16_bf16 v[16:31], v[80:83], v[100:103], v[16:31]\n\t" \
  "v_exp_f32 v70, v70\n\t" \
  "v_exp_f32 v71, v71\n\t" \
  "v_exp_f32 v72, v72\n\t" \
  "v_exp_f32 v73, v73\n\t" \
  "v_mfma_f32_32x32x16_bf16 v[32:47], v[80:83], v[104:107], v[32:47]\n\t" \
  "ds_read_b128 v[80:83], v110 offset:46336\n\t" \
  "v_exp_f32 v74, v74\n\t" \
  "v_exp_f32 v75, v75\n\t" \
  "v_exp_f32 v76, v76\n\t" \
  "v_exp_f32 v77, v77\n\t" \
  "v_smfmac_f32_32x32x32_bf16 v[128:143], v[96:99], v[64:71], v144\n\t" \
  "buffer_load_dwordx4 v[192:195], v2, s[28:31], s39 offen offset:3072 sc1\n\t" \
  "v_exp_f32 v78, v78\n\t" \
  "v_exp_f32 v79, v79\n\t" \
    \
  "v_exp_f32 v16, v16\n\t" \
  "v_exp_f32 v17, v17\n\t" \
  "v_smfmac_f32_32x32x32_bf16 v[128:143], v[88:91], v[72:79], v144\n\t" \
  "s_mov_b64 exec, s[34:35]\n\t" \
  "ds_read_b128 v[88:91], v110 offset:49280\n\t" \
  "ds_read_b128 v[96:99], v110 offset:49920\n\t" \
  "s_mov_b64 exec, -1\n\t" \
  "v_exp_f32 v18, v18\n\t" \
  "v_exp_f32 v19, v19\n\t" \
  "v_exp_f32 v20, v20\n\t" \
  "v_exp_f32 v21, v21\n\t" \
  "v_mfma_f32_32x32x4_2b_bf16 v[48:79], v[86:87], v[108:109], 4.0 cbsz:1 abid:1\n\t" \
  "ds_read2st64_b64 v[84:87], v111 offset0:104 offset1:105\n\t" \
  "v_exp_f32 v22, v22\n\t" \
  "v_exp_f32 v23, v23\n\t" \
  "v_exp_f32 v24, v24\n\t" \
  "v_exp_f32 v25, v25\n\t" \
  "v_exp_f32 v26, v26\n\t" \
  "v_exp_f32 v27, v27\n\t" \
  "v_exp_f32 v28, v28\n\t" \
  "v_exp_f32 v29, v29\n\t" \
  "v_smfmac_f32_32x32x32_bf16 v[112:127], v[92:95], v[16:23], v144\n\t" \
  "s_waitcnt lgkmcnt(0)\n\t" \
  "v_exp_f32 v30, v30\n\t" \
  "v_exp_f32 v31, v31\n\t" \
  "v_exp_f32 v32, v32\n\t" \
  "v_exp_f32 v33, v33\n\t" \
  "v_smfmac_f32_32x32x32_bf16 v[112:127], v[88:91], v[24:31], v144\n\t" \
  "s_add_u32 s39, s43, 12288\n\t" \
  "buffer_load_dwordx4 v[196:199], v2, s[28:31], s39 offen offset:0 sc1\n\t" \
  "v_exp_f32 v34, v34\n\t" \
  "v_exp_f32 v35, v35\n\t" \
  "v_exp_f32 v36, v36\n\t" \
  "v_exp_f32 v37, v37\n\t" \
  "v_mfma_f32_32x32x16_bf16 v[48:63], v[80:83], v[100:103], v[48:63]\n\t" \
  "v_exp_f32 v38, v38\n\t" \
  "v_exp_f32 v39, v39\n\t" \
  "v_exp_f32 v40, v40\n\t" \
  "v_exp_f32 v41, v41\n\t" \
  "v_mfma_f32_32x32x16_bf16 v[64:79], v[80:83], v[104:107], v[64:79]\n\t" \
  "ds_read_b128 v[80:83], v110 offset:51200\n\t" \
  "v_exp_f32 v42, v42\n\t" \
  "v_exp_f32 v43, v43\n\t" \
  "v_exp_f32 v44, v44\n\t" \
  "v_exp_f32 v45, v45\n\t" \
  "v_smfmac_f32_32x32x32_bf16 v[128:143], v[92:95], v[32:39], v144\n\t" \
  "buffer_load_dwordx4 v[200:203], v2, s[28:31], s39 offen offset:1024 sc1\n\t" \
  "v_exp_f32 v46, v46\n\t" \
  "v_exp_f32 v47, v47\n\t" \
    \
  "v_exp_f32 v48, v48\n\t" \
  "v_exp_f32 v49, v49\n\t" \
  "v_smfmac_f32_32x32x32_bf16 v[128:143], v[88:91], v[40:47], v144\n\t" \
  "s_mov_b64 exec, s[34:35]\n\t" \
  "ds_read_b128 v[88:91], v110 offset:50560\n\t" \
  "ds_read_b128 v[92:95], v110 offset:54272\n\t" \
  "s_mov_b64 exec, -1\n\t" \
  "v_exp_f32 v50, v50\n\t" \
  "v_exp_f32 v51, v51\n\t" \
  "v_exp_f32 v52, v52\n\t" \
  "v_exp_f32 v53, v53\n\t" \
  "v_mfma_f32_32x32x4_2b_bf16 v[16:47], v[84:85], v[108:109], 4.0 cbsz:1 abid:0\n\t" \
  "v_exp_f32 v54, v54\n\t" \
  "v_exp_f32 v55, v55\n\t" \
  "v_exp_f32 v56, v56\n\t" \
  "v_exp_f32 v57, v57\n\t" \
  "v_exp_f32 v58, v58\n\t" \
  "v_exp_f32 v59, v59\n\t" \
  "v_exp_f32 v60, v60\n\t" \
  "v_exp_f32 v61, v61\n\t" \
  "v_smfmac_f32_32x32x32_bf16 v[112:127], v[96:99], v[48:55], v144\n\t" \
  "s_waitcnt lgkmcnt(0)\n\t" \
  "v_exp_f32 v62, v62\n\t" \
  "v_exp_f32 v63, v63\n\t" \
  "v_exp_f32 v64, v64\n\t" \
  "v_exp_f32 v65, v65\n\t" \
  "v_smfmac_f32_32x32x32_bf16 v[112:127], v[88:91], v[56:63], v144\n\t" \
  "buffer_load_dwordx4 v[204:207], v2, s[28:31], s39 offen offset:2048 sc1\n\t" \
  "v_exp_f32 v66, v66\n\t" \
  "v_exp_f32 v67, v67\n\t" \
  "v_exp_f32 v68, v68\n\t" \
  "v_exp_f32 v69, v69\n\t" \
  "v_mfma_f32_32x32x16_bf16 v[16:31], v[80:83], v[100:103], v[16:31]\n\t" \
  "v_exp_f32 v70, v70\n\t" \
  "v_exp_f32 v71, v71\n\t" \
  "v_exp_f32 v72, v72\n\t" \
  "v_exp_f32 v73, v73\n\t" \
  "v_mfma_f32_32x32x16_bf16 v[32:47], v[80:83], v[104:107], v[32:47]\n\t" \
  "ds_read_b128 v[80:83], v110 offset:52224\n\t" \
  "v_exp_f32 v74, v74\n\t" \
  "v_exp_f32 v75, v75\n\t" \
  "v_exp_f32 v76, v76\n\t" \
  "v_exp_f32 v77, v77\n\t" \
  "v_smfmac_f32_32x32x32_bf16 v[128:143], v[96:99], v[64:71], v144\n\t" \
  "buffer_load_dwordx4 v[208:211], v2, s[28:31], s39 offen offset:3072 sc1\n\t" \
  "v_exp_f32 v78, v78\n\t" \
  "v_exp_f32 v79, v79\n\t" \
  "v_add_u32 v110, 51200, v110\n\t" \
  "v_add_u32 v111, 51200, v111\n\t" \
    \
  "v_exp_f32 v16, v16\n\t" \
  "v_exp_f32 v17, v17\n\t" \
  "v_smfmac_f32_32x32x32_bf16 v[128:143], v[88:91], v[72:79], v144\n\t" \
  "s_mov_b64 exec, s[34:35]\n\t" \
  "ds_read_b128 v[88:91], v110 offset:3712\n\t" \
  "ds_read_b128 v[96:99], v110 offset:6400\n\t" \
  "s_mov_b64 exec, -1\n\t" \
  "v_exp_f32 v18, v18\n\t" \
  "v_exp_f32 v19, v19\n\t" \
  "v_exp_f32 v20, v20\n\t" \
  "v_exp_f32 v21, v21\n\t" \
  "v_mfma_f32_32x32x4_2b_bf16 v[48:79], v[84:85], v[108:109], 4.0 cbsz:1 abid:1\n\t" \
  "v_exp_f32 v22, v22\n\t" \
  "v_exp_f32 v23, v23\n\t" \
  "v_exp_f32 v24, v24\n\t" \
  "v_exp_f32 v25, v25\n\t" \
  "v_exp_f32 v26, v26\n\t" \
  "v_exp_f32 v27, v27\n\t" \
  "v_exp_f32 v28, v28\n\t" \
  "v_exp_f32 v29, v29\n\t" \
  "v_smfmac_f32_32x32x32_bf16 v[112:127], v[92:95], v[16:23], v144\n\t" \
  "s_waitcnt lgkmcnt(0)\n\t" \
  "v_exp_f32 v30, v30\n\t" \
  "v_exp_f32 v31, v31\n\t" \
  "v_exp_f32 v32, v32\n\t" \
  "v_exp_f32 v33, v33\n\t" \
  "v_smfmac_f32_32x32x32_bf16 v[112:127], v[88:91], v[24:31], v144\n\t" \
  "s_add_u32 s39, s43, 16384\n\t" \
  "buffer_load_dwordx4 v[212:215], v2, s[28:31], s39 offen offset:0 sc1\n\t" \
  "v_exp_f32 v34, v34\n\t" \
  "v_exp_f32 v35, v35\n\t" \
  "v_exp_f32 v36, v36\n\t" \
  "v_exp_f32 v37, v37\n\t" \
  "v_mfma_f32_32x32x16_bf16 v[48:63], v[80:83], v[100:103], v[48:63]\n\t" \
  "v_exp_f32 v38, v38\n\t" \
  "v_exp_f32 v39, v39\n\t" \
  "v_exp_f32 v40, v40\n\t" \
  "v_exp_f32 v41, v41\n\t" \
  "v_mfma_f32_32x32x16_bf16 v[64:79], v[80:83], v[104:107], v[64:79]\n\t" \
  "ds_read_b128 v[80:83], v110 offset:4352\n\t" \
  "v_exp_f32 v42, v42\n\t" \
  "v_exp_f32 v43, v43\n\t" \
  "v_exp_f32 v44, v44\n\t" \
  "v_exp_f32 v45, v45\n\t" \
  "v_smfmac_f32_32x32x32_bf16 v[128:143], v[92:95], v[32:39], v144\n\t" \
  "buffer_load_dwordx4 v[216:219], v2, s[28:31], s39 offen offset:1024 sc1\n\t" \
  "v_exp_f32 v46, v46\n\t" \
  "v_exp_f32 v47, v47\n\t" \
    \
  "v_exp_f32 v48, v48\n\t" \
  "v_exp_f32 v49, v49\n\t" \
  "v_smfmac_f32_32x32x32_bf16 v[128:143], v[88:91], v[40:47], v144\n\t" \
  "s_mov_b64 exec, s[34:35]\n\t" \
  "ds_read_b128 v[88:91], v110 offset:7040\n\t" \
  "ds_read_b128 v[92:95], v110 offset:7680\n\t" \
  "s_mov_b64 exec, -1\n\t" \
  "v_exp_f32 v50, v50\n\t" \
  "v_exp_f32 v51, v51\n\t" \
  "v_exp_f32 v52, v52\n\t" \
  "v_exp_f32 v53, v53\n\t" \
  "v_mfma_f32_32x32x4_2b_bf16 v[16:47], v[86:87], v[108:109], 4.0 cbsz:1 abid:0\n\t" \
  "v_exp_f32 v54, v54\n\t" \
  "v_exp_f32 v55, v55\n\t" \
  "v_exp_f32 v56, v56\n\t" \
  "v_exp_f32 v57, v57\n\t" \
  "v_exp_f32 v58, v58\n\t" \
  "v_exp_f32 v59, v59\n\t" \
  "v_exp_f32 v60, v60\n\t" \
  "v_exp_f32 v61, v61\n\t" \
  "v_smfmac_f32_32x32x32_bf16 v[112:127], v[96:99], v[48:55], v144\n\t" \
  "s_waitcnt lgkmcnt(0)\n\t" \
  "v_exp_f32 v62, v62\n\t" \
  "v_exp_f32 v63, v63\n\t" \
  "v_exp_f32 v64, v64\n\t" \
  "v_exp_f32 v65, v65\n\t" \
  "v_smfmac_f32_32x32x32_bf16 v[112:127], v[88:91], v[56:63], v144\n\t" \
  "buffer_load_dwordx4 v[220:223], v2, s[28:31], s39 offen offset:2048 sc1\n\t" \
  "v_exp_f32 v66, v66\n\t" \
  "v_exp_f32 v67, v67\n\t" \
  "v_exp_f32 v68, v68\n\t" \
  "v_exp_f32 v69, v69\n\t" \
  "v_mfma_f32_32x32x16_bf16 v[16:31], v[80:83], v[100:103], v[16:31]\n\t" \
  "v_exp_f32 v70, v70\n\t" \
  "v_exp_f32 v71, v71\n\t" \
  "v_exp_f32 v72, v72\n\t" \
  "v_exp_f32 v73, v73\n\t" \
  "v_mfma_f32_32x32x16_bf16 v[32:47], v[80:83], v[104:107], v[32:47]\n\t" \
  "ds_read_b128 v[80:83], v110 offset:5376\n\t" \
  "v_exp_f32 v74, v74\n\t" \
  "v_exp_f32 v75, v75\n\t" \
  "v_exp_f32 v76, v76\n\t" \
  "v_exp_f32 v77, v77\n\t" \
  "v_smfmac_f32_32x32x32_bf16 v[128:143], v[96:99], v[64:71], v144\n\t" \
  "buffer_load_dwordx4 v[224:227], v2, s[28:31], s39 offen offset:3072 sc1\n\t" \
  "v_exp_f32 v78, v78\n\t" \
  "v_exp_f32 v79, v79\n\t" \
    \
  "v_exp_f32 v16, v16\n\t" \
  "v_exp_f32 v17, v17\n\t" \
  "v_smfmac_f32_32x32x32_bf16 v[128:143], v[88:91], v[72:79], v144\n\t" \
  "s_mov_b64 exec, s[34:35]\n\t" \
  "ds_read_b128 v[88:91], v110 offset:8320\n\t" \
  "ds_read_b128 v[96:99], v110 offset:8960\n\t" \
  "s_mov_b64 exec, -1\n\t" \
  "v_exp_f32 v18, v18\n\t" \
  "v_exp_f32 v19, v19\n\t" \
  "v_exp_f32 v20, v20\n\t" \
  "v_exp_f32 v21, v21\n\t" \
  "v_mfma_f32_32x32x4_2b_bf16 v[48:79], v[86:87], v[108:109], 4.0 cbsz:1 abid:1\n\t" \
  "ds_read2st64_b64 v[84:87], v111 offset0:24 offset1:25\n\t" \
  "v_exp_f32 v22, v22\n\t" \
  "v_exp_f32 v23, v23\n\t" \
  "v_exp_f32 v24, v24\n\t" \
  "v_exp_f32 v25, v25\n\t" \
  "v_exp_f32 v26, v26\n\t" \
  "v_exp_f32 v27, v27\n\t" \
  "v_exp_f32 v28, v28\n\t" \
  "v_exp_f32 v29, v29\n\t" \
  "v_smfmac_f32_32x32x32_bf16 v[112:127], v[92:95], v[16:23], v144\n\t" \
  "s_waitcnt lgkmcnt(0)\n\t" \
  "v_exp_f32 v30, v30\n\t" \
  "v_exp_f32 v31, v31\n\t" \
  "v_exp_f32 v32, v32\n\t" \
  "v_exp_f32 v33, v33\n\t" \
  "v_smfmac_f32_32x32x32_bf16 v[112:127], v[88:91], v[24:31], v144\n\t" \
  "s_add_u32 s39, s43, 20480\n\t" \
  "buffer_load_dwordx4 v[228:231], v2, s[28:31], s39 offen offset:0 sc1\n\t" \
  "v_exp_f32 v34, v34\n\t" \
  "v_exp_f32 v35, v35\n\t" \
  "v_exp_f32 v36, v36\n\t" \
  "v_exp_f32 v37, v37\n\t" \
  "v_mfma_f32_32x32x16_bf16 v[48:63], v[80:83], v[100:103], v[48:63]\n\t" \
  "v_exp_f32 v38, v38\n\t" \
  "v_exp_f32 v39, v39\n\t" \
  "v_exp_f32 v40, v40\n\t" \
  "v_exp_f32 v41, v41\n\t" \
  "v_mfma_f32_32x32x16_bf16 v[64:79], v[80:83], v[104:107], v[64:79]\n\t" \
  "ds_read_b128 v[80:83], v110 offset:10240\n\t" \
  "v_exp_f32 v42, v42\n\t" \
  "v_exp_f32 v43, v43\n\t" \
  "v_exp_f32 v44, v44\n\t" \
  "v_exp_f32 v45, v45\n\t" \
  "v_smfmac_f32_32x32x32_bf16 v[128:143], v[92:95], v[32:39], v144\n\t" \
  "buffer_load_dwordx4 v[232:235], v2, s[28:31], s39 offen offset:1024 sc1\n\t" \
  "v_exp_f32 v46, v46\n\t" \
  "v_exp_f32 v47, v47\n\t" \
    \
  "v_exp_f32 v48, v48\n\t" \
  "v_exp_f32 v49, v49\n\t" \
  "v_smfmac_f32_32x32x32_bf16 v[128:143], v[88:91], v[40:47], v144\n\t" \
  "s_mov_b64 exec, s[34:35]\n\t" \
  "ds_read_b128 v[88:91], v110 offset:9600\n\t" \
  "ds_read_b128 v[92:95], v110 offset:13312\n\t" \
  "s_mov_b64 exec, -1\n\t" \
  "v_exp_f32 v50, v50\n\t" \
  "v_exp_f32 v51, v51\n\t" \
  "v_exp_f32 v52, v52\n\t" \
  "v_exp_f32 v53, v53\n\t" \
  "v_mfma_f32_32x32x4_2b_bf16 v[16:47], v[84:85], v[108:109], 4.0 cbsz:1 abid:0\n\t" \
  "v_exp_f32 v54, v54\n\t" \
  "v_exp_f32 v55, v55\n\t" \
  "v_exp_f32 v56, v56\n\t" \
  "v_exp_f32 v57, v57\n\t" \
  "v_exp_f32 v58, v58\n\t" \
  "v_exp_f32 v59, v59\n\t" \
  "v_exp_f32 v60, v60\n\t" \
  "v_exp_f32 v61, v61\n\t" \
  "v_smfmac_f32_32x32x32_bf16 v[112:127], v[96:99], v[48:55], v144\n\t" \
  "s_waitcnt lgkmcnt(0)\n\t" \
  "v_exp_f32 v62, v62\n\t" \
  "v_exp_f32 v63, v63\n\t" \
  "v_exp_f32 v64, v64\n\t" \
  "v_exp_f32 v65, v65\n\t" \
  "v_smfmac_f32_32x32x32_bf16 v[112:127], v[88:91], v[56:63], v144\n\t" \
  "buffer_load_dwordx4 v[236:239], v2, s[28:31], s39 offen offset:2048 sc1\n\t" \
  "v_exp_f32 v66, v66\n\t" \
  "v_exp_f32 v67, v67\n\t" \
  "v_exp_f32 v68, v68\n\t" \
  "v_exp_f32 v69, v69\n\t" \
  "v_mfma_f32_32x32x16_bf16 v[16:31], v[80:83], v[100:103], v[16:31]\n\t" \
  "v_exp_f32 v70, v70\n\t" \
  "v_exp_f32 v71, v71\n\t" \
  "v_exp_f32 v72, v72\n\t" \
  "v_exp_f32 v73, v73\n\t" \
  "v_mfma_f32_32x32x16_bf16 v[32:47], v[80:83], v[104:107], v[32:47]\n\t" \
  "ds_read_b128 v[80:83], v110 offset:11264\n\t" \
  "v_exp_f32 v74, v74\n\t" \
  "v_exp_f32 v75, v75\n\t" \
  "v_exp_f32 v76, v76\n\t" \
  "v_exp_f32 v77, v77\n\t" \
  "v_smfmac_f32_32x32x32_bf16 v[128:143], v[96:99], v[64:71], v144\n\t" \
  "buffer_load_dwordx4 v[240:243], v2, s[28:31], s39 offen offset:3072 sc1\n\t" \
  "v_exp_f32 v78, v78\n\t" \
  "v_exp_f32 v79, v79\n\t" \
    \
  "v_exp_f32 v16, v16\n\t" \
  "v_exp_f32 v17, v17\n\t" \
  "v_smfmac_f32_32x32x32_bf16 v[128:143], v[88:91], v[72:79], v144\n\t" \
  "s_mov_b64 exec, s[34:35]\n\t" \
  "ds_read_b128 v[88:91], v110 offset:13952\n\t" \
  "ds_read_b128 v[96:99], v110 offset:16640\n\t" \
  "s_mov_b64 exec, -1\n\t" \
  "v_exp_f32 v18, v18\n\t" \
  "v_exp_f32 v19, v19\n\t" \
  "v_exp_f32 v20, v20\n\t" \
  "v_exp_f32 v21, v21\n\t" \
  "v_mfma_f32_32x32x4_2b_bf16 v[48:79], v[84:85], v[108:109], 4.0 cbsz:1 abid:1\n\t" \
  "v_exp_f32 v22, v22\n\t" \
  "v_exp_f32 v23, v23\n\t" \
  "v_exp_f32 v24, v24\n\t" \
  "v_exp_f32 v25, v25\n\t" \
  "v_exp_f32 v26, v26\n\t" \
  "v_exp_f32 v27, v27\n\t" \
  "v_exp_f32 v28, v28\n\t" \
  "v_exp_f32 v29, v29\n\t" \
  "v_smfmac_f32_32x32x32_bf16 v[112:127], v[92:95], v[16:23], v144\n\t" \
  "s_waitcnt lgkmcnt(0)\n\t" \
  "v_exp_f32 v30, v30\n\t" \
  "v_exp_f32 v31, v31\n\t" \
  "v_exp_f32 v32, v32\n\t" \
  "v_exp_f32 v33, v33\n\t" \
  "v_smfmac_f32_32x32x32_bf16 v[112:127], v[88:91], v[24:31], v144\n\t" \
  "s_add_u32 s39, s43, 24576\n\t" \
  "buffer_load_dwordx4 v[244:247], v2, s[28:31], s39 offen offset:0 sc1\n\t" \
  "v_exp_f32 v34, v34\n\t" \
  "v_exp_f32 v35, v35\n\t" \
  "v_exp_f32 v36, v36\n\t" \
  "v_exp_f32 v37, v37\n\t" \
  "v_mfma_f32_32x32x16_bf16 v[48:63], v[80:83], v[100:103], v[48:63]\n\t" \
  "v_exp_f32 v38, v38\n\t" \
  "v_exp_f32 v39, v39\n\t" \
  "v_exp_f32 v40, v40\n\t" \
  "v_exp_f32 v41, v41\n\t" \
  "v_mfma_f32_32x32x16_bf16 v[64:79], v[80:83], v[104:107], v[64:79]\n\t" \
  "ds_read_b128 v[80:83], v110 offset:14592\n\t" \
  "v_exp_f32 v42, v42\n\t" \
  "v_exp_f32 v43, v43\n\t" \
  "v_exp_f32 v44, v44\n\t" \
  "v_exp_f32 v45, v45\n\t" \
  "v_smfmac_f32_32x32x32_bf16 v[128:143], v[92:95], v[32:39], v144\n\t" \
  "buffer_load_dwordx4 v[248:251], v2, s[28:31], s39 offen offset:1024 sc1\n\t" \
  "v_exp_f32 v46, v46\n\t" \
  "v_exp_f32 v47, v47\n\t" \
    \
  "v_exp_f32 v48, v48\n\t" \
  "v_exp_f32 v49, v49\n\t" \
  "v_smfmac_f32_32x32x32_bf16 v[128:143], v[88:91], v[40:47], v144\n\t" \
  "s_mov_b64 exec, s[34:35]\n\t" \
  "ds_read_b128 v[88:91], v110 offset:17280\n\t" \
  "ds_read_b128 v[92:95], v110 offset:17920\n\t" \
  "s_mov_b64 exec, -1\n\t" \
  "v_exp_f32 v50, v50\n\t" \
  "v_exp_f32 v51, v51\n\t" \
  "v_exp_f32 v52, v52\n\t" \
  "v_exp_f32 v53, v53\n\t" \
  "v_mfma_f32_32x32x4_2b_bf16 v[16:47], v[86:87], v[108:109], 4.0 cbsz:1 abid:0\n\t" \
  "v_exp_f32 v54, v54\n\t" \
  "v_exp_f32 v55, v55\n\t" \
  "v_exp_f32 v56, v56\n\t" \
  "v_exp_f32 v57, v57\n\t" \
  "v_exp_f32 v58, v58\n\t" \
  "v_exp_f32 v59, v59\n\t" \
  "v_exp_f32 v60, v60\n\t" \
  "v_exp_f32 v61, v61\n\t" \
  "v_smfmac_f32_32x32x32_bf16 v[112:127], v[96:99], v[48:55], v144\n\t" \
  "s_waitcnt lgkmcnt(0)\n\t" \
  "v_exp_f32 v62, v62\n\t" \
  "v_exp_f32 v63, v63\n\t" \
  "v_exp_f32 v64, v64\n\t" \
  "v_exp_f32 v65, v65\n\t" \
  "v_smfmac_f32_32x32x32_bf16 v[112:127], v[88:91], v[56:63], v144\n\t" \
  "buffer_load_dwordx4 v[252:255], v2, s[28:31], s39 offen offset:2048 sc1\n\t" \
  "v_exp_f32 v66, v66\n\t" \
  "v_exp_f32 v67, v67\n\t" \
  "v_exp_f32 v68, v68\n\t" \
  "v_exp_f32 v69, v69\n\t" \
  "v_mfma_f32_32x32x16_bf16 v[16:31], v[80:83], v[100:103], v[16:31]\n\t" \
  "v_exp_f32 v70, v70\n\t" \
  "v_exp_f32 v71, v71\n\t" \
  "v_exp_f32 v72, v72\n\t" \
  "v_exp_f32 v73, v73\n\t" \
  "v_mfma_f32_32x32x16_bf16 v[32:47], v[80:83], v[104:107], v[32:47]\n\t" \
  "ds_read_b128 v[80:83], v110 offset:15616\n\t" \
  "s_cmp_eq_u32 s41, 0\n\t" \
  "s_cbranch_scc0 42f\n\t" \
  "s_setprio 0\n\t" \
  "42:\n\t" \
  "v_exp_f32 v74, v74\n\t" \
  "v_exp_f32 v75, v75\n\t" \
  "v_exp_f32 v76, v76\n\t" \
  "v_exp_f32 v77, v77\n\t" \
  "v_smfmac_f32_32x32x32_bf16 v[128:143], v[96:99], v[64:71], v144\n\t" \
  "s_add_u32 s39, s43, s44\n\t" \
  "buffer_load_dword v146, v146, s[28:31], s39 offen sc1\n\t" \
  "v_exp_f32 v78, v78\n\t" \
  "v_exp_f32 v79, v79\n\t" \
    \
  "v_exp_f32 v16, v16\n\t" \
  "v_exp_f32 v17, v17\n\t" \
  "v_smfmac_f32_32x32x32_bf16 v[128:143], v[88:91], v[72:79], v144\n\t" \
  "s_mov_b64 exec, s[34:35]\n\t" \
  "ds_read_b128 v[88:91], v110 offset:18560\n\t" \
  "ds_read_b128 v[96:99], v110 offset:19200\n\t" \
  "s_mov_b64 exec, -1\n\t" \
  "v_exp_f32 v18, v18\n\t" \
  "v_exp_f32 v19, v19\n\t" \
  "v_exp_f32 v20, v20\n\t" \
  "v_exp_f32 v21, v21\n\t" \
  "v_mfma_f32_32x32x4_2b_bf16 v[48:79], v[86:87], v[108:109], 4.0 cbsz:1 abid:1\n\t" \
  "ds_read2st64_b64 v[84:87], v111 offset0:44 offset1:45\n\t" \
  "v_exp_f32 v22, v22\n\t" \
  "v_exp_f32 v23, v23\n\t" \
  "v_exp_f32 v24, v24\n\t" \
  "v_exp_f32 v25, v25\n\t" \
  "v_exp_f32 v26, v26\n\t" \
  "v_exp_f32 v27, v27\n\t" \
  "v_exp_f32 v28, v28\n\t" \
  "v_exp_f32 v29, v29\n\t" \
  "v_smfmac_f32_32x32x32_bf16 v[112:127], v[92:95], v[16:23], v144\n\t" \
  "s_waitcnt lgkmcnt(0)\n\t" \
  "v_exp_f32 v30, v30\n\t" \
  "v_exp_f32 v31, v31\n\t" \
  "v_exp_f32 v32, v32\n\t" \
  "v_exp_f32 v33, v33\n\t" \
  "v_smfmac_f32_32x32x32_bf16 v[112:127], v[88:91], v[24:31], v144\n\t" \
  "v_exp_f32 v34, v34\n\t" \
  "v_exp_f32 v35, v35\n\t" \
  "v_exp_f32 v36, v36\n\t" \
  "v_exp_f32 v37, v37\n\t" \
  "v_mfma_f32_32x32x16_bf16 v[48:63], v[80:83], v[100:103], v[48:63]\n\t" \
  "v_exp_f32 v38, v38\n\t" \
  "v_exp_f32 v39, v39\n\t" \
  "v_exp_f32 v40, v40\n\t" \
  "v_exp_f32 v41, v41\n\t" \
  "v_mfma_f32_32x32x16_bf16 v[64:79], v[80:83], v[104:107], v[64:79]\n\t" \
  "ds_read_b128 v[80:83], v110 offset:20480\n\t" \
  "v_exp_f32 v42, v42\n\t" \
  "v_exp_f32 v43, v43\n\t" \
  "v_exp_f32 v44, v44\n\t" \
  "v_exp_f32 v45, v45\n\t" \
  "v_smfmac_f32_32x32x32_bf16 v[128:143], v[92:95], v[32:39], v144\n\t" \
  "v_exp_f32 v46, v46\n\t" \
  "v_exp_f32 v47, v47\n\t" \
    \
  "v_exp_f32 v48, v48\n\t" \
  "v_exp_f32 v49, v49\n\t" \
  "v_smfmac_f32_32x32x32_bf16 v[128:143], v[88:91], v[40:47], v144\n\t" \
  "s_mov_b64 exec, s[34:35]\n\t" \
  "ds_read_b128 v[88:91], v110 offset:19840\n\t" \
  "ds_read_b128 v[92:95], v110 offset:23552\n\t" \
  "s_mov_b64 exec, -1\n\t" \
  "v_exp_f32 v50, v50\n\t" \
  "v_exp_f32 v51, v51\n\t" \
  "v_exp_f32 v52, v52\n\t" \
  "v_exp_f32 v53, v53\n\t" \
  "v_mfma_f32_32x32x4_2b_bf16 v[16:47], v[84:85], v[108:109], 4.0 cbsz:1 abid:0\n\t" \
  "v_exp_f32 v54, v54\n\t" \
  "v_exp_f32 v55, v55\n\t" \
  "v_exp_f32 v56, v56\n\t" \
  "v_exp_f32 v57, v57\n\t" \
  "v_exp_f32 v58, v58\n\t" \
  "v_exp_f32 v59, v59\n\t" \
  "v_exp_f32 v60, v60\n\t" \
  "v_exp_f32 v61, v61\n\t" \
  "v_smfmac_f32_32x32x32_bf16 v[112:127], v[96:99], v[48:55], v144\n\t" \
  "s_waitcnt lgkmcnt(0)\n\t" \
  "v_exp_f32 v62, v62\n\t" \
  "v_exp_f32 v63, v63\n\t" \
  "v_exp_f32 v64, v64\n\t" \
  "v_exp_f32 v65, v65\n\t" \
  "v_smfmac_f32_32x32x32_bf16 v[112:127], v[88:91], v[56:63], v144\n\t" \
  "v_exp_f32 v66, v66\n\t" \
  "v_exp_f32 v67, v67\n\t" \
  "v_exp_f32 v68, v68\n\t" \
  "v_exp_f32 v69, v69\n\t" \
  "v_mfma_f32_32x32x16_bf16 v[16:31], v[80:83], v[100:103], v[16:31]\n\t" \
  "v_exp_f32 v70, v70\n\t" \
  "v_exp_f32 v71, v71\n\t" \
  "v_exp_f32 v72, v72\n\t" \
  "v_exp_f32 v73, v73\n\t" \
  "v_mfma_f32_32x32x16_bf16 v[32:47], v[80:83], v[104:107], v[32:47]\n\t" \
  "ds_read_b128 v[80:83], v110 offset:21504\n\t" \
  "v_exp_f32 v74, v74\n\t" \
  "v_exp_f32 v75, v75\n\t" \
  "v_exp_f32 v76, v76\n\t" \
  "v_exp_f32 v77, v77\n\t" \
  "v_smfmac_f32_32x32x32_bf16 v[128:143], v[96:99], v[64:71], v144\n\t" \
  "v_exp_f32 v78, v78\n\t" \
  "v_exp_f32 v79, v79\n\t" \
    \
  "v_exp_f32 v16, v16\n\t" \
  "v_exp_f32 v17, v17\n\t" \
  "v_smfmac_f32_32x32x32_bf16 v[128:143], v[88:91], v[72:79], v144\n\t" \
  "s_mov_b64 exec, s[34:35]\n\t" \
  "ds_read_b128 v[88:91], v110 offset:24192\n\t" \
  "ds_read_b128 v[96:99], v110 offset:26880\n\t" \
  "s_mov_b64 exec, -1\n\t" \
  "v_exp_f32 v18, v18\n\t" \
  "v_exp_f32 v19, v19\n\t" \
  "v_exp_f32 v20, v20\n\t" \
  "v_exp_f32 v21, v21\n\t" \
  "v_mfma_f32_32x32x4_2b_bf16 v[48:79], v[84:85], v[108:109], 4.0 cbsz:1 abid:1\n\t" \
  "v_exp_f32 v22, v22\n\t" \
  "v_exp_f32 v23, v23\n\t" \
  "v_exp_f32 v24, v24\n\t" \
  "v_exp_f32 v25, v25\n\t" \
  "v_exp_f32 v26, v26\n\t" \
  "v_exp_f32 v27, v27\n\t" \
  "v_exp_f32 v28, v28\n\t" \
  "v_exp_f32 v29, v29\n\t" \
  "v_smfmac_f32_32x32x32_bf16 v[112:127], v[92:95], v[16:23], v144\n\t" \
  "s_waitcnt lgkmcnt(0)\n\t" \
  "v_exp_f32 v30, v30\n\t" \
  "v_exp_f32 v31, v31\n\t" \
  "v_exp_f32 v32, v32\n\t" \
  "v_exp_f32 v33, v33\n\t" \
  "v_smfmac_f32_32x32x32_bf16 v[112:127], v[88:91], v[24:31], v144\n\t" \
  "v_exp_f32 v34, v34\n\t" \
  "v_exp_f32 v35, v35\n\t" \
  "v_exp_f32 v36, v36\n\t" \
  "v_exp_f32 v37, v37\n\t" \
  "v_mfma_f32_32x32x16_bf16 v[48:63], v[80:83], v[100:103], v[48:63]\n\t" \
  "v_exp_f32 v38, v38\n\t" \
  "v_exp_f32 v39, v39\n\t" \
  "v_exp_f32 v40, v40\n\t" \
  "v_exp_f32 v41, v41\n\t" \
  "v_mfma_f32_32x32x16_bf16 v[64:79], v[80:83], v[104:107], v[64:79]\n\t" \
  "ds_read_b128 v[80:83], v110 offset:24832\n\t" \
  "v_exp_f32 v42, v42\n\t" \
  "v_exp_f32 v43, v43\n\t" \
  "v_exp_f32 v44, v44\n\t" \
  "v_exp_f32 v45, v45\n\t" \
  "v_smfmac_f32_32x32x32_bf16 v[128:143], v[92:95], v[32:39], v144\n\t" \
  "v_exp_f32 v46, v46\n\t" \
  "v_exp_f32 v47, v47\n\t" \
    \
  "v_exp_f32 v48, v48\n\t" \
  "v_exp_f32 v49, v49\n\t" \
  "v_smfmac_f32_32x32x32_bf16 v[128:143], v[88:91], v[40:47], v144\n\t" \
  "s_mov_b64 exec, s[34:35]\n\t" \
  "ds_read_b128 v[88:91], v110 offset:27520\n\t" \
  "ds_read_b128 v[92:95], v110 offset:28160\n\t" \
  "s_mov_b64 exec, -1\n\t" \
  "v_exp_f32 v50, v50\n\t" \
  "v_exp_f32 v51, v51\n\t" \
  "v_exp_f32 v52, v52\n\t" \
  "v_exp_f32 v53, v53\n\t" \
  "v_mfma_f32_32x32x4_2b_bf16 v[16:47], v[86:87], v[108:109], 4.0 cbsz:1 abid:0\n\t" \
  "v_exp_f32 v54, v54\n\t" \
  "v_exp_f32 v55, v55\n\t" \
  "v_exp_f32 v56, v56\n\t" \
  "v_exp_f32 v57, v57\n\t" \
  "v_exp_f32 v58, v58\n\t" \
  "v_exp_f32 v59, v59\n\t" \
  "v_exp_f32 v60, v60\n\t" \
  "v_exp_f32 v61, v61\n\t" \
  "v_smfmac_f32_32x32x32_bf16 v[112:127], v[96:99], v[48:55], v144\n\t" \
  "s_waitcnt lgkmcnt(0)\n\t" \
  "v_exp_f32 v62, v62\n\t" \
  "v_exp_f32 v63, v63\n\t" \
  "v_exp_f32 v64, v64\n\t" \
  "v_exp_f32 v65, v65\n\t" \
  "v_smfmac_f32_32x32x32_bf16 v[112:127], v[88:91], v[56:63], v144\n\t" \
  "v_exp_f32 v66, v66\n\t" \
  "v_exp_f32 v67, v67\n\t" \
  "v_exp_f32 v68, v68\n\t" \
  "v_exp_f32 v69, v69\n\t" \
  "v_mfma_f32_32x32x16_bf16 v[16:31], v[80:83], v[100:103], v[16:31]\n\t" \
  "v_exp_f32 v70, v70\n\t" \
  "v_exp_f32 v71, v71\n\t" \
  "v_exp_f32 v72, v72\n\t" \
  "v_exp_f32 v73, v73\n\t" \
  "v_mfma_f32_32x32x16_bf16 v[32:47], v[80:83], v[104:107], v[32:47]\n\t" \
  "ds_read_b128 v[80:83], v110 offset:25856\n\t" \
  "v_exp_f32 v74, v74\n\t" \
  "v_exp_f32 v75, v75\n\t" \
  "v_exp_f32 v76, v76\n\t" \
  "v_exp_f32 v77, v77\n\t" \
  "v_smfmac_f32_32x32x32_bf16 v[128:143], v[96:99], v[64:71], v144\n\t" \
  "v_exp_f32 v78, v78\n\t" \
  "v_exp_f32 v79, v79\n\t" \
    \
  "v_exp_f32 v16, v16\n\t" \
  "v_exp_f32 v17, v17\n\t" \
  "v_smfmac_f32_32x32x32_bf16 v[128:143], v[88:91], v[72:79], v144\n\t" \
  "s_mov_b64 exec, s[34:35]\n\t" \
  "ds_read_b128 v[88:91], v110 offset:28800\n\t" \
  "ds_read_b128 v[96:99], v110 offset:29440\n\t" \
  "s_mov_b64 exec, -1\n\t" \
  "v_exp_f32 v18, v18\n\t" \
  "v_exp_f32 v19, v19\n\t" \
  "v_exp_f32 v20, v20\n\t" \
  "v_exp_f32 v21, v21\n\t" \
  "v_mfma_f32_32x32x4_2b_bf16 v[48:79], v[86:87], v[108:109], 4.0 cbsz:1 abid:1\n\t" \
  "ds_read2st64_b64 v[84:87], v111 offset0:64 offset1:65\n\t" \
  "v_exp_f32 v22, v22\n\t" \
  "v_exp_f32 v23, v23\n\t" \
  "v_exp_f32 v24, v24\n\t" \
  "v_exp_f32 v25, v25\n\t" \
  "v_exp_f32 v26, v26\n\t" \
  "v_exp_f32 v27, v27\n\t" \
  "v_exp_f32 v28, v28\n\t" \
  "v_exp_f32 v29, v29\n\t" \
  "v_smfmac_f32_32x32x32_bf16 v[112:127], v[92:95], v[16:23], v144\n\t" \
  "s_waitcnt lgkmcnt(0)\n\t" \
  "v_exp_f32 v30, v30\n\t" \
  "v_exp_f32 v31, v31\n\t" \
  "v_exp_f32 v32, v32\n\t" \
  "v_exp_f32 v33, v33\n\t" \
  "v_smfmac_f32_32x32x32_bf16 v[112:127], v[88:91], v[24:31], v144\n\t" \
  "v_exp_f32 v34, v34\n\t" \
  "v_exp_f32 v35, v35\n\t" \
  "v_exp_f32 v36, v36\n\t" \
  "v_exp_f32 v37, v37\n\t" \
  "v_mfma_f32_32x32x16_bf16 v[48:63], v[80:83], v[100:103], v[48:63]\n\t" \
  "v_exp_f32 v38, v38\n\t" \
  "v_exp_f32 v39, v39\n\t" \
  "v_exp_f32 v40, v40\n\t" \
  "v_exp_f32 v41, v41\n\t" \
  "v_mfma_f32_32x32x16_bf16 v[64:79], v[80:83], v[104:107], v[64:79]\n\t" \
  "ds_read_b128 v[80:83], v110 offset:30720\n\t" \
  "v_exp_f32 v42, v42\n\t" \
  "v_exp_f32 v43, v43\n\t" \
  "v_exp_f32 v44, v44\n\t" \
  "v_exp_f32 v45, v45\n\t" \
  "v_smfmac_f32_32x32x32_bf16 v[128:143], v[92:95], v[32:39], v144\n\t" \
  "v_exp_f32 v46, v46\n\t" \
  "v_exp_f32 v47, v47\n\t" \
    \
  "v_exp_f32 v48, v48\n\t" \
  "v_exp_f32 v49, v49\n\t" \
  "v_smfmac_f32_32x32x32_bf16 v[128:143], v[88:91], v[40:47], v144\n\t" \
  "s_mov_b64 exec, s[34:35]\n\t" \
  "ds_read_b128 v[88:91], v110 offset:30080\n\t" \
  "ds_read_b128 v[92:95], v110 offset:33792\n\t" \
  "s_mov_b64 exec, -1\n\t" \
  "v_exp_f32 v50, v50\n\t" \
  "v_exp_f32 v51, v51\n\t" \
  "v_exp_f32 v52, v52\n\t" \
  "v_exp_f32 v53, v53\n\t" \
  "v_mfma_f32_32x32x4_2b_bf16 v[16:47], v[84:85], v[108:109], 4.0 cbsz:1 abid:0\n\t" \
  "v_exp_f32 v54, v54\n\t" \
  "v_exp_f32 v55, v55\n\t" \
  "v_exp_f32 v56, v56\n\t" \
  "v_exp_f32 v57, v57\n\t" \
  "v_exp_f32 v58, v58\n\t" \
  "v_exp_f32 v59, v59\n\t" \
  "v_exp_f32 v60, v60\n\t" \
  "v_exp_f32 v61, v61\n\t" \
  "v_smfmac_f32_32x32x32_bf16 v[112:127], v[96:99], v[48:55], v144\n\t" \
  "s_waitcnt lgkmcnt(0)\n\t" \
  "v_exp_f32 v62, v62\n\t" \
  "v_exp_f32 v63, v63\n\t" \
  "v_exp_f32 v64, v64\n\t" \
  "v_exp_f32 v65, v65\n\t" \
  "v_smfmac_f32_32x32x32_bf16 v[112:127], v[88:91], v[56:63], v144\n\t" \
  "v_exp_f32 v66, v66\n\t" \
  "v_exp_f32 v67, v67\n\t" \
  "v_exp_f32 v68, v68\n\t" \
  "v_exp_f32 v69, v69\n\t" \
  "v_mfma_f32_32x32x16_bf16 v[16:31], v[80:83], v[100:103], v[16:31]\n\t" \
  "v_exp_f32 v70, v70\n\t" \
  "v_exp_f32 v71, v71\n\t" \
  "v_exp_f32 v72, v72\n\t" \
  "v_exp_f32 v73, v73\n\t" \
  "v_mfma_f32_32x32x16_bf16 v[32:47], v[80:83], v[104:107], v[32:47]\n\t" \
  "ds_read_b128 v[80:83], v110 offset:31744\n\t" \
  "v_exp_f32 v74, v74\n\t" \
  "v_exp_f32 v75, v75\n\t" \
  "v_exp_f32 v76, v76\n\t" \
  "v_exp_f32 v77, v77\n\t" \
  "v_smfmac_f32_32x32x32_bf16 v[128:143], v[96:99], v[64:71], v144\n\t" \
  "v_exp_f32 v78, v78\n\t" \
  "v_exp_f32 v79, v79\n\t" \
    \
  "v_exp_f32 v16, v16\n\t" \
  "v_exp_f32 v17, v17\n\t" \
  "v_smfmac_f32_32x32x32_bf16 v[128:143], v[88:91], v[72:79], v144\n\t" \
  "s_mov_b64 exec, s[34:35]\n\t" \
  "ds_read_b128 v[88:91], v110 offset:34432\n\t" \
  "ds_read_b128 v[96:99], v110 offset:37120\n\t" \
  "s_mov_b64 exec, -1\n\t" \
  "v_exp_f32 v18, v18\n\t" \
  "v_exp_f32 v19, v19\n\t" \
  "v_exp_f32 v20, v20\n\t" \
  "v_exp_f32 v21, v21\n\t" \
  "v_mfma_f32_32x32x4_2b_bf16 v[48:79], v[84:85], v[108:109], 4.0 cbsz:1 abid:1\n\t" \
  "v_exp_f32 v22, v22\n\t" \
  "v_exp_f32 v23, v23\n\t" \
  "v_exp_f32 v24, v24\n\t" \
  "v_exp_f32 v25, v25\n\t" \
  "v_exp_f32 v26, v26\n\t" \
  "v_exp_f32 v27, v27\n\t" \
  "v_exp_f32 v28, v28\n\t" \
  "v_exp_f32 v29, v29\n\t" \
  "v_smfmac_f32_32x32x32_bf16 v[112:127], v[92:95], v[16:23], v144\n\t" \
  "s_waitcnt lgkmcnt(0)\n\t" \
  "v_exp_f32 v30, v30\n\t" \
  "v_exp_f32 v31, v31\n\t" \
  "v_exp_f32 v32, v32\n\t" \
  "v_exp_f32 v33, v33\n\t" \
  "v_smfmac_f32_32x32x32_bf16 v[112:127], v[88:91], v[24:31], v144\n\t" \
  "v_exp_f32 v34, v34\n\t" \
  "v_exp_f32 v35, v35\n\t" \
  "v_exp_f32 v36, v36\n\t" \
  "v_exp_f32 v37, v37\n\t" \
  "v_mfma_f32_32x32x16_bf16 v[48:63], v[80:83], v[100:103], v[48:63]\n\t" \
  "v_exp_f32 v38, v38\n\t" \
  "v_exp_f32 v39, v39\n\t" \
  "v_exp_f32 v40, v40\n\t" \
  "v_exp_f32 v41, v41\n\t" \
  "v_mfma_f32_32x32x16_bf16 v[64:79], v[80:83], v[104:107], v[64:79]\n\t" \
  "ds_read_b128 v[80:83], v110 offset:35072\n\t" \
  "v_exp_f32 v42, v42\n\t" \
  "v_exp_f32 v43, v43\n\t" \
  "v_exp_f32 v44, v44\n\t" \
  "v_exp_f32 v45, v45\n\t" \
  "v_smfmac_f32_32x32x32_bf16 v[128:143], v[92:95], v[32:39], v144\n\t" \
  "v_exp_f32 v46, v46\n\t" \
  "v_exp_f32 v47, v47\n\t" \
    \
  "v_exp_f32 v48, v48\n\t" \
  "v_exp_f32 v49, v49\n\t" \
  "v_smfmac_f32_32x32x32_bf16 v[128:143], v[88:91], v[40:47], v144\n\t" \
  "s_mov_b64 exec, s[34:35]\n\t" \
  "ds_read_b128 v[88:91], v110 offset:37760\n\t" \
  "ds_read_b128 v[92:95], v110 offset:38400\n\t" \
  "s_mov_b64 exec, -1\n\t" \
  "v_exp_f32 v50, v50\n\t" \
  "v_exp_f32 v51, v51\n\t" \
  "v_exp_f32 v52, v52\n\t" \
  "v_exp_f32 v53, v53\n\t" \
  "v_mfma_f32_32x32x4_2b_bf16 v[16:47], v[86:87], v[108:109], 4.0 cbsz:1 abid:0\n\t" \
  "v_exp_f32 v54, v54\n\t" \
  "v_exp_f32 v55, v55\n\t" \
  "v_exp_f32 v56, v56\n\t" \
  "v_exp_f32 v57, v57\n\t" \
  "v_exp_f32 v58, v58\n\t" \
  "v_exp_f32 v59, v59\n\t" \
  "v_exp_f32 v60, v60\n\t" \
  "v_exp_f32 v61, v61\n\t" \
  "v_smfmac_f32_32x32x32_bf16 v[112:127], v[96:99], v[48:55], v144\n\t" \
  "s_waitcnt lgkmcnt(0)\n\t" \
  "v_exp_f32 v62, v62\n\t" \
  "v_exp_f32 v63, v63\n\t" \
  "v_exp_f32 v64, v64\n\t" \
  "v_exp_f32 v65, v65\n\t" \
  "v_smfmac_f32_32x32x32_bf16 v[112:127], v[88:91], v[56:63], v144\n\t" \
  "v_exp_f32 v66, v66\n\t" \
  "v_exp_f32 v67, v67\n\t" \
  "v_exp_f32 v68, v68\n\t" \
  "v_exp_f32 v69, v69\n\t" \
  "v_mfma_f32_32x32x16_bf16 v[16:31], v[80:83], v[100:103], v[16:31]\n\t" \
  "v_exp_f32 v70, v70\n\t" \
  "v_exp_f32 v71, v71\n\t" \
  "v_exp_f32 v72, v72\n\t" \
  "v_exp_f32 v73, v73\n\t" \
  "v_mfma_f32_32x32x16_bf16 v[32:47], v[80:83], v[104:107], v[32:47]\n\t" \
  "ds_read_b128 v[80:83], v110 offset:36096\n\t" \
  "v_exp_f32 v74, v74\n\t" \
  "v_exp_f32 v75, v75\n\t" \
  "v_exp_f32 v76, v76\n\t" \
  "v_exp_f32 v77, v77\n\t" \
  "v_smfmac_f32_32x32x32_bf16 v[128:143], v[96:99], v[64:71], v144\n\t" \
  "v_exp_f32 v78, v78\n\t" \
  "v_exp_f32 v79, v79\n\t" \
    \
  "v_exp_f32 v16, v16\n\t" \
  "v_exp_f32 v17, v17\n\t" \
  "v_smfmac_f32_32x32x32_bf16 v[128:143], v[88:91], v[72:79], v144\n\t" \
  "s_mov_b64 exec, s[34:35]\n\t" \
  "ds_read_b128 v[88:91], v110 offset:39040\n\t" \
  "ds_read_b128 v[96:99], v110 offset:39680\n\t" \
  "s_mov_b64 exec, -1\n\t" \
  "v_exp_f32 v18, v18\n\t" \
  "v_exp_f32 v19, v19\n\t" \
  "v_exp_f32 v20, v20\n\t" \
  "v_exp_f32 v21, v21\n\t" \
  "v_mfma_f32_32x32x4_2b_bf16 v[48:79], v[86:87], v[108:109], 4.0 cbsz:1 abid:1\n\t" \
  "ds_read2st64_b64 v[84:87], v111 offset0:84 offset1:85\n\t" \
  "v_exp_f32 v22, v22\n\t" \
  "v_exp_f32 v23, v23\n\t" \
  "v_exp_f32 v24, v24\n\t" \
  "v_exp_f32 v25, v25\n\t" \
  "v_exp_f32 v26, v26\n\t" \
  "v_exp_f32 v27, v27\n\t" \
  "v_exp_f32 v28, v28\n\t" \
  "v_exp_f32 v29, v29\n\t" \
  "v_smfmac_f32_32x32x32_bf16 v[112:127], v[92:95], v[16:23], v144\n\t" \
  "s_waitcnt lgkmcnt(0)\n\t" \
  "v_exp_f32 v30, v30\n\t" \
  "v_exp_f32 v31, v31\n\t" \
  "v_exp_f32 v32, v32\n\t" \
  "v_exp_f32 v33, v33\n\t" \
  "v_smfmac_f32_32x32x32_bf16 v[112:127], v[88:91], v[24:31], v144\n\t" \
  "v_exp_f32 v34, v34\n\t" \
  "v_exp_f32 v35, v35\n\t" \
  "v_exp_f32 v36, v36\n\t" \
  "v_exp_f32 v37, v37\n\t" \
  "v_mfma_f32_32x32x16_bf16 v[48:63], v[80:83], v[100:103], v[48:63]\n\t" \
  "v_exp_f32 v38, v38\n\t" \
  "v_exp_f32 v39, v39\n\t" \
  "v_exp_f32 v40, v40\n\t" \
  "v_exp_f32 v41, v41\n\t" \
  "v_mfma_f32_32x32x16_bf16 v[64:79], v[80:83], v[104:107], v[64:79]\n\t" \
  "ds_read_b128 v[80:83], v110 offset:40960\n\t" \
  "v_exp_f32 v42, v42\n\t" \
  "v_exp_f32 v43, v43\n\t" \
  "v_exp_f32 v44, v44\n\t" \
  "v_exp_f32 v45, v45\n\t" \
  "v_smfmac_f32_32x32x32_bf16 v[128:143], v[92:95], v[32:39], v144\n\t" \
  "v_exp_f32 v46, v46\n\t" \
  "v_exp_f32 v47, v47\n\t" \
    \
  "v_exp_f32 v48, v48\n\t" \
  "v_exp_f32 v49, v49\n\t" \
  "v_smfmac_f32_32x32x32_bf16 v[128:143], v[88:91], v[40:47], v144\n\t" \
  "s_mov_b64 exec, s[34:35]\n\t" \
  "ds_read_b128 v[88:91], v110 offset:40320\n\t" \
  "ds_read_b128 v[92:95], v110 offset:44032\n\t" \
  "s_mov_b64 exec, -1\n\t" \
  "v_exp_f32 v50, v50\n\t" \
  "v_exp_f32 v51, v51\n\t" \
  "v_exp_f32 v52, v52\n\t" \
  "v_exp_f32 v53, v53\n\t" \
  "v_mfma_f32_32x32x4_2b_bf16 v[16:47], v[84:85], v[108:109], 4.0 cbsz:1 abid:0\n\t" \
  "v_exp_f32 v54, v54\n\t" \
  "v_exp_f32 v55, v55\n\t" \
  "v_exp_f32 v56, v56\n\t" \
  "v_exp_f32 v57, v57\n\t" \
  "v_exp_f32 v58, v58\n\t" \
  "v_exp_f32 v59, v59\n\t" \
  "v_exp_f32 v60, v60\n\t" \
  "v_exp_f32 v61, v61\n\t" \
  "v_smfmac_f32_32x32x32_bf16 v[112:127], v[96:99], v[48:55], v144\n\t" \
  "s_waitcnt lgkmcnt(0)\n\t" \
  "v_exp_f32 v62, v62\n\t" \
  "v_exp_f32 v63, v63\n\t" \
  "v_exp_f32 v64, v64\n\t" \
  "v_exp_f32 v65, v65\n\t" \
  "v_smfmac_f32_32x32x32_bf16 v[112:127], v[88:91], v[56:63], v144\n\t" \
  "v_exp_f32 v66, v66\n\t" \
  "v_exp_f32 v67, v67\n\t" \
  "v_exp_f32 v68, v68\n\t" \
  "v_exp_f32 v69, v69\n\t" \
  "v_mfma_f32_32x32x16_bf16 v[16:31], v[80:83], v[100:103], v[16:31]\n\t" \
  "v_exp_f32 v70, v70\n\t" \
  "v_exp_f32 v71, v71\n\t" \
  "v_exp_f32 v72, v72\n\t" \
  "v_exp_f32 v73, v73\n\t" \
  "v_mfma_f32_32x32x16_bf16 v[32:47], v[80:83], v[104:107], v[32:47]\n\t" \
  "ds_read_b128 v[80:83], v110 offset:41984\n\t" \
  "v_exp_f32 v74, v74\n\t" \
  "v_exp_f32 v75, v75\n\t" \
  "v_exp_f32 v76, v76\n\t" \
  "v_exp_f32 v77, v77\n\t" \
  "v_smfmac_f32_32x32x32_bf16 v[128:143], v[96:99], v[64:71], v144\n\t" \
  "v_exp_f32 v78, v78\n\t" \
  "v_exp_f32 v79, v79\n\t" \
    \
  "v_exp_f32 v16, v16\n\t" \
  "v_exp_f32 v17, v17\n\t" \
  "v_smfmac_f32_32x32x32_bf16 v[128:143], v[88:91], v[72:79], v144\n\t" \
  "s_mov_b64 exec, s[34:35]\n\t" \
  "ds_read_b128 v[88:91], v110 offset:44672\n\t" \
  "ds_read_b128 v[96:99], v110 offset:47360\n\t" \
  "s_mov_b64 exec, -1\n\t" \
  "v_exp_f32 v18, v18\n\t" \
  "v_exp_f32 v19, v19\n\t" \
  "v_exp_f32 v20, v20\n\t" \
  "v_exp_f32 v21, v21\n\t" \
  "v_mfma_f32_32x32x4_2b_bf16 v[48:79], v[84:85], v[108:109], 4.0 cbsz:1 abid:1\n\t" \
  "v_exp_f32 v22, v22\n\t" \
  "v_exp_f32 v23, v23\n\t" \
  "v_exp_f32 v24, v24\n\t" \
  "v_exp_f32 v25, v25\n\t" \
  "v_exp_f32 v26, v26\n\t" \
  "v_exp_f32 v27, v27\n\t" \
  "v_exp_f32 v28, v28\n\t" \
  "v_exp_f32 v29, v29\n\t" \
  "v_smfmac_f32_32x32x32_bf16 v[112:127], v[92:95], v[16:23], v144\n\t" \
  "s_waitcnt lgkmcnt(0)\n\t" \
  "v_exp_f32 v30, v30\n\t" \
  "v_exp_f32 v31, v31\n\t" \
  "v_exp_f32 v32, v32\n\t" \
  "v_exp_f32 v33, v33\n\t" \
  "v_smfmac_f32_32x32x32_bf16 v[112:127], v[88:91], v[24:31], v144\n\t" \
  "v_exp_f32 v34, v34\n\t" \
  "v_exp_f32 v35, v35\n\t" \
  "v_exp_f32 v36, v36\n\t" \
  "v_exp_f32 v37, v37\n\t" \
  "v_mfma_f32_32x32x16_bf16 v[48:63], v[80:83], v[100:103], v[48:63]\n\t" \
  "v_exp_f32 v38, v38\n\t" \
  "v_exp_f32 v39, v39\n\t" \
  "v_exp_f32 v40, v40\n\t" \
  "v_exp_f32 v41, v41\n\t" \
  "v_mfma_f32_32x32x16_bf16 v[64:79], v[80:83], v[104:107], v[64:79]\n\t" \
  "ds_read_b128 v[80:83], v110 offset:45312\n\t" \
  "v_exp_f32 v42, v42\n\t" \
  "v_exp_f32 v43, v43\n\t" \
  "v_exp_f32 v44, v44\n\t" \
  "v_exp_f32 v45, v45\n\t" \
  "v_smfmac_f32_32x32x32_bf16 v[128:143], v[92:95], v[32:39], v144\n\t" \
  "v_exp_f32 v46, v46\n\t" \
  "v_exp_f32 v47, v47\n\t" \
    \
  "v_exp_f32 v48, v48\n\t" \
  "v_exp_f32 v49, v49\n\t" \
  "v_smfmac_f32_32x32x32_bf16 v[128:143], v[88:91], v[40:47], v144\n\t" \
  "s_mov_b64 exec, s[34:35]\n\t" \
  "ds_read_b128 v[88:91], v110 offset:48000\n\t" \
  "ds_read_b128 v[92:95], v110 offset:48640\n\t" \
  "s_mov_b64 exec, -1\n\t" \
  "v_exp_f32 v50, v50\n\t" \
  "v_exp_f32 v51, v51\n\t" \
  "v_exp_f32 v52, v52\n\t" \
  "v_exp_f32 v53, v53\n\t" \
  "v_mfma_f32_32x32x4_2b_bf16 v[16:47], v[86:87], v[108:109], 4.0 cbsz:1 abid:0\n\t" \
  "v_exp_f32 v54, v54\n\t" \
  "v_exp_f32 v55, v55\n\t" \
  "v_exp_f32 v56, v56\n\t" \
  "v_exp_f32 v57, v57\n\t" \
  "v_exp_f32 v58, v58\n\t" \
  "v_exp_f32 v59, v59\n\t" \
  "v_exp_f32 v60, v60\n\t" \
  "v_exp_f32 v61, v61\n\t" \
  "v_smfmac_f32_32x32x32_bf16 v[112:127], v[96:99], v[48:55], v144\n\t" \
  "s_waitcnt lgkmcnt(0)\n\t" \
  "v_exp_f32 v62, v62\n\t" \
  "v_exp_f32 v63, v63\n\t" \
  "v_exp_f32 v64, v64\n\t" \
  "v_exp_f32 v65, v65\n\t" \
  "v_smfmac_f32_32x32x32_bf16 v[112:127], v[88:91], v[56:63], v144\n\t" \
  "v_exp_f32 v66, v66\n\t" \
  "v_exp_f32 v67, v67\n\t" \
  "v_exp_f32 v68, v68\n\t" \
  "v_exp_f32 v69, v69\n\t" \
  "v_mfma_f32_32x32x16_bf16 v[16:31], v[80:83], v[100:103], v[16:31]\n\t" \
  "v_exp_f32 v70, v70\n\t" \
  "v_exp_f32 v71, v71\n\t" \
  "v_exp_f32 v72, v72\n\t" \
  "v_exp_f32 v73, v73\n\t" \
  "v_mfma_f32_32x32x16_bf16 v[32:47], v[80:83], v[104:107], v[32:47]\n\t" \
  "ds_read_b128 v[80:83], v110 offset:46336\n\t" \
  "v_exp_f32 v74, v74\n\t" \
  "v_exp_f32 v75, v75\n\t" \
  "v_exp_f32 v76, v76\n\t" \
  "v_exp_f32 v77, v77\n\t" \
  "v_smfmac_f32_32x32x32_bf16 v[128:143], v[96:99], v[64:71], v144\n\t" \
  "v_exp_f32 v78, v78\n\t" \
  "v_exp_f32 v79, v79\n\t" \
    \
  "v_exp_f32 v16, v16\n\t" \
  "v_exp_f32 v17, v17\n\t" \
  "v_smfmac_f32_32x32x32_bf16 v[128:143], v[88:91], v[72:79], v144\n\t" \
  "s_mov_b64 exec, s[34:35]\n\t" \
  "ds_read_b128 v[88:91], v110 offset:49280\n\t" \
  "ds_read_b128 v[96:99], v110 offset:49920\n\t" \
  "s_mov_b64 exec, -1\n\t" \
  "v_exp_f32 v18, v18\n\t" \
  "v_exp_f32 v19, v19\n\t" \
  "v_exp_f32 v20, v20\n\t" \
  "v_exp_f32 v21, v21\n\t" \
  "v_mfma_f32_32x32x4_2b_bf16 v[48:79], v[86:87], v[108:109], 4.0 cbsz:1 abid:1\n\t" \
  "ds_read2st64_b64 v[84:87], v111 offset0:104 offset1:105\n\t" \
  "v_exp_f32 v22, v22\n\t" \
  "v_exp_f32 v23, v23\n\t" \
  "v_exp_f32 v24, v24\n\t" \
  "v_exp_f32 v25, v25\n\t" \
  "v_exp_f32 v26, v26\n\t" \
  "v_exp_f32 v27, v27\n\t" \
  "v_exp_f32 v28, v28\n\t" \
  "v_exp_f32 v29, v29\n\t" \
  "v_smfmac_f32_32x32x32_bf16 v[112:127], v[92:95], v[16:23], v144\n\t" \
  "s_waitcnt lgkmcnt(0)\n\t" \
  "v_exp_f32 v30, v30\n\t" \
  "v_exp_f32 v31, v31\n\t" \
  "v_exp_f32 v32, v32\n\t" \
  "v_exp_f32 v33, v33\n\t" \
  "v_smfmac_f32_32x32x32_bf16 v[112:127], v[88:91], v[24:31], v144\n\t" \
  "v_exp_f32 v34, v34\n\t" \
  "v_exp_f32 v35, v35\n\t" \
  "v_exp_f32 v36, v36\n\t" \
  "v_exp_f32 v37, v37\n\t" \
  "v_mfma_f32_32x32x16_bf16 v[48:63], v[80:83], v[100:103], v[48:63]\n\t" \
  "v_exp_f32 v38, v38\n\t" \
  "v_exp_f32 v39, v39\n\t" \
  "v_exp_f32 v40, v40\n\t" \
  "v_exp_f32 v41, v41\n\t" \
  "v_mfma_f32_32x32x16_bf16 v[64:79], v[80:83], v[104:107], v[64:79]\n\t" \
  "ds_read_b128 v[80:83], v110 offset:51200\n\t" \
  "v_exp_f32 v42, v42\n\t" \
  "v_exp_f32 v43, v43\n\t" \
  "v_exp_f32 v44, v44\n\t" \
  "v_exp_f32 v45, v45\n\t" \
  "v_smfmac_f32_32x32x32_bf16 v[128:143], v[92:95], v[32:39], v144\n\t" \
  "v_exp_f32 v46, v46\n\t" \
  "v_exp_f32 v47, v47\n\t" \
    \
  "v_exp_f32 v48, v48\n\t" \
  "v_exp_f32 v49, v49\n\t" \
  "v_smfmac_f32_32x32x32_bf16 v[128:143], v[88:91], v[40:47], v144\n\t" \
  "s_mov_b64 exec, s[34:35]\n\t" \
  "ds_read_b128 v[88:91], v110 offset:50560\n\t" \
  "ds_read_b128 v[92:95], v110 offset:54272\n\t" \
  "s_mov_b64 exec, -1\n\t" \
  "v_exp_f32 v50, v50\n\t" \
  "v_exp_f32 v51, v51\n\t" \
  "v_exp_f32 v52, v52\n\t" \
  "v_exp_f32 v53, v53\n\t" \
  "v_mfma_f32_32x32x4_2b_bf16 v[16:47], v[84:85], v[108:109], 4.0 cbsz:1 abid:0\n\t" \
  "v_exp_f32 v54, v54\n\t" \
  "v_exp_f32 v55, v55\n\t" \
  "v_exp_f32 v56, v56\n\t" \
  "v_exp_f32 v57, v57\n\t" \
  "v_exp_f32 v58, v58\n\t" \
  "v_exp_f32 v59, v59\n\t" \
  "v_exp_f32 v60, v60\n\t" \
  "v_exp_f32 v61, v61\n\t" \
  "v_smfmac_f32_32x32x32_bf16 v[112:127], v[96:99], v[48:55], v144\n\t" \
  "s_waitcnt lgkmcnt(0)\n\t" \
  "v_exp_f32 v62, v62\n\t" \
  "v_exp_f32 v63, v63\n\t" \
  "v_exp_f32 v64, v64\n\t" \
  "v_exp_f32 v65, v65\n\t" \
  "v_smfmac_f32_32x32x32_bf16 v[112:127], v[88:91], v[56:63], v144\n\t" \
  "v_exp_f32 v66, v66\n\t" \
  "v_exp_f32 v67, v67\n\t" \
  "v_exp_f32 v68, v68\n\t" \
  "v_exp_f32 v69, v69\n\t" \
  "v_mfma_f32_32x32x16_bf16 v[16:31], v[80:83], v[100:103], v[16:31]\n\t" \
  "v_exp_f32 v70, v70\n\t" \
  "v_exp_f32 v71, v71\n\t" \
  "v_exp_f32 v72, v72\n\t" \
  "v_exp_f32 v73, v73\n\t" \
  "v_mfma_f32_32x32x16_bf16 v[32:47], v[80:83], v[104:107], v[32:47]\n\t" \
  "ds_read_b128 v[80:83], v110 offset:52224\n\t" \
  "v_exp_f32 v74, v74\n\t" \
  "v_exp_f32 v75, v75\n\t" \
  "v_exp_f32 v76, v76\n\t" \
  "v_exp_f32 v77, v77\n\t" \
  "v_smfmac_f32_32x32x32_bf16 v[128:143], v[96:99], v[64:71], v144\n\t" \
  "v_exp_f32 v78, v78\n\t" \
  "v_exp_f32 v79, v79\n\t" \
  "v_add_u32 v110, 51200, v110\n\t" \
  "v_add_u32 v111, 51200, v111\n\t" \
    \
  "v_exp_f32 v16, v16\n\t" \
  "v_exp_f32 v17, v17\n\t" \
  "v_smfmac_f32_32x32x32_bf16 v[128:143], v[88:91], v[72:79], v144\n\t" \
  "s_mov_b64 exec, s[34:35]\n\t" \
  "ds_read_b128 v[88:91], v110 offset:3712\n\t" \
  "ds_read_b128 v[96:99], v110 offset:6400\n\t" \
  "s_mov_b64 exec, -1\n\t" \
  "v_exp_f32 v18, v18\n\t" \
  "v_exp_f32 v19, v19\n\t" \
  "v_exp_f32 v20, v20\n\t" \
  "v_exp_f32 v21, v21\n\t" \
  "v_mfma_f32_32x32x4_2b_bf16 v[48:79], v[84:85], v[108:109], 4.0 cbsz:1 abid:1\n\t" \
  "v_exp_f32 v22, v22\n\t" \
  "v_exp_f32 v23, v23\n\t" \
  "v_exp_f32 v24, v24\n\t" \
  "v_exp_f32 v25, v25\n\t" \
  "v_exp_f32 v26, v26\n\t" \
  "v_exp_f32 v27, v27\n\t" \
  "v_exp_f32 v28, v28\n\t" \
  "v_exp_f32 v29, v29\n\t" \
  "v_smfmac_f32_32x32x32_bf16 v[112:127], v[92:95], v[16:23], v144\n\t" \
  "s_waitcnt lgkmcnt(0)\n\t" \
  "v_exp_f32 v30, v30\n\t" \
  "v_exp_f32 v31, v31\n\t" \
  "v_exp_f32 v32, v32\n\t" \
  "v_exp_f32 v33, v33\n\t" \
  "v_smfmac_f32_32x32x32_bf16 v[112:127], v[88:91], v[24:31], v144\n\t" \
  "v_exp_f32 v34, v34\n\t" \
  "v_exp_f32 v35, v35\n\t" \
  "v_exp_f32 v36, v36\n\t" \
  "v_exp_f32 v37, v37\n\t" \
  "v_mfma_f32_32x32x16_bf16 v[48:63], v[80:83], v[100:103], v[48:63]\n\t" \
  "v_exp_f32 v38, v38\n\t" \
  "v_exp_f32 v39, v39\n\t" \
  "v_exp_f32 v40, v40\n\t" \
  "v_exp_f32 v41, v41\n\t" \
  "v_mfma_f32_32x32x16_bf16 v[64:79], v[80:83], v[104:107], v[64:79]\n\t" \
  "ds_read_b128 v[80:83], v110 offset:4352\n\t" \
  "v_exp_f32 v42, v42\n\t" \
  "v_exp_f32 v43, v43\n\t" \
  "v_exp_f32 v44, v44\n\t" \
  "v_exp_f32 v45, v45\n\t" \
  "v_smfmac_f32_32x32x32_bf16 v[128:143], v[92:95], v[32:39], v144\n\t" \
  "v_exp_f32 v46, v46\n\t" \
  "v_exp_f32 v47, v47\n\t" \
    \
  "v_exp_f32 v48, v48\n\t" \
  "v_exp_f32 v49, v49\n\t" \
  "v_smfmac_f32_32x32x32_bf16 v[128:143], v[88:91], v[40:47], v144\n\t" \
  "s_mov_b64 exec, s[34:35]\n\t" \
  "ds_read_b128 v[88:91], v110 offset:7040\n\t" \
  "ds_read_b128 v[92:95], v110 offset:7680\n\t" \
  "s_mov_b64 exec, -1\n\t" \
  "v_exp_f32 v50, v50\n\t" \
  "v_exp_f32 v51, v51\n\t" \
  "v_exp_f32 v52, v52\n\t" \
  "v_exp_f32 v53, v53\n\t" \
  "v_mfma_f32_32x32x4_2b_bf16 v[16:47], v[86:87], v[108:109], 4.0 cbsz:1 abid:0\n\t" \
  "v_exp_f32 v54, v54\n\t" \
  "v_exp_f32 v55, v55\n\t" \
  "v_exp_f32 v56, v56\n\t" \
  "v_exp_f32 v57, v57\n\t" \
  "v_exp_f32 v58, v58\n\t" \
  "v_exp_f32 v59, v59\n\t" \
  "v_exp_f32 v60, v60\n\t" \
  "v_exp_f32 v61, v61\n\t" \
  "v_smfmac_f32_32x32x32_bf16 v[112:127], v[96:99], v[48:55], v144\n\t" \
  "s_waitcnt lgkmcnt(0)\n\t" \
  "v_exp_f32 v62, v62\n\t" \
  "v_exp_f32 v63, v63\n\t" \
  "v_exp_f32 v64, v64\n\t" \
  "v_exp_f32 v65, v65\n\t" \
  "v_smfmac_f32_32x32x32_bf16 v[112:127], v[88:91], v[56:63], v144\n\t" \
  "v_exp_f32 v66, v66\n\t" \
  "v_exp_f32 v67, v67\n\t" \
  "v_exp_f32 v68, v68\n\t" \
  "v_exp_f32 v69, v69\n\t" \
  "v_mfma_f32_32x32x16_bf16 v[16:31], v[80:83], v[100:103], v[16:31]\n\t" \
  "v_exp_f32 v70, v70\n\t" \
  "v_exp_f32 v71, v71\n\t" \
  "v_exp_f32 v72, v72\n\t" \
  "v_exp_f32 v73, v73\n\t" \
  "v_mfma_f32_32x32x16_bf16 v[32:47], v[80:83], v[104:107], v[32:47]\n\t" \
  "ds_read_b128 v[80:83], v110 offset:5376\n\t" \
  "v_exp_f32 v74, v74\n\t" \
  "v_exp_f32 v75, v75\n\t" \
  "v_exp_f32 v76, v76\n\t" \
  "v_exp_f32 v77, v77\n\t" \
  "v_smfmac_f32_32x32x32_bf16 v[128:143], v[96:99], v[64:71], v144\n\t" \
  "v_exp_f32 v78, v78\n\t" \
  "v_exp_f32 v79, v79\n\t" \
    \
  "v_exp_f32 v16, v16\n\t" \
  "v_exp_f32 v17, v17\n\t" \
  "v_smfmac_f32_32x32x32_bf16 v[128:143], v[88:91], v[72:79], v144\n\t" \
  "s_mov_b64 exec, s[34:35]\n\t" \
  "ds_read_b128 v[88:91], v110 offset:8320\n\t" \
  "ds_read_b128 v[96:99], v110 offset:8960\n\t" \
  "s_mov_b64 exec, -1\n\t" \
  "v_exp_f32 v18, v18\n\t" \
  "v_exp_f32 v19, v19\n\t" \
  "v_exp_f32 v20, v20\n\t" \
  "v_exp_f32 v21, v21\n\t" \
  "v_mfma_f32_32x32x4_2b_bf16 v[48:79], v[86:87], v[108:109], 4.0 cbsz:1 abid:1\n\t" \
  "v_exp_f32 v22, v22\n\t" \
  "v_exp_f32 v23, v23\n\t" \
  "v_exp_f32 v24, v24\n\t" \
  "v_exp_f32 v25, v25\n\t" \
  "v_exp_f32 v26, v26\n\t" \
  "v_exp_f32 v27, v27\n\t" \
  "v_exp_f32 v28, v28\n\t" \
  "v_exp_f32 v29, v29\n\t" \
  "v_smfmac_f32_32x32x32_bf16 v[112:127], v[92:95], v[16:23], v144\n\t" \
  "s_waitcnt lgkmcnt(0)\n\t" \
  "v_exp_f32 v30, v30\n\t" \
  "v_exp_f32 v31, v31\n\t" \
  "v_exp_f32 v32, v32\n\t" \
  "v_exp_f32 v33, v33\n\t" \
  "v_smfmac_f32_32x32x32_bf16 v[112:127], v[88:91], v[24:31], v144\n\t" \
  "v_exp_f32 v34, v34\n\t" \
  "v_exp_f32 v35, v35\n\t" \
  "v_exp_f32 v36, v36\n\t" \
  "v_exp_f32 v37, v37\n\t" \
  "v_mfma_f32_32x32x16_bf16 v[48:63], v[80:83], v[100:103], v[48:63]\n\t" \
  "v_exp_f32 v38, v38\n\t" \
  "v_exp_f32 v39, v39\n\t" \
  "v_exp_f32 v40, v40\n\t" \
  "v_exp_f32 v41, v41\n\t" \
  "v_mfma_f32_32x32x16_bf16 v[64:79], v[80:83], v[104:107], v[64:79]\n\t" \
  "v_exp_f32 v42, v42\n\t" \
  "v_exp_f32 v43, v43\n\t" \
  "v_exp_f32 v44, v44\n\t" \
  "v_exp_f32 v45, v45\n\t" \
  "v_smfmac_f32_32x32x32_bf16 v[128:143], v[92:95], v[32:39], v144\n\t" \
  "v_exp_f32 v46, v46\n\t" \
  "v_exp_f32 v47, v47\n\t" \
    \
  "v_exp_f32 v48, v48\n\t" \
  "v_exp_f32 v49, v49\n\t" \
  "v_smfmac_f32_32x32x32_bf16 v[128:143], v[88:91], v[40:47], v144\n\t" \
  "s_mov_b64 exec, s[34:35]\n\t" \
  "ds_read_b128 v[88:91], v110 offset:9600\n\t" \
  "s_mov_b64 exec, -1\n\t" \
  "v_exp_f32 v50, v50\n\t" \
  "v_exp_f32 v51, v51\n\t" \
  "v_exp_f32 v52, v52\n\t" \
  "v_exp_f32 v53, v53\n\t" \
  "v_exp_f32 v54, v54\n\t" \
  "v_exp_f32 v55, v55\n\t" \
  "v_exp_f32 v56, v56\n\t" \
  "v_exp_f32 v57, v57\n\t" \
  "v_exp_f32 v58, v58\n\t" \
  "v_exp_f32 v59, v59\n\t" \
  "v_exp_f32 v60, v60\n\t" \
  "v_exp_f32 v61, v61\n\t" \
  "v_smfmac_f32_32x32x32_bf16 v[112:127], v[96:99], v[48:55], v144\n\t" \
  "s_waitcnt lgkmcnt(0)\n\t" \
  "v_exp_f32 v62, v62\n\t" \
  "v_exp_f32 v63, v63\n\t" \
  "v_exp_f32 v64, v64\n\t" \
  "v_exp_f32 v65, v65\n\t" \
  "v_smfmac_f32_32x32x32_bf16 v[112:127], v[88:91], v[56:63], v144\n\t" \
  "v_exp_f32 v66, v66\n\t" \
  "v_exp_f32 v67, v67\n\t" \
  "v_exp_f32 v68, v68\n\t" \
  "v_exp_f32 v69, v69\n\t" \
  "v_exp_f32 v70, v70\n\t" \
  "v_exp_f32 v71, v71\n\t" \
  "v_exp_f32 v72, v72\n\t" \
  "v_exp_f32 v73, v73\n\t" \
  "v_exp_f32 v74, v74\n\t" \
  "v_exp_f32 v75, v75\n\t" \
  "v_exp_f32 v76, v76\n\t" \
  "v_exp_f32 v77, v77\n\t" \
  "v_smfmac_f32_32x32x32_bf16 v[128:143], v[96:99], v[64:71], v144\n\t" \
  "v_exp_f32 v78, v78\n\t" \
  "v_exp_f32 v79, v79\n\t" \
    \
  "s_setprio 3\n\t" \
  "s_waitcnt vmcnt(0)\n\t" \
  "v_smfmac_f32_32x32x32_bf16 v[128:143], v[88:91], v[72:79], v144\n\t" \
  "v_rcp_f32 v2, v122\n\t" \
  "s_mov_b32 s39, 0x7f000000\n\t" \
  "v_mul_f32 v2, v145, v2\n\t" \
  "v_mul_f32 v112, v112, v2\n\t" \
  "v_mul_f32 v113, v113, v2\n\t" \
  "v_mul_f32 v114, v114, v2\n\t" \
  "v_mul_f32 v115, v115, v2\n\t" \
  "v_mul_f32 v116, v116, v2\n\t" \
  "v_mul_f32 v117, v117, v2\n\t" \
  "v_mul_f32 v118, v118, v2\n\t" \
  "v_mul_f32 v119, v119, v2\n\t" \
  "v_mul_f32 v120, v120, v2\n\t" \
  "v_mul_f32 v121, v121, v2\n\t" \
  "v_rcp_f32 v3, v138\n\t" \
  "v_min_f32 v4, v122, v138\n\t" \
  "v_mul_f32 v3, v145, v3\n\t" \
  "v_max_f32 v5, v122, v138\n\t" \
  "v_mul_f32 v128, v128, v3\n\t" \
  "v_mul_f32 v129, v129, v3\n\t" \
  "v_mul_f32 v130, v130, v3\n\t" \
  "v_mul_f32 v131, v131, v3\n\t" \
  "v_mul_f32 v132, v132, v3\n\t" \
  "v_mul_f32 v133, v133, v3\n\t" \
  "v_mul_f32 v134, v134, v3\n\t" \
  "v_mul_f32 v135, v135, v3\n\t" \
  "v_mul_f32 v136, v136, v3\n\t" \
  "v_mul_f32 v137, v137, v3\n\t" \
  "v_cmp_lt_f32 vcc, 0x71800000, v5\n\t" \
  "v_maximum3_f32 v8, abs(v121), abs(v112), abs(v113)\n\t" \
  "v_maximum3_f32 v8, v8, abs(v114), abs(v115)\n\t" \
  "v_maximum3_f32 v8, v8, abs(v116), abs(v117)\n\t" \
  "v_maximum3_f32 v8, v8, abs(v118), abs(v119)\n\t" \
  "v_maximum3_f32 v8, v8, abs(v120), abs(v121)\n\t" \
  "s_mov_b64 s[36:37], vcc\n\t" \
  "v_cmp_gt_f32 vcc, 0x2b800000, v4\n\t" \
  "v_maximum3_f32 v8, v8, abs(v128), abs(v129)\n\t" \
  "v_maximum3_f32 v8, v8, abs(v130), abs(v131)\n\t" \
  "v_maximum3_f32 v8, v8, abs(v132), abs(v133)\n\t" \
  "v_maximum3_f32 v8, v8, abs(v134), abs(v135)\n\t" \
  "v_maximum3_f32 v8, v8, abs(v136), abs(v137)\n\t" \
  "s_or_b64 s[36:37], s[36:37], vcc\n\t" \
  "v_cmp_nge_f32 vcc, s39, v8\n\t" \
  "v_cvt_pk_bf16_f32 v16, v112, v113\n\t" \
  "v_cvt_pk_bf16_f32 v17, v114, v115\n\t" \
  "v_cvt_pk_bf16_f32 v18, v116, v117\n\t" \
  "v_cvt_pk_bf16_f32 v19, v118, v119\n\t" \
  "s_or_b64 vcc, s[36:37], vcc\n\t" \
  "s_cbranch_scc1 6f\n\t" \
  "buffer_store_dwordx4 v[16:19], v14, s[28:31], 0 offen sc1\n\t" \
  "v_cvt_pk_bf16_f32 v20, v128, v129\n\t" \
  "v_cvt_pk_bf16_f32 v21, v130, v131\n\t" \
  "v_cvt_pk_bf16_f32 v22, v132, v133\n\t" \
  "v_cvt_pk_bf16_f32 v23, v134, v135\n\t" \
  "buffer_store_dwordx4 v[20:23], v14, s[28:31], 0 offen offset:1024 sc1\n\t" \
  "v_cvt_pk_bf16_f32 v24, v120, v121\n\t" \
  "v_cvt_pk_bf16_f32 v25, v136, v137\n\t" \
  "buffer_store_dwordx2 v[24:25], v15, s[28:31], 0 offen offset:2048 sc1\n\t" \
  "8:\n\t" \
    \
    \
  "s_cmp_eq_u32 s41, 0\n\t" \
  "s_cbranch_scc0 50f\n\t" \
  "s_barrier\n\t" \
  "s_add_u32 s39, s43, 24576\n\t" \
  "buffer_load_dwordx4 v[64:67], v0, s[28:31], s39 offen offset:3072 sc1\n\t" \
  "s_add_u32 s39, s43, 28672\n\t" \
  "buffer_load_dwordx4 v[68:71], v0, s[28:31], s39 offen offset:0 sc1\n\t" \
  "buffer_load_dwordx4 v[72:75], v0, s[28:31], s39 offen offset:1024 sc1\n\t" \
  "buffer_load_dwordx4 v[76:79], v0, s[28:31], s39 offen offset:2048 sc1\n\t" \
  "buffer_load_dwordx4 v[80:83], v0, s[28:31], s39 offen offset:3072 sc1\n\t" \
  "s_add_u32 s39, s43, 32768\n\t" \
  "buffer_load_dwordx4 v[84:87], v0, s[28:31], s39 offen offset:0 sc1\n\t" \
  "buffer_load_dwordx4 v[88:91], v0, s[28:31], s39 offen offset:1024 sc1\n\t" \
  "buffer_load_dwordx4 v[92:95], v0, s[28:31], s39 offen offset:2048 sc1\n\t" \
  "s_mov_b32 s40, 0x40000\n\t" \
  "s_mov_b32 m0, s47\n\t" \
  "s_add_u32 s54, s43, 32768\n\t" \
  "s_add_u32 s50, s43, 36864\n\t" \
  "s_add_u32 s51, s43, 40960\n\t" \
  "s_add_u32 s52, s43, 45056\n\t" \
  "s_add_u32 s53, s43, 49152\n\t" \
  "s_waitcnt vmcnt(8)\n\t" \
  "s_add_u32 s39, s46, 0\n\t" \
  "buffer_load_dwordx4 v[24:27], v0, s[28:31], s39 offen sc1\n\t" \
  "buffer_load_dwordx4 v[28:31], v0, s[28:31], s39 offen offset:1024 sc1\n\t" \
  "buffer_load_dwordx2 v[56:57], v1, s[28:31], s39 offen offset:2048 sc1\n\t" \
  "s_add_u32 s39, s46, 2560\n\t" \
  "buffer_load_dwordx4 v[32:35], v0, s[28:31], s39 offen sc1\n\t" \
  "buffer_load_dwordx4 v[36:39], v0, s[28:31], s39 offen offset:1024 sc1\n\t" \
  "buffer_load_dwordx2 v[58:59], v1, s[28:31], s39 offen offset:2048 sc1\n\t" \
  "s_add_u32 s39, s46, 5120\n\t" \
  "buffer_load_dwordx4 v[40:43], v0, s[28:31], s39 offen sc1\n\t" \
  "buffer_load_dwordx4 v[44:47], v0, s[28:31], s39 offen offset:1024 sc1\n\t" \
  "buffer_load_dwordx2 v[60:61], v1, s[28:31], s39 offen offset:2048 sc1\n\t" \
  "s_add_u32 s39, s46, 7680\n\t" \
  "buffer_load_dwordx4 v[48:51], v0, s[28:31], s39 offen sc1\n\t" \
  "buffer_load_dwordx4 v[52:55], v0, s[28:31], s39 offen offset:1024 sc1\n\t" \
  "buffer_load_dwordx2 v[62:63], v1, s[28:31], s39 offen offset:2048 sc1\n\t" \
  "51:\n\t" \
  "s_waitcnt vmcnt(11)\n\t" \
  "v_mfma_f32_32x32x16_bf16 v[8:23], v[24:27], v[148:151], 0\n\t" \
  "v_max3_u32 v6, v24, v25, v26\n\t" \
  "v_max_u32 v6, v6, v27\n\t" \
  "v_cmp_eq_u32 vcc, -1, v6\n\t" \
  "v_mfma_f32_32x32x16_bf16 v[132:147], v[24:27], v[188:191], 0\n\t" \
  "s_waitcnt vmcnt(10)\n\t" \
  "v_mfma_f32_32x32x16_bf16 v[8:23], v[28:31], v[152:155], v[8:23]\n\t" \
  "v_max3_u32 v6, v6, v28, v29\n\t" \
  "v_max3_u32 v6, v6, v30, v31\n\t" \
  "v_mfma_f32_32x32x16_bf16 v[132:147], v[28:31], v[192:195], v[132:147]\n\t" \
  "s_cbranch_vccnz 54f\n\t" \
  "s_waitcnt vmcnt(8)\n\t" \
  "v_mfma_f32_32x32x16_bf16 v[8:23], v[32:35], v[156:159], v[8:23]\n\t" \
  "v_max3_u32 v6, v6, v32, v33\n\t" \
  "v_max3_u32 v6, v6, v34, v35\n\t" \
  "v_cmp_eq_u32 vcc, -1, v6\n\t" \
  "v_mfma_f32_32x32x16_bf16 v[132:147], v[32:35], v[196:199], v[132:147]\n\t" \
  "s_waitcnt vmcnt(7)\n\t" \
  "v_mfma_f32_32x32x16_bf16 v[8:23], v[36:39], v[160:163], v[8:23]\n\t" \
  "v_max3_u32 v6, v6, v36, v37\n\t" \
  "v_max3_u32 v6, v6, v38, v39\n\t" \
  "v_mfma_f32_32x32x16_bf16 v[132:147], v[36:39], v[200:203], v[132:147]\n\t" \
  "s_cbranch_vccnz 54f\n\t" \
  "s_waitcnt vmcnt(6)\n\t" \
  "v_mfma_f32_32x32x16_bf16 v[8:23], v[56:59], v[180:183], v[8:23]\n\t" \
  "v_max3_u32 v6, v6, v56, v57\n\t" \
  "v_max3_u32 v6, v6, v58, v59\n\t" \
  "v_mfma_f32_32x32x16_bf16 v[132:147], v[56:59], v[220:223], v[132:147]\n\t" \
  "s_cmp_eq_u32 s40, 0x40000\n\t" \
  "s_cbranch_scc0 53f\n\t" \
  "s_barrier\n\t" \
  "s_sub_u32 s40, s40, 1\n\t" \
  "53:\n\t" \
  "buffer_load_dwordx4 v[96:99], v0, s[28:31], s54 offen offset:3072 sc1\n\t" \
  "s_waitcnt vmcnt(6)\n\t" \
  "v_mfma_f32_32x32x16_bf16 v[8:23], v[40:43], v[164:167], v[8:23]\n\t" \
  "v_max3_u32 v6, v6, v40, v41\n\t" \
  "v_max3_u32 v6, v6, v42, v43\n\t" \
  "v_cmp_eq_u32 vcc, -1, v6\n\t" \
  "v_mfma_f32_32x32x16_bf16 v[132:147], v[40:43], v[204:207], v[132:147]\n\t" \
  "buffer_load_dwordx4 v[100:103], v0, s[28:31], s50 offen offset:0 sc1\n\t" \
  "s_waitcnt vmcnt(6)\n\t" \
  "v_mfma_f32_32x32x16_bf16 v[8:23], v[44:47], v[168:171], v[8:23]\n\t" \
  "v_max3_u32 v6, v6, v44, v45\n\t" \
  "v_max3_u32 v6, v6, v46, v47\n\t" \
  "v_mfma_f32_32x32x16_bf16 v[132:147], v[44:47], v[208:211], v[132:147]\n\t" \
  "s_cbranch_vccnz 54f\n\t" \
  "buffer_load_dwordx4 v[104:107], v0, s[28:31], s50 offen offset:1024 sc1\n\t" \
  "s_waitcnt vmcnt(5)\n\t" \
  "v_mfma_f32_32x32x16_bf16 v[8:23], v[48:51], v[172:175], v[8:23]\n\t" \
  "v_max3_u32 v6, v6, v48, v49\n\t" \
  "v_max3_u32 v6, v6, v50, v51\n\t" \
  "v_cmp_eq_u32 vcc, -1, v6\n\t" \
  "v_mfma_f32_32x32x16_bf16 v[132:147], v[48:51], v[212:215], v[132:147]\n\t" \
  "buffer_load_dwordx4 v[108:111], v0, s[28:31], s50 offen offset:2048 sc1\n\t" \
  "s_waitcnt vmcnt(5)\n\t" \
  "v_mfma_f32_32x32x16_bf16 v[8:23], v[52:55], v[176:179], v[8:23]\n\t" \
  "v_max3_u32 v6, v6, v52, v53\n\t" \
  "v_max3_u32 v6, v6, v54, v55\n\t" \
  "v_mfma_f32_32x32x16_bf16 v[132:147], v[52:55], v[216:219], v[132:147]\n\t" \
  "s_cbranch_vccnz 54f\n\t" \
  "buffer_load_dwordx4 v[112:115], v0, s[28:31], s50 offen offset:3072 sc1\n\t" \
  "s_waitcnt vmcnt(5)\n\t" \
  "v_mfma_f32_32x32x16_bf16 v[8:23], v[60:63], v[184:187], v[8:23]\n\t" \
  "v_max3_u32 v6, v6, v60, v61\n\t" \
  "v_max3_u32 v6, v6, v62, v63\n\t" \
  "v_cmp_eq_u32 vcc, -1, v6\n\t" \
  "v_mfma_f32_32x32x16_bf16 v[132:147], v[60:63], v[224:227], v[132:147]\n\t" \
  "buffer_load_dwordx4 v[116:119], v0, s[28:31], s51 offen offset:0 sc1\n\t" \
  "s_nop 3\n\t" \
  "s_cbranch_vccnz 54f\n\t" \
  "52:\n\t" \
  "v_mfma_f32_32x32x16_bf16 v[172:187], v[24:27], v[228:231], 0\n\t" \
  "buffer_load_dwordx4 v[120:123], v0, s[28:31], s51 offen offset:1024 sc1\n\t" \
  "s_nop 3\n\t" \
  "v_mfma_f32_32x32x16_bf16 v[172:187], v[28:31], v[232:235], v[172:187]\n\t" \
  "ds_write_addtid_b32 v8 offset:0\n\t" \
  "ds_write_addtid_b32 v9 offset:256\n\t" \
  "ds_write_addtid_b32 v10 offset:512\n\t" \
  "ds_write_addtid_b32 v11 offset:768\n\t" \
  "v_mfma_f32_32x32x16_bf16 v[172:187], v[32:35], v[236:239], v[172:187]\n\t" \
  "buffer_load_dwordx4 v[124:127], v0, s[28:31], s51 offen offset:2048 sc1\n\t" \
  "ds_write_addtid_b32 v12 offset:1024\n\t" \
  "ds_write_addtid_b32 v13 offset:1280\n\t" \
  "ds_write_addtid_b32 v14 offset:1536\n\t" \
  "ds_write_addtid_b32 v15 offset:1792\n\t" \
  "v_mfma_f32_32x32x16_bf16 v[172:187], v[36:39], v[240:243], v[172:187]\n\t" \
  "ds_write_addtid_b32 v16 offset:2048\n\t" \
  "ds_write_addtid_b32 v17 offset:2304\n\t" \
  "ds_write_addtid_b32 v18 offset:2560\n\t" \
  "ds_write_addtid_b32 v19 offset:2816\n\t" \
  "v_mfma_f32_32x32x16_bf16 v[172:187], v[40:43], v[244:247], v[172:187]\n\t" \
  "buffer_load_dwordx4 v[128:131], v0, s[28:31], s51 offen offset:3072 sc1\n\t" \
  "ds_write_addtid_b32 v20 offset:3072\n\t" \
  "ds_write_addtid_b32 v21 offset:3328\n\t" \
  "ds_write_addtid_b32 v22 offset:3584\n\t" \
  "ds_write_addtid_b32 v23 offset:3840\n\t" \
  "v_mfma_f32_32x32x16_bf16 v[172:187], v[44:47], v[248:251], v[172:187]\n\t" \
  "ds_write_addtid_b32 v132 offset:4096\n\t" \
  "ds_write_addtid_b32 v133 offset:4352\n\t" \
  "ds_write_addtid_b32 v134 offset:4608\n\t" \
  "ds_write_addtid_b32 v135 offset:4864\n\t" \
  "v_mfma_f32_32x32x16_bf16 v[172:187], v[48:51], v[252:255], v[172:187]\n\t" \
  "buffer_load_dwordx4 v[148:151], v0, s[28:31], s52 offen offset:0 sc1\n\t" \
  "ds_write_addtid_b32 v136 offset:5120\n\t" \
  "ds_write_addtid_b32 v137 offset:5376\n\t" \
  "ds_write_addtid_b32 v138 offset:5632\n\t" \
  "ds_write_addtid_b32 v139 offset:5888\n\t" \
  "v_mfma_f32_32x32x16_bf16 v[172:187], v[52:55], v[64:67], v[172:187]\n\t" \
  "ds_write_addtid_b32 v140 offset:6144\n\t" \
  "ds_write_addtid_b32 v141 offset:6400\n\t" \
  "ds_write_addtid_b32 v142 offset:6656\n\t" \
  "ds_write_addtid_b32 v143 offset:6912\n\t" \
  "v_mfma_f32_32x32x16_bf16 v[172:187], v[56:59], v[68:71], v[172:187]\n\t" \
  "buffer_load_dwordx4 v[152:155], v0, s[28:31], s52 offen offset:1024 sc1\n\t" \
  "ds_write_addtid_b32 v144 offset:7168\n\t" \
  "ds_write_addtid_b32 v145 offset:7424\n\t" \
  "ds_write_addtid_b32 v146 offset:7680\n\t" \
  "ds_write_addtid_b32 v147 offset:7936\n\t" \
  "v_mfma_f32_32x32x16_bf16 v[172:187], v[60:63], v[72:75], v[172:187]\n\t" \
  "v_mfma_f32_32x32x16_bf16 v[188:203], v[24:27], v[76:79], 0\n\t" \
  "buffer_load_dwordx4 v[156:159], v0, s[28:31], s52 offen offset:2048 sc1\n\t" \
  "s_nop 3\n\t" \
  "s_waitcnt lgkmcnt(0)\n\t" \
  "s_barrier\n\t" \
  "v_mfma_f32_32x32x16_bf16 v[188:203], v[28:31], v[80:83], v[188:203]\n\t" \
  "s_nop 3\n\t" \
  "v_mfma_f32_32x32x16_bf16 v[188:203], v[32:35], v[84:87], v[188:203]\n\t" \
  "buffer_load_dwordx4 v[160:163], v0, s[28:31], s52 offen offset:3072 sc1\n\t" \
  "s_nop 0\n\t" \
  "ds_write_addtid_b32 v172 offset:8192\n\t" \
  "ds_write_addtid_b32 v173 offset:8448\n\t" \
  "ds_write_addtid_b32 v174 offset:8704\n\t" \
  "v_mfma_f32_32x32x16_bf16 v[188:203], v[36:39], v[88:91], v[188:203]\n\t" \
  "ds_write_addtid_b32 v175 offset:8960\n\t" \
  "ds_write_addtid_b32 v176 offset:9216\n\t" \
  "ds_write_addtid_b32 v177 offset:9472\n\t" \
  "v_mfma_f32_32x32x16_bf16 v[188:203], v[40:43], v[92:95], v[188:203]\n\t" \
  "buffer_load_dwordx4 v[164:167], v0, s[28:31], s53 offen offset:0 sc1\n\t" \
  "ds_write_addtid_b32 v178 offset:9728\n\t" \
  "ds_write_addtid_b32 v179 offset:9984\n\t" \
  "ds_write_addtid_b32 v180 offset:10240\n\t" \
  "s_waitcnt vmcnt(13)\n\t" \
  "v_mfma_f32_32x32x16_bf16 v[188:203], v[44:47], v[96:99], v[188:203]\n\t" \
  "ds_write_addtid_b32 v181 offset:10496\n\t" \
  "ds_write_addtid_b32 v182 offset:10752\n\t" \
  "ds_write_addtid_b32 v183 offset:11008\n\t" \
  "s_waitcnt vmcnt(12)\n\t" \
  "v_mfma_f32_32x32x16_bf16 v[188:203], v[48:51], v[100:103], v[188:203]\n\t" \
  "buffer_load_dwordx4 v[168:171], v0, s[28:31], s53 offen offset:1024 sc1\n\t" \
  "ds_write_addtid_b32 v184 offset:11264\n\t" \
  "ds_write_addtid_b32 v185 offset:11520\n\t" \
  "ds_write_addtid_b32 v186 offset:11776\n\t" \
  "s_waitcnt vmcnt(12)\n\t" \
  "v_mfma_f32_32x32x16_bf16 v[188:203], v[52:55], v[104:107], v[188:203]\n\t" \
  "ds_write_addtid_b32 v187 offset:12032\n\t" \
  "s_waitcnt vmcnt(11)\n\t" \
  "v_mfma_f32_32x32x16_bf16 v[188:203], v[56:59], v[108:111], v[188:203]\n\t" \
  "s_waitcnt vmcnt(10)\n\t" \
  "v_mfma_f32_32x32x16_bf16 v[188:203], v[60:63], v[112:115], v[188:203]\n\t" \
  "s_waitcnt vmcnt(9)\n\t" \
  "v_mfma_f32_32x32x16_bf16 v[204:219], v[24:27], v[116:119], 0\n\t" \
  "s_nop 3\n\t" \
  "s_waitcnt lgkmcnt(0)\n\t" \
  "s_barrier\n\t" \
  "s_waitcnt vmcnt(8)\n\t" \
  "v_mfma_f32_32x32x16_bf16 v[204:219], v[28:31], v[120:123], v[204:219]\n\t" \
  "s_nop 3\n\t" \
  "s_waitcnt vmcnt(7)\n\t" \
  "v_mfma_f32_32x32x16_bf16 v[204:219], v[32:35], v[124:127], v[204:219]\n\t" \
  "s_nop 0\n\t" \
  "ds_write_addtid_b32 v188 offset:12288\n\t" \
  "ds_write_addtid_b32 v189 offset:12544\n\t" \
  "ds_write_addtid_b32 v190 offset:12800\n\t" \
  "ds_write_addtid_b32 v191 offset:13056\n\t" \
  "s_waitcnt vmcnt(6)\n\t" \
  "v_mfma_f32_32x32x16_bf16 v[204:219], v[36:39], v[128:131], v[204:219]\n\t" \
  "ds_write_addtid_b32 v192 offset:13312\n\t" \
  "ds_write_addtid_b32 v193 offset:13568\n\t" \
  "ds_write_addtid_b32 v194 offset:13824\n\t" \
  "ds_write_addtid_b32 v195 offset:14080\n\t" \
  "s_waitcnt vmcnt(5)\n\t" \
  "v_mfma_f32_32x32x16_bf16 v[204:219], v[40:43], v[148:151], v[204:219]\n\t" \
  "ds_write_addtid_b32 v196 offset:14336\n\t" \
  "ds_write_addtid_b32 v197 offset:14592\n\t" \
  "ds_write_addtid_b32 v198 offset:14848\n\t" \
  "ds_write_addtid_b32 v199 offset:15104\n\t" \
  "s_waitcnt vmcnt(4)\n\t" \
  "v_mfma_f32_32x32x16_bf16 v[204:219], v[44:47], v[152:155], v[204:219]\n\t" \
  "ds_write_addtid_b32 v200 offset:15360\n\t" \
  "ds_write_addtid_b32 v201 offset:15616\n\t" \
  "ds_write_addtid_b32 v202 offset:15872\n\t" \
  "ds_write_addtid_b32 v203 offset:16128\n\t" \
  "s_waitcnt vmcnt(3)\n\t" \
  "v_mfma_f32_32x32x16_bf16 v[204:219], v[48:51], v[156:159], v[204:219]\n\t" \
  "s_waitcnt vmcnt(2)\n\t" \
  "v_mfma_f32_32x32x16_bf16 v[204:219], v[52:55], v[160:163], v[204:219]\n\t" \
  "s_waitcnt vmcnt(1)\n\t" \
  "v_mfma_f32_32x32x16_bf16 v[204:219], v[56:59], v[164:167], v[204:219]\n\t" \
  "s_waitcnt vmcnt(0)\n\t" \
  "v_mfma_f32_32x32x16_bf16 v[204:219], v[60:63], v[168:171], v[204:219]\n\t" \
  "s_waitcnt lgkmcnt(0)\n\t" \
  "s_barrier\n\t" \
  "s_nop 9\n\t" \
  "ds_write_addtid_b32 v204 offset:16384\n\t" \
  "ds_write_addtid_b32 v205 offset:16640\n\t" \
  "ds_write_addtid_b32 v206 offset:16896\n\t" \
  "ds_write_addtid_b32 v207 offset:17152\n\t" \
  "ds_write_addtid_b32 v208 offset:17408\n\t" \
  "ds_write_addtid_b32 v209 offset:17664\n\t" \
  "ds_write_addtid_b32 v210 offset:17920\n\t" \
  "ds_write_addtid_b32 v211 offset:18176\n\t" \
  "ds_write_addtid_b32 v212 offset:18432\n\t" \
  "ds_write_addtid_b32 v213 offset:18688\n\t" \
  "ds_write_addtid_b32 v214 offset:18944\n\t" \
  "ds_write_addtid_b32 v215 offset:19200\n\t" \
  "ds_write_addtid_b32 v216 offset:19456\n\t" \
  "ds_write_addtid_b32 v217 offset:19712\n\t" \
  "ds_write_addtid_b32 v218 offset:19968\n\t" \
  "ds_write_addtid_b32 v219 offset:20224\n\t" \
  "s_waitcnt lgkmcnt(0)\n\t" \
  "s_barrier\n\t" \
  "s_branch 99f\n\t" \
    \
  "50:\n\t" \
  "s_setprio 3\n\t" \
  "s_add_u32 s39, s43, 24576\n\t" \
  "buffer_load_dwordx4 v[64:67], v0, s[28:31], s39 offen offset:3072 sc1\n\t" \
  "s_nop 7\n\t" \
  "s_add_u32 s39, s43, 28672\n\t" \
  "buffer_load_dwordx4 v[68:71], v0, s[28:31], s39 offen offset:0 sc1\n\t" \
  "s_nop 7\n\t" \
  "buffer_load_dwordx4 v[72:75], v0, s[28:31], s39 offen offset:1024 sc1\n\t" \
  "s_nop 7\n\t" \
  "buffer_load_dwordx4 v[76:79], v0, s[28:31], s39 offen offset:2048 sc1\n\t" \
  "s_nop 7\n\t" \
  "buffer_load_dwordx4 v[80:83], v0, s[28:31], s39 offen offset:3072 sc1\n\t" \
  "s_nop 7\n\t" \
  "s_add_u32 s39, s43, 32768\n\t" \
  "buffer_load_dwordx4 v[84:87], v0, s[28:31], s39 offen offset:0 sc1\n\t" \
  "s_nop 7\n\t" \
  "buffer_load_dwordx4 v[88:91], v0, s[28:31], s39 offen offset:1024 sc1\n\t" \
  "s_nop 7\n\t" \
  "buffer_load_dwordx4 v[92:95], v0, s[28:31], s39 offen offset:2048 sc1\n\t" \
  "s_nop 7\n\t" \
  "buffer_load_dwordx4 v[96:99], v0, s[28:31], s39 offen offset:3072 sc1\n\t" \
  "s_nop 7\n\t" \
  "s_add_u32 s39, s43, 36864\n\t" \
  "buffer_load_dwordx4 v[100:103], v0, s[28:31], s39 offen offset:0 sc1\n\t" \
  "s_nop 7\n\t" \
  "buffer_load_dwordx4 v[104:107], v0, s[28:31], s39 offen offset:1024 sc1\n\t" \
  "s_nop 7\n\t" \
  "buffer_load_dwordx4 v[108:111], v0, s[28:31], s39 offen offset:2048 sc1\n\t" \
  "s_nop 7\n\t" \
  "buffer_load_dwordx4 v[112:115], v0, s[28:31], s39 offen offset:3072 sc1\n\t" \
  "s_nop 7\n\t" \
  "s_setprio 3\n\t" \
  "v_lshrrev_b32 v3, 4, v0\n\t" \
  "v_and_b32 v3, 32, v3\n\t" \
  "v_xor_b32 v2, v3, v0\n\t" \
  "v_add_u32 v2, s47, v2\n\t" \
  "v_xor_b32 v3, 64, v2\n\t" \
  "v_xor_b32 v4, 0x80, v2\n\t" \
  "v_xor_b32 v5, 0xc0, v2\n\t" \
  "s_mov_b32 s40, 0x40000\n\t" \
  "s_waitcnt vmcnt(13)\n\t" \
  "55:\n\t" \
  "s_add_u32 s39, s46, 0\n\t" \
  "buffer_load_dwordx4 v[24:27], v0, s[28:31], s39 offen sc1\n\t" \
  "s_nop 7\n\t" \
  "buffer_load_dwordx4 v[28:31], v0, s[28:31], s39 offen offset:1024 sc1\n\t" \
  "s_nop 7\n\t" \
  "buffer_load_dwordx2 v[56:57], v1, s[28:31], s39 offen offset:2048 sc1\n\t" \
  "s_nop 7\n\t" \
  "s_add_u32 s39, s46, 2560\n\t" \
  "buffer_load_dwordx4 v[32:35], v0, s[28:31], s39 offen sc1\n\t" \
  "s_nop 7\n\t" \
  "buffer_load_dwordx4 v[36:39], v0, s[28:31], s39 offen offset:1024 sc1\n\t" \
  "s_nop 7\n\t" \
  "buffer_load_dwordx2 v[58:59], v1, s[28:31], s39 offen offset:2048 sc1\n\t" \
  "s_nop 7\n\t" \
  "s_add_u32 s39, s46, 5120\n\t" \
  "buffer_load_dwordx4 v[40:43], v0, s[28:31], s39 offen sc1\n\t" \
  "s_nop 7\n\t" \
  "buffer_load_dwordx4 v[44:47], v0, s[28:31], s39 offen offset:1024 sc1\n\t" \
  "s_nop 7\n\t" \
  "buffer_load_dwordx2 v[60:61], v1, s[28:31], s39 offen offset:2048 sc1\n\t" \
  "s_nop 7\n\t" \
  "s_add_u32 s39, s46, 7680\n\t" \
  "buffer_load_dwordx4 v[48:51], v0, s[28:31], s39 offen sc1\n\t" \
  "s_nop 7\n\t" \
  "buffer_load_dwordx4 v[52:55], v0, s[28:31], s39 offen offset:1024 sc1\n\t" \
  "s_nop 7\n\t" \
  "buffer_load_dwordx2 v[62:63], v1, s[28:31], s39 offen offset:2048 sc1\n\t" \
  "s_nop 7\n\t" \
  "s_waitcnt vmcnt(0)\n\t" \
  "v_max3_u32 v6, v24, v25, v26\n\t" \
  "v_max_u32 v6, v6, v27\n\t" \
  "v_max3_u32 v6, v6, v28, v29\n\t" \
  "v_max3_u32 v6, v6, v30, v31\n\t" \
  "v_max3_u32 v6, v6, v32, v33\n\t" \
  "v_max3_u32 v6, v6, v34, v35\n\t" \
  "v_max3_u32 v6, v6, v36, v37\n\t" \
  "v_max3_u32 v6, v6, v38, v39\n\t" \
  "v_max3_u32 v6, v6, v40, v41\n\t" \
  "v_max3_u32 v6, v6, v42, v43\n\t" \
  "v_max3_u32 v6, v6, v44, v45\n\t" \
  "v_max3_u32 v6, v6, v46, v47\n\t" \
  "v_max3_u32 v6, v6, v48, v49\n\t" \
  "v_max3_u32 v6, v6, v50, v51\n\t" \
  "v_max3_u32 v6, v6, v52, v53\n\t" \
  "v_max3_u32 v6, v6, v54, v55\n\t" \
  "v_max3_u32 v6, v6, v56, v57\n\t" \
  "v_max3_u32 v6, v6, v58, v59\n\t" \
  "v_max3_u32 v6, v6, v60, v61\n\t" \
  "v_max3_u32 v6, v6, v62, v63\n\t" \
  "v_cmp_eq_u32 vcc, -1, v6\n\t" \
  "s_nop 4\n\t" \
  "s_cbranch_vccz 56f\n\t" \
  "s_sleep 4\n\t" \
  "s_sub_u32 s40, s40, 1\n\t" \
  "s_cmp_lg_u32 s40, 0\n\t" \
  "s_cbranch_scc1 55b\n\t" \
  "56:\n\t" \
  "v_mfma_f32_32x32x16_bf16 v[8:23], v[148:151], v[24:27], 0\n\t" \
  "v_mfma_f32_32x32x16_bf16 v[8:23], v[152:155], v[28:31], v[8:23]\n\t" \
  "s_add_u32 s39, s43, 40960\n\t" \
  "buffer_load_dwordx4 v[116:119], v0, s[28:31], s39 offen offset:0 sc1\n\t" \
  "buffer_load_dwordx4 v[120:123], v0, s[28:31], s39 offen offset:1024 sc1\n\t" \
  "buffer_load_dwordx4 v[124:127], v0, s[28:31], s39 offen offset:2048 sc1\n\t" \
  "buffer_load_dwordx4 v[128:131], v0, s[28:31], s39 offen offset:3072 sc1\n\t" \
  "s_add_u32 s39, s43, 45056\n\t" \
  "buffer_load_dwordx4 v[132:135], v0, s[28:31], s39 offen offset:0 sc1\n\t" \
  "buffer_load_dwordx4 v[136:139], v0, s[28:31], s39 offen offset:1024 sc1\n\t" \
  "buffer_load_dwordx4 v[140:143], v0, s[28:31], s39 offen offset:2048 sc1\n\t" \
  "buffer_load_dwordx4 v[144:147], v0, s[28:31], s39 offen offset:3072 sc1\n\t" \
  "s_add_u32 s39, s43, 49152\n\t" \
  "buffer_load_dwordx4 v[148:151], v0, s[28:31], s39 offen offset:0 sc1\n\t" \
  "buffer_load_dwordx4 v[152:155], v0, s[28:31], s39 offen offset:1024 sc1\n\t" \
  "s_barrier\n\t" \
  "v_mfma_f32_32x32x16_bf16 v[8:23], v[156:159], v[32:35], v[8:23]\n\t" \
  "v_mfma_f32_32x32x16_bf16 v[8:23], v[160:163], v[36:39], v[8:23]\n\t" \
  "v_mfma_f32_32x32x16_bf16 v[8:23], v[164:167], v[40:43], v[8:23]\n\t" \
  "v_mfma_f32_32x32x16_bf16 v[8:23], v[168:171], v[44:47], v[8:23]\n\t" \
  "v_mfma_f32_32x32x16_bf16 v[8:23], v[172:175], v[48:51], v[8:23]\n\t" \
  "v_mfma_f32_32x32x16_bf16 v[8:23], v[176:179], v[52:55], v[8:23]\n\t" \
  "v_mfma_f32_32x32x16_bf16 v[8:23], v[180:183], v[56:59], v[8:23]\n\t" \
  "v_mfma_f32_32x32x16_bf16 v[8:23], v[184:187], v[60:63], v[8:23]\n\t" \
  "v_mfma_f32_32x32x16_bf16 v[156:171], v[188:191], v[24:27], 0\n\t" \
  "s_nop 3\n\t" \
  "v_mfma_f32_32x32x16_bf16 v[156:171], v[192:195], v[28:31], v[156:171]\n\t" \
  "s_nop 3\n\t" \
  "v_mfma_f32_32x32x16_bf16 v[156:171], v[196:199], v[32:35], v[156:171]\n\t" \
  "s_nop 1\n\t" \
  "ds_write_b128 v2, v[8:11] offset:0\n\t" \
  "v_mfma_f32_32x32x16_bf16 v[156:171], v[200:203], v[36:39], v[156:171]\n\t" \
  "v_mfma_f32_32x32x16_bf16 v[156:171], v[204:207], v[40:43], v[156:171]\n\t" \
  "ds_write_b128 v3, v[12:15] offset:1024\n\t" \
  "v_mfma_f32_32x32x16_bf16 v[156:171], v[208:211], v[44:47], v[156:171]\n\t" \
  "v_mfma_f32_32x32x16_bf16 v[156:171], v[212:215], v[48:51], v[156:171]\n\t" \
  "ds_write_b128 v4, v[16:19] offset:2048\n\t" \
  "v_mfma_f32_32x32x16_bf16 v[156:171], v[216:219], v[52:55], v[156:171]\n\t" \
  "v_mfma_f32_32x32x16_bf16 v[156:171], v[220:223], v[56:59], v[156:171]\n\t" \
  "ds_write_b128 v5, v[20:23] offset:3072\n\t" \
  "v_mfma_f32_32x32x16_bf16 v[156:171], v[224:227], v[60:63], v[156:171]\n\t" \
  "v_mfma_f32_32x32x16_bf16 v[172:187], v[228:231], v[24:27], 0\n\t" \
  "s_nop 3\n\t" \
  "v_mfma_f32_32x32x16_bf16 v[172:187], v[232:235], v[28:31], v[172:187]\n\t" \
  "s_nop 3\n\t" \
  "v_mfma_f32_32x32x16_bf16 v[172:187], v[236:239], v[32:35], v[172:187]\n\t" \
  "s_nop 1\n\t" \
  "ds_write_b128 v2, v[156:159] offset:4096\n\t" \
  "v_mfma_f32_32x32x16_bf16 v[172:187], v[240:243], v[36:39], v[172:187]\n\t" \
  "v_mfma_f32_32x32x16_bf16 v[172:187], v[244:247], v[40:43], v[172:187]\n\t" \
  "ds_write_b128 v3, v[160:163] offset:5120\n\t" \
  "v_mfma_f32_32x32x16_bf16 v[172:187], v[248:251], v[44:47], v[172:187]\n\t" \
  "v_mfma_f32_32x32x16_bf16 v[172:187], v[252:255], v[48:51], v[172:187]\n\t" \
  "ds_write_b128 v4, v[164:167] offset:6144\n\t" \
  "v_mfma_f32_32x32x16_bf16 v[172:187], v[64:67], v[52:55], v[172:187]\n\t" \
  "v_mfma_f32_32x32x16_bf16 v[172:187], v[68:71], v[56:59], v[172:187]\n\t" \
  "ds_write_b128 v5, v[168:171] offset:7168\n\t" \
  "v_mfma_f32_32x32x16_bf16 v[172:187], v[72:75], v[60:63], v[172:187]\n\t" \
  "v_mfma_f32_32x32x16_bf16 v[188:203], v[76:79], v[24:27], 0\n\t" \
  "s_nop 3\n\t" \
  "v_mfma_f32_32x32x16_bf16 v[188:203], v[80:83], v[28:31], v[188:203]\n\t" \
  "s_nop 3\n\t" \
  "v_mfma_f32_32x32x16_bf16 v[188:203], v[84:87], v[32:35], v[188:203]\n\t" \
  "s_nop 1\n\t" \
  "ds_write_b128 v2, v[172:175] offset:8192\n\t" \
  "v_mfma_f32_32x32x16_bf16 v[188:203], v[88:91], v[36:39], v[188:203]\n\t" \
  "v_mfma_f32_32x32x16_bf16 v[188:203], v[92:95], v[40:43], v[188:203]\n\t" \
  "ds_write_b128 v3, v[176:179] offset:9216\n\t" \
  "v_mfma_f32_32x32x16_bf16 v[188:203], v[96:99], v[44:47], v[188:203]\n\t" \
  "v_mfma_f32_32x32x16_bf16 v[188:203], v[100:103], v[48:51], v[188:203]\n\t" \
  "ds_write_b128 v4, v[180:183] offset:10240\n\t" \
  "v_mfma_f32_32x32x16_bf16 v[188:203], v[104:107], v[52:55], v[188:203]\n\t" \
  "v_mfma_f32_32x32x16_bf16 v[188:203], v[108:111], v[56:59], v[188:203]\n\t" \
  "ds_write_b128 v5, v[184:187] offset:11264\n\t" \
  "v_mfma_f32_32x32x16_bf16 v[188:203], v[112:115], v[60:63], v[188:203]\n\t" \
  "s_waitcnt vmcnt(0)\n\t" \
  "v_mfma_f32_32x32x16_bf16 v[204:219], v[116:119], v[24:27], 0\n\t" \
  "s_nop 3\n\t" \
  "v_mfma_f32_32x32x16_bf16 v[204:219], v[120:123], v[28:31], v[204:219]\n\t" \
  "s_nop 3\n\t" \
  "v_mfma_f32_32x32x16_bf16 v[204:219], v[124:127], v[32:35], v[204:219]\n\t" \
  "s_nop 1\n\t" \
  "ds_write_b128 v2, v[188:191] offset:12288\n\t" \
  "v_mfma_f32_32x32x16_bf16 v[204:219], v[128:131], v[36:39], v[204:219]\n\t" \
  "ds_write_b128 v3, v[192:195] offset:13312\n\t" \
  "v_mfma_f32_32x32x16_bf16 v[204:219], v[132:135], v[40:43], v[204:219]\n\t" \
  "ds_write_b128 v4, v[196:199] offset:14336\n\t" \
  "v_mfma_f32_32x32x16_bf16 v[204:219], v[136:139], v[44:47], v[204:219]\n\t" \
  "ds_write_b128 v5, v[200:203] offset:15360\n\t" \
  "v_mfma_f32_32x32x16_bf16 v[204:219], v[140:143], v[48:51], v[204:219]\n\t" \
  "v_mfma_f32_32x32x16_bf16 v[204:219], v[144:147], v[52:55], v[204:219]\n\t" \
  "v_mfma_f32_32x32x16_bf16 v[204:219], v[148:151], v[56:59], v[204:219]\n\t" \
  "v_mfma_f32_32x32x16_bf16 v[204:219], v[152:155], v[60:63], v[204:219]\n\t" \
  "s_nop 11\n\t" \
  "ds_write_b128 v2, v[204:207] offset:16384\n\t" \
  "ds_write_b128 v3, v[208:211] offset:17408\n\t" \
  "ds_write_b128 v4, v[212:215] offset:18432\n\t" \
  "ds_write_b128 v5, v[216:219] offset:19456\n\t" \
    \
  "v_lshrrev_b32 v2, 8, v0\n\t" \
  "v_bfe_u32 v3, v0, 7, 1\n\t" \
  "v_lshl_add_u32 v2, v3, 2, v2\n\t" \
  "s_lshl_b32 s50, s3, 3\n\t" \
  "v_add_u32 v2, s50, v2\n\t" \
  "v_bfe_u32 v3, v0, 4, 3\n\t" \
  "v_lshlrev_b32 v4, 1, v3\n\t" \
  "v_xor_b32 v4, v4, v2\n\t" \
  "v_lshlrev_b32 v4, 4, v4\n\t" \
  "v_lshl_add_u32 v4, v3, 9, v4\n\t" \
  "v_add_u32 v5, 122880, v4\n\t" \
  "v_add_u32 v4, 81920, v4\n\t" \
  "s_lshl_b32 s50, s3, 10\n\t" \
  "v_add_u32 v0, s50, v0\n\t" \
  "v_add_u32 v1, 40960, v0\n\t" \
  "v_mul_u32_u24 v2, 1280, v2\n\t" \
  "v_lshl_add_u32 v2, v3, 3, v2\n\t" \
  "s_setprio 3\n\t" \
  "s_waitcnt lgkmcnt(0)\n\t" \
  "s_barrier\n\t" \
  "ds_read_b128 v[64:67], v4 offset:0\n\t" \
  "ds_read_b128 v[68:71], v4 offset:20480\n\t" \
  "ds_read_b128 v[72:75], v5 offset:0\n\t" \
  "ds_read_b128 v[76:79], v5 offset:20480\n\t" \
  "ds_read_b128 v[80:83], v4 offset:4096\n\t" \
  "ds_read_b128 v[84:87], v4 offset:24576\n\t" \
  "ds_read_b128 v[88:91], v5 offset:4096\n\t" \
  "ds_read_b128 v[92:95], v5 offset:24576\n\t" \
  "s_waitcnt lgkmcnt(4)\n\t" \
  "v_add_f32 v64, v64, v68\n\t" \
  "v_add_f32 v65, v65, v69\n\t" \
  "v_add_f32 v66, v66, v70\n\t" \
  "v_add_f32 v67, v67, v71\n\t" \
  "v_add_f32 v72, v72, v76\n\t" \
  "v_add_f32 v73, v73, v77\n\t" \
  "v_add_f32 v74, v74, v78\n\t" \
  "v_add_f32 v75, v75, v79\n\t" \
  "v_add_f32 v44, v64, v72\n\t" \
  "v_add_f32 v45, v65, v73\n\t" \
  "v_add_f32 v46, v66, v74\n\t" \
  "v_add_f32 v47, v67, v75\n\t" \
  "ds_read_b128 v[96:99], v4 offset:8192\n\t" \
  "ds_read_b128 v[100:103], v4 offset:28672\n\t" \
  "ds_read_b128 v[104:107], v5 offset:8192\n\t" \
  "ds_read_b128 v[108:111], v5 offset:28672\n\t" \
  "s_waitcnt lgkmcnt(4)\n\t" \
  "v_add_f32 v80, v80, v84\n\t" \
  "v_add_f32 v81, v81, v85\n\t" \
  "v_add_f32 v82, v82, v86\n\t" \
  "v_add_f32 v83, v83, v87\n\t" \
  "v_add_f32 v88, v88, v92\n\t" \
  "v_add_f32 v89, v89, v93\n\t" \
  "v_add_f32 v90, v90, v94\n\t" \
  "v_add_f32 v91, v91, v95\n\t" \
  "v_add_f32 v48, v80, v88\n\t" \
  "v_add_f32 v49, v81, v89\n\t" \
  "v_add_f32 v50, v82, v90\n\t" \
  "v_add_f32 v51, v83, v91\n\t" \
  "ds_read_b128 v[112:115], v4 offset:12288\n\t" \
  "ds_read_b128 v[116:119], v4 offset:32768\n\t" \
  "ds_read_b128 v[120:123], v5 offset:12288\n\t" \
  "ds_read_b128 v[124:127], v5 offset:32768\n\t" \
  "s_waitcnt lgkmcnt(4)\n\t" \
  "v_add_f32 v96, v96, v100\n\t" \
  "v_add_f32 v97, v97, v101\n\t" \
  "v_add_f32 v98, v98, v102\n\t" \
  "v_add_f32 v99, v99, v103\n\t" \
  "v_add_f32 v104, v104, v108\n\t" \
  "v_add_f32 v105, v105, v109\n\t" \
  "v_add_f32 v106, v106, v110\n\t" \
  "v_add_f32 v107, v107, v111\n\t" \
  "v_add_f32 v52, v96, v104\n\t" \
  "v_add_f32 v53, v97, v105\n\t" \
  "v_add_f32 v54, v98, v106\n\t" \
  "v_add_f32 v55, v99, v107\n\t" \
  "ds_read_b128 v[128:131], v4 offset:16384\n\t" \
  "ds_read_b128 v[132:135], v4 offset:36864\n\t" \
  "ds_read_b128 v[136:139], v5 offset:16384\n\t" \
  "ds_read_b128 v[140:143], v5 offset:36864\n\t" \
  "s_waitcnt lgkmcnt(4)\n\t" \
  "v_add_f32 v112, v112, v116\n\t" \
  "v_add_f32 v113, v113, v117\n\t" \
  "v_add_f32 v114, v114, v118\n\t" \
  "v_add_f32 v115, v115, v119\n\t" \
  "v_add_f32 v120, v120, v124\n\t" \
  "v_add_f32 v121, v121, v125\n\t" \
  "v_add_f32 v122, v122, v126\n\t" \
  "v_add_f32 v123, v123, v127\n\t" \
  "v_add_f32 v56, v112, v120\n\t" \
  "v_add_f32 v57, v113, v121\n\t" \
  "v_add_f32 v58, v114, v122\n\t" \
  "v_add_f32 v59, v115, v123\n\t" \
  "s_waitcnt lgkmcnt(0)\n\t" \
  "v_add_f32 v128, v128, v132\n\t" \
  "v_add_f32 v129, v129, v133\n\t" \
  "v_add_f32 v130, v130, v134\n\t" \
  "v_add_f32 v131, v131, v135\n\t" \
  "v_add_f32 v136, v136, v140\n\t" \
  "v_add_f32 v137, v137, v141\n\t" \
  "v_add_f32 v138, v138, v142\n\t" \
  "v_add_f32 v139, v139, v143\n\t" \
  "v_add_f32 v60, v128, v136\n\t" \
  "v_add_f32 v61, v129, v137\n\t" \
  "v_add_f32 v62, v130, v138\n\t" \
  "v_add_f32 v63, v131, v139\n\t" \
  "s_barrier\n\t" \
  "ds_read_b128 v[64:67], v0 offset:0\n\t" \
  "ds_read_b128 v[68:71], v0 offset:20480\n\t" \
  "ds_read_b128 v[72:75], v1 offset:0\n\t" \
  "ds_read_b128 v[76:79], v1 offset:20480\n\t" \
  "s_waitcnt lgkmcnt(3)\n\t" \
  "v_add_f32 v44, v44, v64\n\t" \
  "v_add_f32 v45, v45, v65\n\t" \
  "v_add_f32 v46, v46, v66\n\t" \
  "v_add_f32 v47, v47, v67\n\t" \
  "s_waitcnt lgkmcnt(1)\n\t" \
  "v_add_f32 v68, v68, v72\n\t" \
  "v_add_f32 v69, v69, v73\n\t" \
  "v_add_f32 v70, v70, v74\n\t" \
  "v_add_f32 v71, v71, v75\n\t" \
  "v_add_f32 v44, v44, v68\n\t" \
  "v_add_f32 v45, v45, v69\n\t" \
  "v_add_f32 v46, v46, v70\n\t" \
  "v_add_f32 v47, v47, v71\n\t" \
  "s_waitcnt lgkmcnt(0)\n\t" \
  "v_add_f32 v44, v44, v76\n\t" \
  "v_add_f32 v45, v45, v77\n\t" \
  "v_add_f32 v46, v46, v78\n\t" \
  "v_add_f32 v47, v47, v79\n\t" \
  "v_cvt_pk_bf16_f32 v44, v44, v45\n\t" \
  "v_cvt_pk_bf16_f32 v45, v46, v47\n\t" \
  "global_store_dwordx2 v2, v[44:45], s[22:23] offset:0 sc1\n\t" \
  "ds_read_b128 v[64:67], v0 offset:4096\n\t" \
  "ds_read_b128 v[68:71], v0 offset:24576\n\t" \
  "ds_read_b128 v[72:75], v1 offset:4096\n\t" \
  "ds_read_b128 v[76:79], v1 offset:24576\n\t" \
  "s_waitcnt lgkmcnt(3)\n\t" \
  "v_add_f32 v48, v48, v64\n\t" \
  "v_add_f32 v49, v49, v65\n\t" \
  "v_add_f32 v50, v50, v66\n\t" \
  "v_add_f32 v51, v51, v67\n\t" \
  "s_waitcnt lgkmcnt(1)\n\t" \
  "v_add_f32 v68, v68, v72\n\t" \
  "v_add_f32 v69, v69, v73\n\t" \
  "v_add_f32 v70, v70, v74\n\t" \
  "v_add_f32 v71, v71, v75\n\t" \
  "v_add_f32 v48, v48, v68\n\t" \
  "v_add_f32 v49, v49, v69\n\t" \
  "v_add_f32 v50, v50, v70\n\t" \
  "v_add_f32 v51, v51, v71\n\t" \
  "s_waitcnt lgkmcnt(0)\n\t" \
  "v_add_f32 v48, v48, v76\n\t" \
  "v_add_f32 v49, v49, v77\n\t" \
  "v_add_f32 v50, v50, v78\n\t" \
  "v_add_f32 v51, v51, v79\n\t" \
  "v_cvt_pk_bf16_f32 v48, v48, v49\n\t" \
  "v_cvt_pk_bf16_f32 v49, v50, v51\n\t" \
  "global_store_dwordx2 v2, v[48:49], s[22:23] offset:64 sc1\n\t" \
  "s_barrier\n\t" \
  "ds_read_b128 v[64:67], v0 offset:8192\n\t" \
  "ds_read_b128 v[68:71], v0 offset:28672\n\t" \
  "ds_read_b128 v[72:75], v1 offset:8192\n\t" \
  "ds_read_b128 v[76:79], v1 offset:28672\n\t" \
  "s_waitcnt lgkmcnt(3)\n\t" \
  "v_add_f32 v52, v52, v64\n\t" \
  "v_add_f32 v53, v53, v65\n\t" \
  "v_add_f32 v54, v54, v66\n\t" \
  "v_add_f32 v55, v55, v67\n\t" \
  "s_waitcnt lgkmcnt(1)\n\t" \
  "v_add_f32 v68, v68, v72\n\t" \
  "v_add_f32 v69, v69, v73\n\t" \
  "v_add_f32 v70, v70, v74\n\t" \
  "v_add_f32 v71, v71, v75\n\t" \
  "v_add_f32 v52, v52, v68\n\t" \
  "v_add_f32 v53, v53, v69\n\t" \
  "v_add_f32 v54, v54, v70\n\t" \
  "v_add_f32 v55, v55, v71\n\t" \
  "s_waitcnt lgkmcnt(0)\n\t" \
  "v_add_f32 v52, v52, v76\n\t" \
  "v_add_f32 v53, v53, v77\n\t" \
  "v_add_f32 v54, v54, v78\n\t" \
  "v_add_f32 v55, v55, v79\n\t" \
  "v_cvt_pk_bf16_f32 v52, v52, v53\n\t" \
  "v_cvt_pk_bf16_f32 v53, v54, v55\n\t" \
  "global_store_dwordx2 v2, v[52:53], s[22:23] offset:128 sc1\n\t" \
  "s_barrier\n\t" \
  "ds_read_b128 v[64:67], v0 offset:12288\n\t" \
  "ds_read_b128 v[68:71], v0 offset:32768\n\t" \
  "ds_read_b128 v[72:75], v1 offset:12288\n\t" \
  "ds_read_b128 v[76:79], v1 offset:32768\n\t" \
  "s_waitcnt lgkmcnt(3)\n\t" \
  "v_add_f32 v56, v56, v64\n\t" \
  "v_add_f32 v57, v57, v65\n\t" \
  "v_add_f32 v58, v58, v66\n\t" \
  "v_add_f32 v59, v59, v67\n\t" \
  "s_waitcnt lgkmcnt(1)\n\t" \
  "v_add_f32 v68, v68, v72\n\t" \
  "v_add_f32 v69, v69, v73\n\t" \
  "v_add_f32 v70, v70, v74\n\t" \
  "v_add_f32 v71, v71, v75\n\t" \
  "v_add_f32 v56, v56, v68\n\t" \
  "v_add_f32 v57, v57, v69\n\t" \
  "v_add_f32 v58, v58, v70\n\t" \
  "v_add_f32 v59, v59, v71\n\t" \
  "s_waitcnt lgkmcnt(0)\n\t" \
  "v_add_f32 v56, v56, v76\n\t" \
  "v_add_f32 v57, v57, v77\n\t" \
  "v_add_f32 v58, v58, v78\n\t" \
  "v_add_f32 v59, v59, v79\n\t" \
  "v_cvt_pk_bf16_f32 v56, v56, v57\n\t" \
  "v_cvt_pk_bf16_f32 v57, v58, v59\n\t" \
  "global_store_dwordx2 v2, v[56:57], s[22:23] offset:192 sc1\n\t" \
  "s_barrier\n\t" \
  "ds_read_b128 v[64:67], v0 offset:16384\n\t" \
  "ds_read_b128 v[68:71], v0 offset:36864\n\t" \
  "ds_read_b128 v[72:75], v1 offset:16384\n\t" \
  "ds_read_b128 v[76:79], v1 offset:36864\n\t" \
  "s_waitcnt lgkmcnt(3)\n\t" \
  "v_add_f32 v60, v60, v64\n\t" \
  "v_add_f32 v61, v61, v65\n\t" \
  "v_add_f32 v62, v62, v66\n\t" \
  "v_add_f32 v63, v63, v67\n\t" \
  "s_waitcnt lgkmcnt(1)\n\t" \
  "v_add_f32 v68, v68, v72\n\t" \
  "v_add_f32 v69, v69, v73\n\t" \
  "v_add_f32 v70, v70, v74\n\t" \
  "v_add_f32 v71, v71, v75\n\t" \
  "v_add_f32 v60, v60, v68\n\t" \
  "v_add_f32 v61, v61, v69\n\t" \
  "v_add_f32 v62, v62, v70\n\t" \
  "v_add_f32 v63, v63, v71\n\t" \
  "s_waitcnt lgkmcnt(0)\n\t" \
  "v_add_f32 v60, v60, v76\n\t" \
  "v_add_f32 v61, v61, v77\n\t" \
  "v_add_f32 v62, v62, v78\n\t" \
  "v_add_f32 v63, v63, v79\n\t" \
  "v_cvt_pk_bf16_f32 v60, v60, v61\n\t" \
  "v_cvt_pk_bf16_f32 v61, v62, v63\n\t" \
  "global_store_dwordx2 v2, v[60:61], s[22:23] offset:256 sc1\n\t" \
  "s_branch 99f\n\t" \
    \
    \
  "54:\n\t" \
  "s_cmp_eq_u32 s40, 0x40000\n\t" \
  "s_cbranch_scc0 58f\n\t" \
  "s_barrier\n\t" \
  "58:\n\t" \
  "s_sleep 2\n\t" \
  "s_waitcnt vmcnt(0)\n\t" \
  "s_sub_u32 s40, s40, 1\n\t" \
  "s_cmp_lg_u32 s40, 0\n\t" \
  "s_cbranch_scc0 52b\n\t" \
  "s_add_u32 s39, s46, 0\n\t" \
  "buffer_load_dwordx4 v[24:27], v0, s[28:31], s39 offen sc1\n\t" \
  "buffer_load_dwordx4 v[28:31], v0, s[28:31], s39 offen offset:1024 sc1\n\t" \
  "buffer_load_dwordx2 v[56:57], v1, s[28:31], s39 offen offset:2048 sc1\n\t" \
  "s_add_u32 s39, s46, 2560\n\t" \
  "buffer_load_dwordx4 v[32:35], v0, s[28:31], s39 offen sc1\n\t" \
  "buffer_load_dwordx4 v[36:39], v0, s[28:31], s39 offen offset:1024 sc1\n\t" \
  "buffer_load_dwordx2 v[58:59], v1, s[28:31], s39 offen offset:2048 sc1\n\t" \
  "s_add_u32 s39, s46, 5120\n\t" \
  "buffer_load_dwordx4 v[40:43], v0, s[28:31], s39 offen sc1\n\t" \
  "buffer_load_dwordx4 v[44:47], v0, s[28:31], s39 offen offset:1024 sc1\n\t" \
  "buffer_load_dwordx2 v[60:61], v1, s[28:31], s39 offen offset:2048 sc1\n\t" \
  "s_add_u32 s39, s46, 7680\n\t" \
  "buffer_load_dwordx4 v[48:51], v0, s[28:31], s39 offen sc1\n\t" \
  "buffer_load_dwordx4 v[52:55], v0, s[28:31], s39 offen offset:1024 sc1\n\t" \
  "buffer_load_dwordx2 v[62:63], v1, s[28:31], s39 offen offset:2048 sc1\n\t" \
  "s_branch 51b\n\t" \
    \
  "202:\n\t" \
  "s_barrier\n\t" \
  "s_branch 402b\n\t" \
  "203:\n\t" \
  "s_waitcnt vmcnt(0)\n\t" \
  "s_lshl_b32 s51, s4, 31\n\t" \
  "s_xor_b32 s51, s51, 0xbfb8aa3b\n\t" \
  "v_mul_f32 v145, s51, v145\n\t" \
  "v_exp_f32 v145, v145\n\t" \
  "s_nop 0\n\t" \
  "v_add_f32 v145, 1.0, v145\n\t" \
  "v_rcp_f32 v145, v145\n\t" \
  "s_lshr_b32 s50, s3, 2\n\t" \
  "s_and_b32 s51, s3, 3\n\t" \
  "s_lshl2_add_u32 s52, s2, s51\n\t" \
  "s_lshl3_add_u32 s42, s52, 8192\n\t" \
  "s_lshl3_add_u32 s53, s6, s3\n\t" \
  "s_mul_i32 s43, s53, 51200\n\t" \
  "s_add_u32 s43, s43, 393216\n\t" \
  "s_lshr_b32 s54, s2, 5\n\t" \
  "s_mul_i32 s44, s54, 2944\n\t" \
  "s_add_u32 s44, s44, 27648\n\t" \
  "s_and_b32 s54, s3, 1\n\t" \
  "s_lshl1_add_u32 s54, s5, s54\n\t" \
  "s_mul_i32 s45, s54, 81920\n\t" \
  "s_lshl1_add_u32 s54, s50, s4\n\t" \
  "s_lshl2_add_u32 s54, s54, s6\n\t" \
  "s_bfe_u32 s55, s3, 0x10001\n\t" \
  "s_lshl1_add_u32 s54, s54, s55\n\t" \
  "s_mul_i32 s54, s54, 2560\n\t" \
  "s_add_u32 s45, s45, s54\n\t" \
  "s_add_u32 s45, s45, 2031616\n\t" \
  "s_lshl1_add_u32 s54, s5, s4\n\t" \
  "s_lshl3_add_u32 s54, s54, s3\n\t" \
  "s_mul_i32 s46, s54, 10240\n\t" \
  "s_add_u32 s46, s46, 2031616\n\t" \
  "s_xor_b32 s47, s3, 4\n\t" \
  "s_mul_i32 s47, s47, 20480\n\t" \
  "s_mul_i32 s53, s53, 50\n\t" \
  "v_add_u32 v8, s53, v1\n\t" \
  "v_and_b32 v8, 0x3ff, v8\n\t" \
  "s_and_b32 s54, s2, 230\n\t" \
  "s_lshl_b32 s55, s55, 0\n\t" \
  "s_or_b32 s54, s54, s55\n\t" \
  "s_and_b32 s56, s3, 1\n\t" \
  "s_lshl4_add_u32 s54, s56, s54\n\t" \
  "s_lshl2_add_u32 s54, s54, s4\n\t" \
  "v_and_b32 v9, 2, v1\n\t" \
  "v_lshlrev_b32 v9, 4, v9\n\t" \
  "v_and_b32 v10, 1, v1\n\t" \
  "v_lshl_add_u32 v9, v10, 1, v9\n\t" \
  "v_add_u32 v9, s54, v9\n\t" \
  "v_cmp_gt_u32 vcc, 56, v1\n\t" \
  "v_mov_b32 v10, s52\n\t" \
  "s_nop 0\n\t" \
  "v_cndmask_b32 v9, v10, v9, vcc\n\t" \
  "v_cmp_gt_u32 vcc, 50, v1\n\t" \
  "s_nop 1\n\t" \
  "v_cndmask_b32 v8, v9, v8, vcc\n\t" \
  "v_lshlrev_b32 v8, 3, v8\n\t" \
  "v_add_u32 v147, 8192, v8\n\t" \
  "v_min_u32 v146, 22, v1\n\t" \
  "v_lshlrev_b32 v146, 7, v146\n\t" \
  "v_mov_b32 v0, v2\n\t" \
  "v_lshlrev_b32 v1, 3, v1\n\t" \
  "v_add_u32 v14, s45, v0\n\t" \
  "v_add_u32 v15, s45, v1\n\t" \
  "s_lshl1_add_u32 s50, s5, s4\n\t" \
  "s_mul_i32 s50, s50, 40960\n\t" \
  "s_mul_i32 s51, s6, 320\n\t" \
  "s_add_u32 s50, s50, s51\n\t" \
  "s_add_u32 s22, s22, s50\n\t" \
  "s_addc_u32 s23, s23, 0\n\t" \
  "s_branch 403b\n\t" \
  "302:\n\t" \
  "s_lshl_b32 s51, s4, 31\n\t" \
  "s_xor_b32 s51, s51, 0xbfb8aa3b\n\t" \
  "v_mul_f32 v145, s51, v145\n\t" \
  "v_exp_f32 v145, v145\n\t" \
  "s_nop 0\n\t" \
  "v_add_f32 v145, 1.0, v145\n\t" \
  "v_rcp_f32 v145, v145\n\t" \
  "s_waitcnt vmcnt(0)\n\t" \
  "v_lshl_or_b32 v168, v176, 16, v168\n\t" \
  "v_lshl_or_b32 v169, v177, 16, v169\n\t" \
  "v_lshl_or_b32 v170, v178, 16, v170\n\t" \
  "v_lshl_or_b32 v171, v179, 16, v171\n\t" \
  "v_lshl_or_b32 v172, v180, 16, v172\n\t" \
  "v_lshl_or_b32 v173, v181, 16, v173\n\t" \
  "v_lshl_or_b32 v174, v182, 16, v174\n\t" \
  "v_lshl_or_b32 v175, v183, 16, v175\n\t" \
  "buffer_store_dwordx4 v[168:171], v210, s[28:31], 0 offen sc1\n\t" \
  "buffer_store_dwordx4 v[172:175], v211, s[28:31], 0 offen sc1\n\t" \
  "s_branch 402b\n\t" \
  "304:\n\t" \
  "v_mov_b32 v6, s42\n\t" \
  "v_mov_b64 v[4:5], s[64:65]\n\t" \
  "s_mov_b64 exec, 1\n\t" \
  "s_waitcnt vmcnt(0)\n\t" \
  "buffer_store_dwordx2 v[4:5], v6, s[28:31], 0 offen sc1\n\t" \
  "s_mov_b64 exec, -1\n\t" \
  "s_cmp_eq_u32 s42, 8192\n\t" \
  "s_cbranch_scc0 404b\n\t" \
  "v_add_u32 v6, 5120, v2\n\t" \
  "s_mov_b32 s39, 0x40000\n\t" \
  "32:\n\t" \
  "buffer_load_dwordx4 v[8:11], v6, s[28:31], 0 offen sc1\n\t" \
  "s_waitcnt vmcnt(0)\n\t" \
  "v_cmp_ne_u64 vcc, s[64:65], v[8:9]\n\t" \
  "v_cmp_ne_u64 s[36:37], s[64:65], v[10:11]\n\t" \
  "s_or_b64 vcc, vcc, s[36:37]\n\t" \
  "s_cbranch_scc0 33f\n\t" \
  "s_sleep 1\n\t" \
  "s_sub_u32 s39, s39, 1\n\t" \
  "s_cmp_lg_u32 s39, 0\n\t" \
  "s_cbranch_scc1 32b\n\t" \
  "33:\n\t" \
  "s_mov_b64 exec, 1\n\t" \
  "buffer_store_dwordx2 v[4:5], off, s[28:31], 0 sc1\n\t" \
  "s_mov_b64 exec, -1\n\t" \
  "s_branch 404b\n\t" \
    \
  "3:\n\t" \
  "s_mov_b32 s39, 0x40000\n\t" \
  "4:\n\t" \
  "s_sleep 1\n\t" \
  "buffer_load_dwordx2 v[4:5], v147, s[28:31], 0 offen sc1\n\t" \
  "s_waitcnt vmcnt(0)\n\t" \
  "v_cmp_ne_u64 vcc, s[64:65], v[4:5]\n\t" \
  "s_nop 4\n\t" \
  "s_cbranch_vccz 5b\n\t" \
  "s_sub_u32 s39, s39, 1\n\t" \
  "s_cmp_lg_u32 s39, 0\n\t" \
  "s_cbranch_scc1 4b\n\t" \
  "s_branch 5b\n\t" \
    \
  "6:\n\t" \
  "v_mov_b32 v2, 0xff800000\n\t" \
  "v_mov_b32 v3, 0xff800000\n\t" \
  "s_mov_b32 s39, 0\n\t" \
  "v_mov_b32 v112, 0\n\t" \
  "v_mov_b32 v113, 0\n\t" \
  "v_mov_b32 v114, 0\n\t" \
  "v_mov_b32 v115, 0\n\t" \
  "v_mov_b32 v116, 0\n\t" \
  "v_mov_b32 v117, 0\n\t" \
  "v_mov_b32 v118, 0\n\t" \
  "v_mov_b32 v119, 0\n\t" \
  "v_mov_b32 v120, 0\n\t" \
  "v_mov_b32 v121, 0\n\t" \
  "v_mov_b32 v122, 0\n\t" \
  "v_mov_b32 v123, 0\n\t" \
  "v_mov_b32 v124, 0\n\t" \
  "v_mov_b32 v125, 0\n\t" \
  "v_mov_b32 v126, 0\n\t" \
  "v_mov_b32 v127, 0\n\t" \
  "v_mov_b32 v128, 0\n\t" \
  "v_mov_b32 v129, 0\n\t" \
  "v_mov_b32 v130, 0\n\t" \
  "v_mov_b32 v131, 0\n\t" \
  "v_mov_b32 v132, 0\n\t" \
  "v_mov_b32 v133, 0\n\t" \
  "v_mov_b32 v134, 0\n\t" \
  "v_mov_b32 v135, 0\n\t" \
  "v_mov_b32 v136, 0\n\t" \
  "v_mov_b32 v137, 0\n\t" \
  "v_mov_b32 v138, 0\n\t" \
  "v_mov_b32 v139, 0\n\t" \
  "v_mov_b32 v140, 0\n\t" \
  "v_mov_b32 v141, 0\n\t" \
  "v_mov_b32 v142, 0\n\t" \
  "v_mov_b32 v143, 0\n\t" \
  "v_add_u32 v110, 10240, v110\n\t" \
  "v_add_u32 v111, 10240, v111\n\t" \
  "60:\n\t" \
  "v_subrev_u32 v110, 163840, v110\n\t" \
  "v_subrev_u32 v111, 163840, v111\n\t" \
  "s_mov_b32 s40, 16\n\t" \
  "61:\n\t" \
  "ds_read2st64_b64 v[84:87], v111 offset0:4 offset1:5\n\t" \
  "ds_read_b128 v[80:83], v110 offset:0\n\t" \
  "s_mov_b64 exec, s[34:35]\n\t" \
  "ds_read_b128 v[92:95], v110 offset:3072\n\t" \
  "ds_read_b128 v[88:91], v110 offset:3712\n\t" \
  "s_mov_b64 exec, -1\n\t" \
  "s_waitcnt lgkmcnt(0)\n\t" \
  "v_mfma_f32_32x32x4_2b_bf16 v[16:47], v[84:85], v[108:109], 0 cbsz:1 abid:0\n\t" \
  "s_nop 15\n\t" \
  "s_nop 1\n\t" \
  "v_mfma_f32_32x32x16_bf16 v[16:31], v[80:83], v[100:103], v[16:31]\n\t" \
  "v_mfma_f32_32x32x16_bf16 v[32:47], v[80:83], v[104:107], v[32:47]\n\t" \
  "s_nop 15\n\t" \
  "s_cmp_eq_u32 s39, 0\n\t" \
  "s_cbranch_scc0 70f\n\t" \
  "v_max3_f32 v2, v2, v16, v17\n\t" \
  "v_max3_f32 v3, v3, v32, v33\n\t" \
  "v_max3_f32 v2, v2, v18, v19\n\t" \
  "v_max3_f32 v3, v3, v34, v35\n\t" \
  "v_max3_f32 v2, v2, v20, v21\n\t" \
  "v_max3_f32 v3, v3, v36, v37\n\t" \
  "v_max3_f32 v2, v2, v22, v23\n\t" \
  "v_max3_f32 v3, v3, v38, v39\n\t" \
  "v_max3_f32 v2, v2, v24, v25\n\t" \
  "v_max3_f32 v3, v3, v40, v41\n\t" \
  "v_max3_f32 v2, v2, v26, v27\n\t" \
  "v_max3_f32 v3, v3, v42, v43\n\t" \
  "v_max3_f32 v2, v2, v28, v29\n\t" \
  "v_max3_f32 v3, v3, v44, v45\n\t" \
  "v_max3_f32 v2, v2, v30, v31\n\t" \
  "v_max3_f32 v3, v3, v46, v47\n\t" \
  "s_branch 80f\n\t" \
  "70:\n\t" \
  "v_sub_f32 v16, v16, v2\n\t" \
  "v_sub_f32 v17, v17, v2\n\t" \
  "v_sub_f32 v18, v18, v2\n\t" \
  "v_sub_f32 v19, v19, v2\n\t" \
  "v_sub_f32 v20, v20, v2\n\t" \
  "v_sub_f32 v21, v21, v2\n\t" \
  "v_sub_f32 v22, v22, v2\n\t" \
  "v_sub_f32 v23, v23, v2\n\t" \
  "v_sub_f32 v24, v24, v2\n\t" \
  "v_sub_f32 v25, v25, v2\n\t" \
  "v_sub_f32 v26, v26, v2\n\t" \
  "v_sub_f32 v27, v27, v2\n\t" \
  "v_sub_f32 v28, v28, v2\n\t" \
  "v_sub_f32 v29, v29, v2\n\t" \
  "v_sub_f32 v30, v30, v2\n\t" \
  "v_sub_f32 v31, v31, v2\n\t" \
  "v_sub_f32 v32, v32, v3\n\t" \
  "v_sub_f32 v33, v33, v3\n\t" \
  "v_sub_f32 v34, v34, v3\n\t" \
  "v_sub_f32 v35, v35, v3\n\t" \
  "v_sub_f32 v36, v36, v3\n\t" \
  "v_sub_f32 v37, v37, v3\n\t" \
  "v_sub_f32 v38, v38, v3\n\t" \
  "v_sub_f32 v39, v39, v3\n\t" \
  "v_sub_f32 v40, v40, v3\n\t" \
  "v_sub_f32 v41, v41, v3\n\t" \
  "v_sub_f32 v42, v42, v3\n\t" \
  "v_sub_f32 v43, v43, v3\n\t" \
  "v_sub_f32 v44, v44, v3\n\t" \
  "v_sub_f32 v45, v45, v3\n\t" \
  "v_sub_f32 v46, v46, v3\n\t" \
  "v_sub_f32 v47, v47, v3\n\t" \
  "v_exp_f32 v16, v16\n\t" \
  "v_exp_f32 v17, v17\n\t" \
  "v_exp_f32 v18, v18\n\t" \
  "v_exp_f32 v19, v19\n\t" \
  "v_exp_f32 v20, v20\n\t" \
  "v_exp_f32 v21, v21\n\t" \
  "v_exp_f32 v22, v22\n\t" \
  "v_exp_f32 v23, v23\n\t" \
  "v_exp_f32 v24, v24\n\t" \
  "v_exp_f32 v25, v25\n\t" \
  "v_exp_f32 v26, v26\n\t" \
  "v_exp_f32 v27, v27\n\t" \
  "v_exp_f32 v28, v28\n\t" \
  "v_exp_f32 v29, v29\n\t" \
  "v_exp_f32 v30, v30\n\t" \
  "v_exp_f32 v31, v31\n\t" \
  "v_exp_f32 v32, v32\n\t" \
  "v_exp_f32 v33, v33\n\t" \
  "v_exp_f32 v34, v34\n\t" \
  "v_exp_f32 v35, v35\n\t" \
  "v_exp_f32 v36, v36\n\t" \
  "v_exp_f32 v37, v37\n\t" \
  "v_exp_f32 v38, v38\n\t" \
  "v_exp_f32 v39, v39\n\t" \
  "v_exp_f32 v40, v40\n\t" \
  "v_exp_f32 v41, v41\n\t" \
  "v_exp_f32 v42, v42\n\t" \
  "v_exp_f32 v43, v43\n\t" \
  "v_exp_f32 v44, v44\n\t" \
  "v_exp_f32 v45, v45\n\t" \
  "v_exp_f32 v46, v46\n\t" \
  "v_exp_f32 v47, v47\n\t" \
  "s_nop 1\n\t" \
  "v_smfmac_f32_32x32x32_bf16 v[112:127], v[92:95], v[16:23], v144\n\t" \
  "v_smfmac_f32_32x32x32_bf16 v[112:127], v[88:91], v[24:31], v144\n\t" \
  "v_smfmac_f32_32x32x32_bf16 v[128:143], v[92:95], v[32:39], v144\n\t" \
  "v_smfmac_f32_32x32x32_bf16 v[128:143], v[88:91], v[40:47], v144\n\t" \
  "80:\n\t" \
  "ds_read_b128 v[80:83], v110 offset:1024\n\t" \
  "s_mov_b64 exec, s[34:35]\n\t" \
  "ds_read_b128 v[92:95], v110 offset:6400\n\t" \
  "ds_read_b128 v[88:91], v110 offset:7040\n\t" \
  "s_mov_b64 exec, -1\n\t" \
  "s_waitcnt lgkmcnt(0)\n\t" \
  "v_mfma_f32_32x32x4_2b_bf16 v[16:47], v[84:85], v[108:109], 0 cbsz:1 abid:1\n\t" \
  "s_nop 15\n\t" \
  "s_nop 1\n\t" \
  "v_mfma_f32_32x32x16_bf16 v[16:31], v[80:83], v[100:103], v[16:31]\n\t" \
  "v_mfma_f32_32x32x16_bf16 v[32:47], v[80:83], v[104:107], v[32:47]\n\t" \
  "s_nop 15\n\t" \
  "s_cmp_eq_u32 s39, 0\n\t" \
  "s_cbranch_scc0 71f\n\t" \
  "v_max3_f32 v2, v2, v16, v17\n\t" \
  "v_max3_f32 v3, v3, v32, v33\n\t" \
  "v_max3_f32 v2, v2, v18, v19\n\t" \
  "v_max3_f32 v3, v3, v34, v35\n\t" \
  "v_max3_f32 v2, v2, v20, v21\n\t" \
  "v_max3_f32 v3, v3, v36, v37\n\t" \
  "v_max3_f32 v2, v2, v22, v23\n\t" \
  "v_max3_f32 v3, v3, v38, v39\n\t" \
  "v_max3_f32 v2, v2, v24, v25\n\t" \
  "v_max3_f32 v3, v3, v40, v41\n\t" \
  "v_max3_f32 v2, v2, v26, v27\n\t" \
  "v_max3_f32 v3, v3, v42, v43\n\t" \
  "v_max3_f32 v2, v2, v28, v29\n\t" \
  "v_max3_f32 v3, v3, v44, v45\n\t" \
  "v_max3_f32 v2, v2, v30, v31\n\t" \
  "v_max3_f32 v3, v3, v46, v47\n\t" \
  "s_branch 81f\n\t" \
  "71:\n\t" \
  "v_sub_f32 v16, v16, v2\n\t" \
  "v_sub_f32 v17, v17, v2\n\t" \
  "v_sub_f32 v18, v18, v2\n\t" \
  "v_sub_f32 v19, v19, v2\n\t" \
  "v_sub_f32 v20, v20, v2\n\t" \
  "v_sub_f32 v21, v21, v2\n\t" \
  "v_sub_f32 v22, v22, v2\n\t" \
  "v_sub_f32 v23, v23, v2\n\t" \
  "v_sub_f32 v24, v24, v2\n\t" \
  "v_sub_f32 v25, v25, v2\n\t" \
  "v_sub_f32 v26, v26, v2\n\t" \
  "v_sub_f32 v27, v27, v2\n\t" \
  "v_sub_f32 v28, v28, v2\n\t" \
  "v_sub_f32 v29, v29, v2\n\t" \
  "v_sub_f32 v30, v30, v2\n\t" \
  "v_sub_f32 v31, v31, v2\n\t" \
  "v_sub_f32 v32, v32, v3\n\t" \
  "v_sub_f32 v33, v33, v3\n\t" \
  "v_sub_f32 v34, v34, v3\n\t" \
  "v_sub_f32 v35, v35, v3\n\t" \
  "v_sub_f32 v36, v36, v3\n\t" \
  "v_sub_f32 v37, v37, v3\n\t" \
  "v_sub_f32 v38, v38, v3\n\t" \
  "v_sub_f32 v39, v39, v3\n\t" \
  "v_sub_f32 v40, v40, v3\n\t" \
  "v_sub_f32 v41, v41, v3\n\t" \
  "v_sub_f32 v42, v42, v3\n\t" \
  "v_sub_f32 v43, v43, v3\n\t" \
  "v_sub_f32 v44, v44, v3\n\t" \
  "v_sub_f32 v45, v45, v3\n\t" \
  "v_sub_f32 v46, v46, v3\n\t" \
  "v_sub_f32 v47, v47, v3\n\t" \
  "v_exp_f32 v16, v16\n\t" \
  "v_exp_f32 v17, v17\n\t" \
  "v_exp_f32 v18, v18\n\t" \
  "v_exp_f32 v19, v19\n\t" \
  "v_exp_f32 v20, v20\n\t" \
  "v_exp_f32 v21, v21\n\t" \
  "v_exp_f32 v22, v22\n\t" \
  "v_exp_f32 v23, v23\n\t" \
  "v_exp_f32 v24, v24\n\t" \
  "v_exp_f32 v25, v25\n\t" \
  "v_exp_f32 v26, v26\n\t" \
  "v_exp_f32 v27, v27\n\t" \
  "v_exp_f32 v28, v28\n\t" \
  "v_exp_f32 v29, v29\n\t" \
  "v_exp_f32 v30, v30\n\t" \
  "v_exp_f32 v31, v31\n\t" \
  "v_exp_f32 v32, v32\n\t" \
  "v_exp_f32 v33, v33\n\t" \
  "v_exp_f32 v34, v34\n\t" \
  "v_exp_f32 v35, v35\n\t" \
  "v_exp_f32 v36, v36\n\t" \
  "v_exp_f32 v37, v37\n\t" \
  "v_exp_f32 v38, v38\n\t" \
  "v_exp_f32 v39, v39\n\t" \
  "v_exp_f32 v40, v40\n\t" \
  "v_exp_f32 v41, v41\n\t" \
  "v_exp_f32 v42, v42\n\t" \
  "v_exp_f32 v43, v43\n\t" \
  "v_exp_f32 v44, v44\n\t" \
  "v_exp_f32 v45, v45\n\t" \
  "v_exp_f32 v46, v46\n\t" \
  "v_exp_f32 v47, v47\n\t" \
  "s_nop 1\n\t" \
  "v_smfmac_f32_32x32x32_bf16 v[112:127], v[92:95], v[16:23], v144\n\t" \
  "v_smfmac_f32_32x32x32_bf16 v[112:127], v[88:91], v[24:31], v144\n\t" \
  "v_smfmac_f32_32x32x32_bf16 v[128:143], v[92:95], v[32:39], v144\n\t" \
  "v_smfmac_f32_32x32x32_bf16 v[128:143], v[88:91], v[40:47], v144\n\t" \
  "81:\n\t" \
  "ds_read_b128 v[80:83], v110 offset:4352\n\t" \
  "s_mov_b64 exec, s[34:35]\n\t" \
  "ds_read_b128 v[92:95], v110 offset:7680\n\t" \
  "ds_read_b128 v[88:91], v110 offset:8320\n\t" \
  "s_mov_b64 exec, -1\n\t" \
  "s_waitcnt lgkmcnt(0)\n\t" \
  "v_mfma_f32_32x32x4_2b_bf16 v[16:47], v[86:87], v[108:109], 0 cbsz:1 abid:0\n\t" \
  "s_nop 15\n\t" \
  "s_nop 1\n\t" \
  "v_mfma_f32_32x32x16_bf16 v[16:31], v[80:83], v[100:103], v[16:31]\n\t" \
  "v_mfma_f32_32x32x16_bf16 v[32:47], v[80:83], v[104:107], v[32:47]\n\t" \
  "s_nop 15\n\t" \
  "s_cmp_eq_u32 s39, 0\n\t" \
  "s_cbranch_scc0 72f\n\t" \
  "v_max3_f32 v2, v2, v16, v17\n\t" \
  "v_max3_f32 v3, v3, v32, v33\n\t" \
  "v_max3_f32 v2, v2, v18, v19\n\t" \
  "v_max3_f32 v3, v3, v34, v35\n\t" \
  "v_max3_f32 v2, v2, v20, v21\n\t" \
  "v_max3_f32 v3, v3, v36, v37\n\t" \
  "v_max3_f32 v2, v2, v22, v23\n\t" \
  "v_max3_f32 v3, v3, v38, v39\n\t" \
  "v_max3_f32 v2, v2, v24, v25\n\t" \
  "v_max3_f32 v3, v3, v40, v41\n\t" \
  "v_max3_f32 v2, v2, v26, v27\n\t" \
  "v_max3_f32 v3, v3, v42, v43\n\t" \
  "v_max3_f32 v2, v2, v28, v29\n\t" \
  "v_max3_f32 v3, v3, v44, v45\n\t" \
  "v_max3_f32 v2, v2, v30, v31\n\t" \
  "v_max3_f32 v3, v3, v46, v47\n\t" \
  "s_branch 82f\n\t" \
  "72:\n\t" \
  "v_sub_f32 v16, v16, v2\n\t" \
  "v_sub_f32 v17, v17, v2\n\t" \
  "v_sub_f32 v18, v18, v2\n\t" \
  "v_sub_f32 v19, v19, v2\n\t" \
  "v_sub_f32 v20, v20, v2\n\t" \
  "v_sub_f32 v21, v21, v2\n\t" \
  "v_sub_f32 v22, v22, v2\n\t" \
  "v_sub_f32 v23, v23, v2\n\t" \
  "v_sub_f32 v24, v24, v2\n\t" \
  "v_sub_f32 v25, v25, v2\n\t" \
  "v_sub_f32 v26, v26, v2\n\t" \
  "v_sub_f32 v27, v27, v2\n\t" \
  "v_sub_f32 v28, v28, v2\n\t" \
  "v_sub_f32 v29, v29, v2\n\t" \
  "v_sub_f32 v30, v30, v2\n\t" \
  "v_sub_f32 v31, v31, v2\n\t" \
  "v_sub_f32 v32, v32, v3\n\t" \
  "v_sub_f32 v33, v33, v3\n\t" \
  "v_sub_f32 v34, v34, v3\n\t" \
  "v_sub_f32 v35, v35, v3\n\t" \
  "v_sub_f32 v36, v36, v3\n\t" \
  "v_sub_f32 v37, v37, v3\n\t" \
  "v_sub_f32 v38, v38, v3\n\t" \
  "v_sub_f32 v39, v39, v3\n\t" \
  "v_sub_f32 v40, v40, v3\n\t" \
  "v_sub_f32 v41, v41, v3\n\t" \
  "v_sub_f32 v42, v42, v3\n\t" \
  "v_sub_f32 v43, v43, v3\n\t" \
  "v_sub_f32 v44, v44, v3\n\t" \
  "v_sub_f32 v45, v45, v3\n\t" \
  "v_sub_f32 v46, v46, v3\n\t" \
  "v_sub_f32 v47, v47, v3\n\t" \
  "v_exp_f32 v16, v16\n\t" \
  "v_exp_f32 v17, v17\n\t" \
  "v_exp_f32 v18, v18\n\t" \
  "v_exp_f32 v19, v19\n\t" \
  "v_exp_f32 v20, v20\n\t" \
  "v_exp_f32 v21, v21\n\t" \
  "v_exp_f32 v22, v22\n\t" \
  "v_exp_f32 v23, v23\n\t" \
  "v_exp_f32 v24, v24\n\t" \
  "v_exp_f32 v25, v25\n\t" \
  "v_exp_f32 v26, v26\n\t" \
  "v_exp_f32 v27, v27\n\t" \
  "v_exp_f32 v28, v28\n\t" \
  "v_exp_f32 v29, v29\n\t" \
  "v_exp_f32 v30, v30\n\t" \
  "v_exp_f32 v31, v31\n\t" \
  "v_exp_f32 v32, v32\n\t" \
  "v_exp_f32 v33, v33\n\t" \
  "v_exp_f32 v34, v34\n\t" \
  "v_exp_f32 v35, v35\n\t" \
  "v_exp_f32 v36, v36\n\t" \
  "v_exp_f32 v37, v37\n\t" \
  "v_exp_f32 v38, v38\n\t" \
  "v_exp_f32 v39, v39\n\t" \
  "v_exp_f32 v40, v40\n\t" \
  "v_exp_f32 v41, v41\n\t" \
  "v_exp_f32 v42, v42\n\t" \
  "v_exp_f32 v43, v43\n\t" \
  "v_exp_f32 v44, v44\n\t" \
  "v_exp_f32 v45, v45\n\t" \
  "v_exp_f32 v46, v46\n\t" \
  "v_exp_f32 v47, v47\n\t" \
  "s_nop 1\n\t" \
  "v_smfmac_f32_32x32x32_bf16 v[112:127], v[92:95], v[16:23], v144\n\t" \
  "v_smfmac_f32_32x32x32_bf16 v[112:127], v[88:91], v[24:31], v144\n\t" \
  "v_smfmac_f32_32x32x32_bf16 v[128:143], v[92:95], v[32:39], v144\n\t" \
  "v_smfmac_f32_32x32x32_bf16 v[128:143], v[88:91], v[40:47], v144\n\t" \
  "82:\n\t" \
  "ds_read_b128 v[80:83], v110 offset:5376\n\t" \
  "s_mov_b64 exec, s[34:35]\n\t" \
  "ds_read_b128 v[92:95], v110 offset:8960\n\t" \
  "ds_read_b128 v[88:91], v110 offset:9600\n\t" \
  "s_mov_b64 exec, -1\n\t" \
  "s_waitcnt lgkmcnt(0)\n\t" \
  "v_mfma_f32_32x32x4_2b_bf16 v[16:47], v[86:87], v[108:109], 0 cbsz:1 abid:1\n\t" \
  "s_nop 15\n\t" \
  "s_nop 1\n\t" \
  "v_mfma_f32_32x32x16_bf16 v[16:31], v[80:83], v[100:103], v[16:31]\n\t" \
  "v_mfma_f32_32x32x16_bf16 v[32:47], v[80:83], v[104:107], v[32:47]\n\t" \
  "s_nop 15\n\t" \
  "s_cmp_eq_u32 s39, 0\n\t" \
  "s_cbranch_scc0 73f\n\t" \
  "v_max3_f32 v2, v2, v16, v17\n\t" \
  "v_max3_f32 v3, v3, v32, v33\n\t" \
  "v_max3_f32 v2, v2, v18, v19\n\t" \
  "v_max3_f32 v3, v3, v34, v35\n\t" \
  "v_max3_f32 v2, v2, v20, v21\n\t" \
  "v_max3_f32 v3, v3, v36, v37\n\t" \
  "v_max3_f32 v2, v2, v22, v23\n\t" \
  "v_max3_f32 v3, v3, v38, v39\n\t" \
  "v_max3_f32 v2, v2, v24, v25\n\t" \
  "v_max3_f32 v3, v3, v40, v41\n\t" \
  "v_max3_f32 v2, v2, v26, v27\n\t" \
  "v_max3_f32 v3, v3, v42, v43\n\t" \
  "v_max3_f32 v2, v2, v28, v29\n\t" \
  "v_max3_f32 v3, v3, v44, v45\n\t" \
  "v_max3_f32 v2, v2, v30, v31\n\t" \
  "v_max3_f32 v3, v3, v46, v47\n\t" \
  "s_branch 83f\n\t" \
  "73:\n\t" \
  "v_sub_f32 v16, v16, v2\n\t" \
  "v_sub_f32 v17, v17, v2\n\t" \
  "v_sub_f32 v18, v18, v2\n\t" \
  "v_sub_f32 v19, v19, v2\n\t" \
  "v_sub_f32 v20, v20, v2\n\t" \
  "v_sub_f32 v21, v21, v2\n\t" \
  "v_sub_f32 v22, v22, v2\n\t" \
  "v_sub_f32 v23, v23, v2\n\t" \
  "v_sub_f32 v24, v24, v2\n\t" \
  "v_sub_f32 v25, v25, v2\n\t" \
  "v_sub_f32 v26, v26, v2\n\t" \
  "v_sub_f32 v27, v27, v2\n\t" \
  "v_sub_f32 v28, v28, v2\n\t" \
  "v_sub_f32 v29, v29, v2\n\t" \
  "v_sub_f32 v30, v30, v2\n\t" \
  "v_sub_f32 v31, v31, v2\n\t" \
  "v_sub_f32 v32, v32, v3\n\t" \
  "v_sub_f32 v33, v33, v3\n\t" \
  "v_sub_f32 v34, v34, v3\n\t" \
  "v_sub_f32 v35, v35, v3\n\t" \
  "v_sub_f32 v36, v36, v3\n\t" \
  "v_sub_f32 v37, v37, v3\n\t" \
  "v_sub_f32 v38, v38, v3\n\t" \
  "v_sub_f32 v39, v39, v3\n\t" \
  "v_sub_f32 v40, v40, v3\n\t" \
  "v_sub_f32 v41, v41, v3\n\t" \
  "v_sub_f32 v42, v42, v3\n\t" \
  "v_sub_f32 v43, v43, v3\n\t" \
  "v_sub_f32 v44, v44, v3\n\t" \
  "v_sub_f32 v45, v45, v3\n\t" \
  "v_sub_f32 v46, v46, v3\n\t" \
  "v_sub_f32 v47, v47, v3\n\t" \
  "v_exp_f32 v16, v16\n\t" \
  "v_exp_f32 v17, v17\n\t" \
  "v_exp_f32 v18, v18\n\t" \
  "v_exp_f32 v19, v19\n\t" \
  "v_exp_f32 v20, v20\n\t" \
  "v_exp_f32 v21, v21\n\t" \
  "v_exp_f32 v22, v22\n\t" \
  "v_exp_f32 v23, v23\n\t" \
  "v_exp_f32 v24, v24\n\t" \
  "v_exp_f32 v25, v25\n\t" \
  "v_exp_f32 v26, v26\n\t" \
  "v_exp_f32 v27, v27\n\t" \
  "v_exp_f32 v28, v28\n\t" \
  "v_exp_f32 v29, v29\n\t" \
  "v_exp_f32 v30, v30\n\t" \
  "v_exp_f32 v31, v31\n\t" \
  "v_exp_f32 v32, v32\n\t" \
  "v_exp_f32 v33, v33\n\t" \
  "v_exp_f32 v34, v34\n\t" \
  "v_exp_f32 v35, v35\n\t" \
  "v_exp_f32 v36, v36\n\t" \
  "v_exp_f32 v37, v37\n\t" \
  "v_exp_f32 v38, v38\n\t" \
  "v_exp_f32 v39, v39\n\t" \
  "v_exp_f32 v40, v40\n\t" \
  "v_exp_f32 v41, v41\n\t" \
  "v_exp_f32 v42, v42\n\t" \
  "v_exp_f32 v43, v43\n\t" \
  "v_exp_f32 v44, v44\n\t" \
  "v_exp_f32 v45, v45\n\t" \
  "v_exp_f32 v46, v46\n\t" \
  "v_exp_f32 v47, v47\n\t" \
  "s_nop 1\n\t" \
  "v_smfmac_f32_32x32x32_bf16 v[112:127], v[92:95], v[16:23], v144\n\t" \
  "v_smfmac_f32_32x32x32_bf16 v[112:127], v[88:91], v[24:31], v144\n\t" \
  "v_smfmac_f32_32x32x32_bf16 v[128:143], v[92:95], v[32:39], v144\n\t" \
  "v_smfmac_f32_32x32x32_bf16 v[128:143], v[88:91], v[40:47], v144\n\t" \
  "83:\n\t" \
  "v_add_u32 v110, 10240, v110\n\t" \
  "v_add_u32 v111, 10240, v111\n\t" \
  "s_sub_u32 s40, s40, 1\n\t" \
  "s_cmp_lg_u32 s40, 0\n\t" \
  "s_cbranch_scc1 61b\n\t" \
  "s_nop 15\n\t" \
  "s_cmp_eq_u32 s39, 0\n\t" \
  "s_cbranch_scc0 9f\n\t" \
  "s_mov_b32 s39, 1\n\t" \
  "v_mov_b32 v4, v2\n\t" \
  "v_mov_b32 v5, v3\n\t" \
  "s_nop 1\n\t" \
  "v_permlane32_swap_b32 v2, v4\n\t" \
  "v_permlane32_swap_b32 v3, v5\n\t" \
  "s_nop 1\n\t" \
  "v_max_f32 v2, v2, v4\n\t" \
  "v_max_f32 v3, v3, v5\n\t" \
  "s_branch 60b\n\t" \
  "9:\n\t" \
  "v_rcp_f32 v2, v122\n\t" \
  "s_mov_b32 s39, 0x7f000000\n\t" \
  "v_mul_f32 v2, v145, v2\n\t" \
  "v_mul_f32 v112, v112, v2\n\t" \
  "v_mul_f32 v113, v113, v2\n\t" \
  "v_mul_f32 v114, v114, v2\n\t" \
  "v_mul_f32 v115, v115, v2\n\t" \
  "v_mul_f32 v116, v116, v2\n\t" \
  "v_mul_f32 v117, v117, v2\n\t" \
  "v_mul_f32 v118, v118, v2\n\t" \
  "v_mul_f32 v119, v119, v2\n\t" \
  "v_mul_f32 v120, v120, v2\n\t" \
  "v_mul_f32 v121, v121, v2\n\t" \
  "v_rcp_f32 v3, v138\n\t" \
  "v_min_f32 v4, v122, v138\n\t" \
  "v_mul_f32 v3, v145, v3\n\t" \
  "v_max_f32 v5, v122, v138\n\t" \
  "v_mul_f32 v128, v128, v3\n\t" \
  "v_mul_f32 v129, v129, v3\n\t" \
  "v_mul_f32 v130, v130, v3\n\t" \
  "v_mul_f32 v131, v131, v3\n\t" \
  "v_mul_f32 v132, v132, v3\n\t" \
  "v_mul_f32 v133, v133, v3\n\t" \
  "v_mul_f32 v134, v134, v3\n\t" \
  "v_mul_f32 v135, v135, v3\n\t" \
  "v_mul_f32 v136, v136, v3\n\t" \
  "v_mul_f32 v137, v137, v3\n\t" \
  "v_cvt_pk_bf16_f32 v16, v112, v113\n\t" \
  "v_min_u32 v16, -2, v16\n\t" \
  "v_cvt_pk_bf16_f32 v17, v114, v115\n\t" \
  "v_min_u32 v17, -2, v17\n\t" \
  "v_cvt_pk_bf16_f32 v18, v116, v117\n\t" \
  "v_min_u32 v18, -2, v18\n\t" \
  "v_cvt_pk_bf16_f32 v19, v118, v119\n\t" \
  "v_min_u32 v19, -2, v19\n\t" \
  "buffer_store_dwordx4 v[16:19], v14, s[28:31], 0 offen sc1\n\t" \
  "v_cvt_pk_bf16_f32 v20, v128, v129\n\t" \
  "v_min_u32 v20, -2, v20\n\t" \
  "v_cvt_pk_bf16_f32 v21, v130, v131\n\t" \
  "v_min_u32 v21, -2, v21\n\t" \
  "v_cvt_pk_bf16_f32 v22, v132, v133\n\t" \
  "v_min_u32 v22, -2, v22\n\t" \
  "v_cvt_pk_bf16_f32 v23, v134, v135\n\t" \
  "v_min_u32 v23, -2, v23\n\t" \
  "buffer_store_dwordx4 v[20:23], v14, s[28:31], 0 offen offset:1024 sc1\n\t" \
  "v_cvt_pk_bf16_f32 v24, v120, v121\n\t" \
  "v_min_u32 v24, -2, v24\n\t" \
  "v_cvt_pk_bf16_f32 v25, v136, v137\n\t" \
  "v_min_u32 v25, -2, v25\n\t" \
  "buffer_store_dwordx2 v[24:25], v15, s[28:31], 0 offen offset:2048 sc1\n\t" \
  "s_branch 8b\n\t" \
    \
  "110:\n\t" \
  "v_mov_b64 v[72:73], 0\n\t" \
  "v_mov_b64 v[74:75], 0\n\t" \
  "v_mov_b64 v[76:77], 0\n\t" \
  "v_mov_b64 v[78:79], 0\n\t" \
  "v_and_b32 v10, 16, v1\n\t" \
  "v_cmp_ne_u32 vcc, 0, v10\n\t" \
  "v_mov_b32 v8, 0x0c0a0200\n\t" \
  "v_mov_b32 v10, 0x3a083412\n\t" \
  "v_cndmask_b32 v8, v8, v10, vcc\n\t" \
  "v_mov_b32 v9, 0x0604100e\n\t" \
  "v_mov_b32 v10, 0x38363e3c\n\t" \
  "v_cndmask_b32 v9, v9, v10, vcc\n\t" \
  "v_and_b32 v10, 14, v1\n\t" \
  "v_lshlrev_b32 v10, 2, v10\n\t" \
  "v_lshrrev_b64 v[8:9], v10, v[8:9]\n\t" \
  "v_and_b32 v8, 0xff, v8\n\t" \
  "v_cmp_gt_u32 vcc, 20, v8\n\t" \
  "v_and_b32 v10, 1, v1\n\t" \
  "v_add_u32 v8, v8, v10\n\t" \
  "v_cndmask_b32_e64 v10, -12, 20, vcc\n\t" \
  "v_cmp_lt_u32 vcc, 31, v1\n\t" \
  "s_nop 1\n\t" \
  "v_cndmask_b32 v10, 0, v10, vcc\n\t" \
  "v_add_u32 v110, v8, v10\n\t" \
  "v_mfma_f32_32x32x16_bf16 v[112:127], v[72:75], v[72:75], 0\n\t" \
  "v_lshlrev_b32 v110, 4, v110\n\t" \
  "v_lshlrev_b32 v111, 3, v1\n\t" \
  "v_and_b32 v8, 27, v1\n\t" \
  "v_cmp_eq_u32 vcc, 18, v8\n\t" \
  "v_mov_b32 v9, 0x3f803f80\n\t" \
  "v_mov_b32 v144, 0xdddddddd\n\t" \
  "v_cndmask_b32 v88, 0, v9, vcc\n\t" \
  "v_mov_b32 v89, v88\n\t" \
  "v_mfma_f32_32x32x16_bf16 v[128:143], v[72:75], v[72:75], 0\n\t" \
  "v_mov_b64 v[90:91], v[88:89]\n\t" \
  "v_mov_b64 v[92:93], v[88:89]\n\t" \
  "v_mov_b64 v[94:95], v[88:89]\n\t" \
  "v_mov_b64 v[96:97], v[88:89]\n\t" \
  "v_mov_b64 v[98:99], v[88:89]\n\t" \
  "s_mov_b32 s34, 0x0033ffff\n\t" \
  "s_mov_b32 s35, 0x0033ffff\n\t" \
  "s_mov_b32 s28, s14\n\t" \
  "s_and_b32 s29, s15, 0xffff\n\t" \
  "s_mov_b32 s30, 7274496\n\t" \
  "s_mov_b32 s31, 0x20000\n\t" \
  "s_cmp_lt_u32 s3, 4\n\t" \
  "s_cselect_b32 s41, 1, 0\n\t" \
  "s_lshl_b32 s54, s4, 5\n\t" \
  "s_add_u32 s54, s54, s5\n\t" \
  "s_and_b32 s53, s3, 1\n\t" \
  "s_lshl1_add_u32 s54, s54, s53\n\t" \
  "s_lshl_b32 s54, s54, 7\n\t" \
  "s_add_u32 s54, s54, 16384\n\t" \
  "v_lshl_add_u32 v145, v3, 2, s54\n\t" \
  "s_mul_i32 s54, s4, 163840\n\t" \
  "s_add_u32 s54, s54, 65536\n\t" \
  "s_add_u32 s48, s14, s54\n\t" \
  "s_addc_u32 s49, s15, 0\n\t" \
  "s_lshl1_add_u32 s51, s5, s7\n\t" \
  "s_lshr_b32 s52, s51, 2\n\t" \
  "s_mul_i32 s52, s52, 10240\n\t" \
  "s_add_u32 s52, s52, s54\n\t" \
  "s_and_b32 s51, s51, 3\n\t" \
  "s_cmp_eq_u32 s8, 0\n\t" \
  "s_cbranch_scc1 111f\n\t" \
  "s_mul_i32 s53, s51, 1280\n\t" \
  "s_add_u32 s53, s53, 5120\n\t" \
  "s_cmp_eq_u32 s51, 0\n\t" \
  "s_cselect_b32 s53, 3072, s53\n\t" \
  "s_add_u32 s52, s52, s53\n\t" \
  "v_cmp_lt_u32 vcc, 17, v3\n\t" \
  "v_add_u32 v6, 2, v1\n\t" \
  "s_nop 0\n\t" \
  "v_cndmask_b32 v6, v1, v6, vcc\n\t" \
  "v_and_b32 v10, 16, v6\n\t" \
  "v_cmp_ne_u32 vcc, 0, v10\n\t" \
  "v_mov_b32 v8, 0x0c0a0200\n\t" \
  "v_mov_b32 v10, 0x3a083412\n\t" \
  "v_cndmask_b32 v8, v8, v10, vcc\n\t" \
  "v_mov_b32 v9, 0x0604100e\n\t" \
  "v_mov_b32 v10, 0x38363e3c\n\t" \
  "v_cndmask_b32 v9, v9, v10, vcc\n\t" \
  "v_and_b32 v10, 14, v6\n\t" \
  "v_lshlrev_b32 v10, 2, v10\n\t" \
  "v_lshrrev_b64 v[8:9], v10, v[8:9]\n\t" \
  "v_and_b32 v8, 0xff, v8\n\t" \
  "v_cmp_gt_u32 vcc, 20, v8\n\t" \
  "v_and_b32 v10, 1, v6\n\t" \
  "v_add_u32 v8, v8, v10\n\t" \
  "v_cndmask_b32_e64 v10, -12, 20, vcc\n\t" \
  "v_cmp_lt_u32 vcc, 31, v6\n\t" \
  "s_nop 1\n\t" \
  "v_cndmask_b32 v10, 0, v10, vcc\n\t" \
  "v_add_u32 v6, v8, v10\n\t" \
  "v_lshl_add_u32 v6, v6, 4, s52\n\t" \
  "s_waitcnt vmcnt(6) lgkmcnt(0)\n\t" \
  "ds_read_b128 v[148:151], v228 offset:0\n\t" \
  "ds_read_b64_tr_b16 v[180:181], v232 offset:0\n\t" \
  "ds_read_b64_tr_b16 v[182:183], v232 offset:160\n\t" \
  "ds_read_b128 v[152:155], v229 offset:0\n\t" \
  "ds_read_b64_tr_b16 v[184:185], v232 offset:640\n\t" \
  "ds_read_b64_tr_b16 v[186:187], v232 offset:800\n\t" \
  "ds_read_b128 v[156:159], v230 offset:0\n\t" \
  "ds_read_b64_tr_b16 v[188:189], v232 offset:1280\n\t" \
  "ds_read_b64_tr_b16 v[190:191], v232 offset:1440\n\t" \
  "ds_read_b128 v[160:163], v231 offset:0\n\t" \
  "ds_read_b64_tr_b16 v[192:193], v232 offset:1920\n\t" \
  "ds_read_b64_tr_b16 v[194:195], v232 offset:2080\n\t" \
  "s_add_u32 s64, s64, 1\n\t" \
  "s_addc_u32 s65, s65, 0\n\t" \
  "s_waitcnt lgkmcnt(9)\n\t" \
  "v_mfma_f32_32x32x16_bf16 v[212:227], v[148:151], v[180:183], 0\n\t" \
  "s_waitcnt lgkmcnt(6)\n\t" \
  "v_mfma_f32_32x32x16_bf16 v[212:227], v[152:155], v[184:187], v[212:227]\n\t" \
  "s_waitcnt lgkmcnt(3)\n\t" \
  "v_mfma_f32_32x32x16_bf16 v[212:227], v[156:159], v[188:191], v[212:227]\n\t" \
  "s_waitcnt lgkmcnt(0)\n\t" \
  "v_mfma_f32_32x32x16_bf16 v[212:227], v[160:163], v[192:195], v[212:227]\n\t" \
  "s_waitcnt vmcnt(0)\n\t" \
  "ds_read_b128 v[164:167], v252 offset:0\n\t" \
  "ds_read_b64_tr_b16 v[196:197], v232 offset:2560\n\t" \
  "ds_read_b64_tr_b16 v[198:199], v232 offset:2720\n\t" \
  "ds_read_b128 v[168:171], v253 offset:0\n\t" \
  "ds_read_b64_tr_b16 v[200:201], v232 offset:3200\n\t" \
  "ds_read_b64_tr_b16 v[202:203], v232 offset:3360\n\t" \
  "ds_read_b128 v[172:175], v254 offset:0\n\t" \
  "ds_read_b64_tr_b16 v[204:205], v232 offset:3840\n\t" \
  "ds_read_b64_tr_b16 v[206:207], v232 offset:4000\n\t" \
  "ds_read_b128 v[176:179], v255 offset:0\n\t" \
  "ds_read_b64_tr_b16 v[208:209], v232 offset:4480\n\t" \
  "ds_read_b64_tr_b16 v[210:211], v232 offset:4640\n\t" \
  "s_waitcnt lgkmcnt(9)\n\t" \
  "v_mfma_f32_32x32x16_bf16 v[212:227], v[164:167], v[196:199], v[212:227]\n\t" \
  "s_waitcnt lgkmcnt(6)\n\t" \
  "v_mfma_f32_32x32x16_bf16 v[212:227], v[168:171], v[200:203], v[212:227]\n\t" \
  "s_waitcnt lgkmcnt(3)\n\t" \
  "v_mfma_f32_32x32x16_bf16 v[212:227], v[172:175], v[204:207], v[212:227]\n\t" \
  "s_waitcnt lgkmcnt(0)\n\t" \
  "v_mfma_f32_32x32x16_bf16 v[212:227], v[176:179], v[208:211], v[212:227]\n\t" \
  "s_barrier\n\t" \
  "ds_read_b128 v[16:19], v233 offset:5120\n\t" \
  "ds_read_b128 v[20:23], v233 offset:5760\n\t" \
  "ds_read_b128 v[24:27], v233 offset:6400\n\t" \
  "ds_read_b128 v[28:31], v233 offset:7040\n\t" \
  "ds_read_b128 v[32:35], v233 offset:8192\n\t" \
  "ds_read_b128 v[36:39], v233 offset:8832\n\t" \
  "ds_read_b128 v[40:43], v233 offset:9472\n\t" \
  "ds_read_b128 v[44:47], v233 offset:10112\n\t" \
  "ds_read_b128 v[48:51], v233 offset:11264\n\t" \
  "ds_read_b128 v[52:55], v233 offset:11904\n\t" \
  "ds_read_b128 v[56:59], v233 offset:12544\n\t" \
  "ds_read_b128 v[60:63], v233 offset:13184\n\t" \
  "ds_read_b128 v[234:237], v233 offset:14336\n\t" \
  "ds_read_b128 v[238:241], v233 offset:14976\n\t" \
  "ds_read_b128 v[242:245], v233 offset:15616\n\t" \
  "ds_read_b128 v[246:249], v233 offset:16256\n\t" \
  "s_waitcnt lgkmcnt(12)\n\t" \
  "v_add_f32 v212, v212, v16\n\t" \
  "v_add_f32 v213, v213, v17\n\t" \
  "v_add_f32 v214, v214, v18\n\t" \
  "v_add_f32 v215, v215, v19\n\t" \
  "v_add_f32 v216, v216, v20\n\t" \
  "v_add_f32 v217, v217, v21\n\t" \
  "v_add_f32 v218, v218, v22\n\t" \
  "v_add_f32 v219, v219, v23\n\t" \
  "v_add_f32 v220, v220, v24\n\t" \
  "v_add_f32 v221, v221, v25\n\t" \
  "v_add_f32 v222, v222, v26\n\t" \
  "v_add_f32 v223, v223, v27\n\t" \
  "v_add_f32 v224, v224, v28\n\t" \
  "v_add_f32 v225, v225, v29\n\t" \
  "v_add_f32 v226, v226, v30\n\t" \
  "v_add_f32 v227, v227, v31\n\t" \
  "s_waitcnt lgkmcnt(8)\n\t" \
  "v_add_f32 v212, v212, v32\n\t" \
  "v_add_f32 v213, v213, v33\n\t" \
  "v_add_f32 v214, v214, v34\n\t" \
  "v_add_f32 v215, v215, v35\n\t" \
  "v_add_f32 v216, v216, v36\n\t" \
  "v_add_f32 v217, v217, v37\n\t" \
  "v_add_f32 v218, v218, v38\n\t" \
  "v_add_f32 v219, v219, v39\n\t" \
  "v_add_f32 v220, v220, v40\n\t" \
  "v_add_f32 v221, v221, v41\n\t" \
  "v_add_f32 v222, v222, v42\n\t" \
  "v_add_f32 v223, v223, v43\n\t" \
  "v_add_f32 v224, v224, v44\n\t" \
  "v_add_f32 v225, v225, v45\n\t" \
  "v_add_f32 v226, v226, v46\n\t" \
  "v_add_f32 v227, v227, v47\n\t" \
  "s_waitcnt lgkmcnt(4)\n\t" \
  "v_add_f32 v212, v212, v48\n\t" \
  "v_add_f32 v213, v213, v49\n\t" \
  "v_add_f32 v214, v214, v50\n\t" \
  "v_add_f32 v215, v215, v51\n\t" \
  "v_add_f32 v216, v216, v52\n\t" \
  "v_add_f32 v217, v217, v53\n\t" \
  "v_add_f32 v218, v218, v54\n\t" \
  "v_add_f32 v219, v219, v55\n\t" \
  "v_add_f32 v220, v220, v56\n\t" \
  "v_add_f32 v221, v221, v57\n\t" \
  "v_add_f32 v222, v222, v58\n\t" \
  "v_add_f32 v223, v223, v59\n\t" \
  "v_add_f32 v224, v224, v60\n\t" \
  "v_add_f32 v225, v225, v61\n\t" \
  "v_add_f32 v226, v226, v62\n\t" \
  "v_add_f32 v227, v227, v63\n\t" \
  "s_waitcnt lgkmcnt(0)\n\t" \
  "v_add_f32 v212, v212, v234\n\t" \
  "v_add_f32 v213, v213, v235\n\t" \
  "v_add_f32 v214, v214, v236\n\t" \
  "v_add_f32 v215, v215, v237\n\t" \
  "v_add_f32 v216, v216, v238\n\t" \
  "v_add_f32 v217, v217, v239\n\t" \
  "v_add_f32 v218, v218, v240\n\t" \
  "v_add_f32 v219, v219, v241\n\t" \
  "v_add_f32 v220, v220, v242\n\t" \
  "v_add_f32 v221, v221, v243\n\t" \
  "v_add_f32 v222, v222, v244\n\t" \
  "v_add_f32 v223, v223, v245\n\t" \
  "v_add_f32 v224, v224, v246\n\t" \
  "v_add_f32 v225, v225, v247\n\t" \
  "v_add_f32 v226, v226, v248\n\t" \
  "v_add_f32 v227, v227, v249\n\t" \
  "v_cvt_pk_bf16_f32 v8, v212, v213\n\t" \
  "v_cvt_pk_bf16_f32 v9, v214, v215\n\t" \
  "v_cvt_pk_bf16_f32 v10, v216, v217\n\t" \
  "v_cvt_pk_bf16_f32 v11, v218, v219\n\t" \
  "v_cvt_pk_bf16_f32 v12, v220, v221\n\t" \
  "v_cvt_pk_bf16_f32 v13, v222, v223\n\t" \
  "v_cvt_pk_bf16_f32 v14, v224, v225\n\t" \
  "v_cvt_pk_bf16_f32 v15, v226, v227\n\t" \
  "s_mov_b32 exec_lo, 0xfffff\n\t" \
  "s_mov_b32 exec_hi, 0xfffff\n\t" \
  "buffer_store_dwordx4 v[8:11], v6, s[28:31], 0 offen sc1\n\t" \
  "buffer_store_dwordx4 v[12:15], v6, s[28:31], 0 offen offset:640 sc1\n\t" \
  "s_branch 112f\n\t" \
  "111:\n\t" \
  "s_and_b32 s53, s51, 1\n\t" \
  "s_lshl_b32 s53, s53, 10\n\t" \
  "s_and_b32 s54, s51, 2\n\t" \
  "s_mul_i32 s54, s54, 2176\n\t" \
  "s_add_u32 s53, s53, s54\n\t" \
  "s_add_u32 s53, s53, s52\n\t" \
  "v_add_u32 v6, s53, v110\n\t" \
  "s_lshl_b32 s51, s51, 8\n\t" \
  "s_add_u32 s52, s52, s51\n\t" \
  "v_add_u32 v5, s52, v111\n\t" \
  "s_lshl_b32 s51, s7, 7\n\t" \
  "v_add_u32 v15, s51, v145\n\t" \
  "s_waitcnt vmcnt(6) lgkmcnt(0)\n\t" \
  "ds_read_b128 v[148:151], v228 offset:0\n\t" \
  "ds_read_b64_tr_b16 v[180:181], v232 offset:0\n\t" \
  "ds_read_b64_tr_b16 v[182:183], v232 offset:160\n\t" \
  "ds_read_b128 v[152:155], v229 offset:0\n\t" \
  "ds_read_b64_tr_b16 v[184:185], v232 offset:640\n\t" \
  "ds_read_b64_tr_b16 v[186:187], v232 offset:800\n\t" \
  "ds_read_b128 v[156:159], v230 offset:0\n\t" \
  "ds_read_b64_tr_b16 v[188:189], v232 offset:1280\n\t" \
  "ds_read_b64_tr_b16 v[190:191], v232 offset:1440\n\t" \
  "ds_read_b128 v[160:163], v231 offset:0\n\t" \
  "ds_read_b64_tr_b16 v[192:193], v232 offset:1920\n\t" \
  "ds_read_b64_tr_b16 v[194:195], v232 offset:2080\n\t" \
  "s_mov_b32 exec_lo, 0x100000\n\t" \
  "s_mov_b32 exec_hi, 0x100000\n\t" \
  "ds_read_b128 v[180:183], v250 offset:17408\n\t" \
  "ds_read_b128 v[184:187], v250 offset:17440\n\t" \
  "ds_read_b128 v[188:191], v250 offset:17472\n\t" \
  "ds_read_b128 v[192:195], v250 offset:17504\n\t" \
  "s_mov_b64 exec, -1\n\t" \
  "s_add_u32 s64, s64, 1\n\t" \
  "s_addc_u32 s65, s65, 0\n\t" \
  "s_waitcnt lgkmcnt(3)\n\t" \
  "v_mfma_f32_32x32x16_bf16 v[212:227], v[180:183], v[148:151], 0\n\t" \
  "s_waitcnt lgkmcnt(2)\n\t" \
  "v_mfma_f32_32x32x16_bf16 v[212:227], v[184:187], v[152:155], v[212:227]\n\t" \
  "s_waitcnt lgkmcnt(1)\n\t" \
  "v_mfma_f32_32x32x16_bf16 v[212:227], v[188:191], v[156:159], v[212:227]\n\t" \
  "s_waitcnt lgkmcnt(0)\n\t" \
  "v_mfma_f32_32x32x16_bf16 v[212:227], v[192:195], v[160:163], v[212:227]\n\t" \
  "s_waitcnt vmcnt(0)\n\t" \
  "ds_read_b128 v[164:167], v252 offset:0\n\t" \
  "ds_read_b64_tr_b16 v[196:197], v232 offset:2560\n\t" \
  "ds_read_b64_tr_b16 v[198:199], v232 offset:2720\n\t" \
  "ds_read_b128 v[168:171], v253 offset:0\n\t" \
  "ds_read_b64_tr_b16 v[200:201], v232 offset:3200\n\t" \
  "ds_read_b64_tr_b16 v[202:203], v232 offset:3360\n\t" \
  "ds_read_b128 v[172:175], v254 offset:0\n\t" \
  "ds_read_b64_tr_b16 v[204:205], v232 offset:3840\n\t" \
  "ds_read_b64_tr_b16 v[206:207], v232 offset:4000\n\t" \
  "ds_read_b128 v[176:179], v255 offset:0\n\t" \
  "ds_read_b64_tr_b16 v[208:209], v232 offset:4480\n\t" \
  "ds_read_b64_tr_b16 v[210:211], v232 offset:4640\n\t" \
  "s_mov_b32 exec_lo, 0x100000\n\t" \
  "s_mov_b32 exec_hi, 0x100000\n\t" \
  "ds_read_b128 v[196:199], v250 offset:17536\n\t" \
  "ds_read_b128 v[200:203], v250 offset:17568\n\t" \
  "ds_read_b128 v[204:207], v250 offset:17600\n\t" \
  "ds_read_b128 v[208:211], v250 offset:17632\n\t" \
  "s_mov_b64 exec, -1\n\t" \
  "s_waitcnt lgkmcnt(3)\n\t" \
  "v_mfma_f32_32x32x16_bf16 v[212:227], v[196:199], v[164:167], v[212:227]\n\t" \
  "s_waitcnt lgkmcnt(2)\n\t" \
  "v_mfma_f32_32x32x16_bf16 v[212:227], v[200:203], v[168:171], v[212:227]\n\t" \
  "s_waitcnt lgkmcnt(1)\n\t" \
  "v_mfma_f32_32x32x16_bf16 v[212:227], v[204:207], v[172:175], v[212:227]\n\t" \
  "s_waitcnt lgkmcnt(0)\n\t" \
  "v_mfma_f32_32x32x16_bf16 v[212:227], v[208:211], v[176:179], v[212:227]\n\t" \
  "s_barrier\n\t" \
  "ds_read_b128 v[16:19], v233 offset:5120\n\t" \
  "ds_read_b128 v[20:23], v233 offset:6144\n\t" \
  "ds_read_b128 v[24:27], v233 offset:7168\n\t" \
  "ds_read_b128 v[32:35], v233 offset:8192\n\t" \
  "ds_read_b128 v[36:39], v233 offset:9216\n\t" \
  "ds_read_b128 v[40:43], v233 offset:10240\n\t" \
  "ds_read_b128 v[48:51], v233 offset:11264\n\t" \
  "ds_read_b128 v[52:55], v233 offset:12288\n\t" \
  "ds_read_b128 v[56:59], v233 offset:13312\n\t" \
  "ds_read_b128 v[234:237], v233 offset:14336\n\t" \
  "ds_read_b128 v[238:241], v233 offset:15360\n\t" \
  "ds_read_b128 v[242:245], v233 offset:16384\n\t" \
  "s_waitcnt lgkmcnt(9)\n\t" \
  "v_add_f32 v212, v212, v16\n\t" \
  "v_add_f32 v213, v213, v17\n\t" \
  "v_add_f32 v214, v214, v18\n\t" \
  "v_add_f32 v215, v215, v19\n\t" \
  "v_add_f32 v216, v216, v20\n\t" \
  "v_add_f32 v217, v217, v21\n\t" \
  "v_add_f32 v218, v218, v22\n\t" \
  "v_add_f32 v219, v219, v23\n\t" \
  "v_add_f32 v220, v220, v24\n\t" \
  "v_add_f32 v221, v221, v25\n\t" \
  "v_add_f32 v222, v222, v26\n\t" \
  "v_add_f32 v223, v223, v27\n\t" \
  "s_waitcnt lgkmcnt(6)\n\t" \
  "v_add_f32 v212, v212, v32\n\t" \
  "v_add_f32 v213, v213, v33\n\t" \
  "v_add_f32 v214, v214, v34\n\t" \
  "v_add_f32 v215, v215, v35\n\t" \
  "v_add_f32 v216, v216, v36\n\t" \
  "v_add_f32 v217, v217, v37\n\t" \
  "v_add_f32 v218, v218, v38\n\t" \
  "v_add_f32 v219, v219, v39\n\t" \
  "v_add_f32 v220, v220, v40\n\t" \
  "v_add_f32 v221, v221, v41\n\t" \
  "v_add_f32 v222, v222, v42\n\t" \
  "v_add_f32 v223, v223, v43\n\t" \
  "s_waitcnt lgkmcnt(3)\n\t" \
  "v_add_f32 v212, v212, v48\n\t" \
  "v_add_f32 v213, v213, v49\n\t" \
  "v_add_f32 v214, v214, v50\n\t" \
  "v_add_f32 v215, v215, v51\n\t" \
  "v_add_f32 v216, v216, v52\n\t" \
  "v_add_f32 v217, v217, v53\n\t" \
  "v_add_f32 v218, v218, v54\n\t" \
  "v_add_f32 v219, v219, v55\n\t" \
  "v_add_f32 v220, v220, v56\n\t" \
  "v_add_f32 v221, v221, v57\n\t" \
  "v_add_f32 v222, v222, v58\n\t" \
  "v_add_f32 v223, v223, v59\n\t" \
  "s_waitcnt lgkmcnt(0)\n\t" \
  "v_add_f32 v212, v212, v234\n\t" \
  "v_add_f32 v213, v213, v235\n\t" \
  "v_add_f32 v214, v214, v236\n\t" \
  "v_add_f32 v215, v215, v237\n\t" \
  "v_add_f32 v216, v216, v238\n\t" \
  "v_add_f32 v217, v217, v239\n\t" \
  "v_add_f32 v218, v218, v240\n\t" \
  "v_add_f32 v219, v219, v241\n\t" \
  "v_add_f32 v220, v220, v242\n\t" \
  "v_add_f32 v221, v221, v243\n\t" \
  "v_add_f32 v222, v222, v244\n\t" \
  "v_add_f32 v223, v223, v245\n\t" \
  "v_mov_b32 v14, v220\n\t" \
  "v_mul_f32 v212, 1051011931, v212\n\t" \
  "v_mul_f32 v213, 1051011931, v213\n\t" \
  "v_mul_f32 v214, 1051011931, v214\n\t" \
  "v_mul_f32 v215, 1051011931, v215\n\t" \
  "v_mul_f32 v216, 1051011931, v216\n\t" \
  "v_mul_f32 v217, 1051011931, v217\n\t" \
  "v_mul_f32 v218, 1051011931, v218\n\t" \
  "v_mul_f32 v219, 1051011931, v219\n\t" \
  "v_mul_f32 v220, 1051011931, v220\n\t" \
  "v_mul_f32 v221, 1051011931, v221\n\t" \
  "v_mul_f32 v222, 1051011931, v222\n\t" \
  "v_mul_f32 v223, 1051011931, v223\n\t" \
  "v_cvt_pk_bf16_f32 v8, v212, v213\n\t" \
  "v_cvt_pk_bf16_f32 v9, v214, v215\n\t" \
  "v_cvt_pk_bf16_f32 v10, v216, v217\n\t" \
  "v_cvt_pk_bf16_f32 v11, v218, v219\n\t" \
  "v_cvt_pk_bf16_f32 v12, v220, v221\n\t" \
  "v_cvt_pk_bf16_f32 v13, v222, v223\n\t" \
  "buffer_store_dwordx4 v[8:11], v6, s[28:31], 0 offen sc1\n\t" \
  "s_mov_b32 exec_hi, 0\n\t" \
  "buffer_store_dwordx2 v[12:13], v5, s[28:31], 0 offen offset:2048 sc1\n\t" \
  "s_not_b64 exec, exec\n\t" \
  "buffer_store_dword v14, v15, s[28:31], 0 offen sc1\n\t" \
  "112:\n\t" \
  "s_mov_b64 exec, -1\n\t" \
  "s_barrier\n\t" \
  "s_lshl2_add_u32 s51, s5, s6\n\t" \
  "s_lshl_b32 s52, s4, 10\n\t" \
  "s_add_u32 s52, s52, 4096\n\t" \
  "s_lshl3_add_u32 s51, s51, s52\n\t" \
  "v_mov_b32 v5, s51\n\t" \
  "v_mov_b64 v[148:149], s[64:65]\n\t" \
  "s_and_b32 s51, s51, 0xffffff00\n\t" \
  "v_and_b32 v6, 0xf0, v2\n\t" \
  "v_add_u32 v6, s51, v6\n\t" \
  "s_barrier\n\t" \
  "s_mov_b64 exec, 1\n\t" \
  "s_waitcnt vmcnt(0)\n\t" \
  "buffer_store_dwordx2 v[148:149], v5, s[28:31], 0 offen sc1\n\t" \
  "s_mov_b64 exec, -1\n\t" \
  "s_setprio 0\n\t" \
  "s_barrier\n\t" \
  "s_barrier\n\t" \
  "s_barrier\n\t" \
  "s_barrier\n\t" \
  "s_barrier\n\t" \
  "buffer_load_dwordx4 v[152:155], v6, s[28:31], 0 offen sc1\n\t" \
  "s_barrier\n\t" \
  "s_waitcnt vmcnt(0)\n\t" \
  "v_cmp_ne_u64 vcc, s[64:65], v[152:153]\n\t" \
  "v_cmp_ne_u64 s[36:37], s[64:65], v[154:155]\n\t" \
  "s_or_b64 vcc, vcc, s[36:37]\n\t" \
  "s_cselect_b32 s62, 0, 1\n\t" \
  "s_cbranch_scc1 113f\n\t" \
  "s_add_u32 s50, s10, 0\n\t" \
  "s_and_b32 s50, s50, 3\n\t" \
  "s_lshr_b32 s54, s5, 3\n\t" \
  "s_lshl2_add_u32 s50, s54, s50\n\t" \
  "s_mul_i32 s50, s50, 10240\n\t" \
  "s_add_u32 s58, s48, s50\n\t" \
  "s_addc_u32 s59, s49, 0\n\t" \
  "s_add_u32 s60, s58, 0\n\t" \
  "s_addc_u32 s61, s59, 0\n\t" \
  "s_mov_b32 m0, 0\n\t" \
  "s_nop 0\n\t" \
  "global_load_lds_dwordx4 v2, s[60:61] sc1\n\t" \
  "global_load_lds_dwordx4 v2, s[60:61] offset:1024 sc1\n\t" \
  "global_load_lds_dwordx4 v2, s[60:61] offset:2048 sc1\n\t" \
  "global_load_lds_dwordx4 v2, s[60:61] offset:3072 sc1\n\t" \
  "s_add_u32 s60, s58, 4096\n\t" \
  "s_addc_u32 s61, s59, 0\n\t" \
  "s_mov_b32 m0, 4096\n\t" \
  "s_nop 0\n\t" \
  "global_load_lds_dwordx4 v2, s[60:61] sc1\n\t" \
  "global_load_lds_dwordx4 v2, s[60:61] offset:1024 sc1\n\t" \
  "global_load_lds_dwordx4 v2, s[60:61] offset:2048 sc1\n\t" \
  "global_load_lds_dwordx4 v2, s[60:61] offset:3072 sc1\n\t" \
  "s_add_u32 s60, s58, 8192\n\t" \
  "s_addc_u32 s61, s59, 0\n\t" \
  "s_mov_b32 m0, 8192\n\t" \
  "s_nop 0\n\t" \
  "global_load_lds_dwordx4 v2, s[60:61] sc1\n\t" \
  "global_load_lds_dwordx4 v2, s[60:61] offset:1024 sc1\n\t" \
  "s_add_u32 s50, s10, 1\n\t" \
  "s_and_b32 s50, s50, 3\n\t" \
  "s_lshr_b32 s54, s5, 3\n\t" \
  "s_lshl2_add_u32 s50, s54, s50\n\t" \
  "s_mul_i32 s50, s50, 10240\n\t" \
  "s_add_u32 s58, s48, s50\n\t" \
  "s_addc_u32 s59, s49, 0\n\t" \
  "s_add_u32 s60, s58, 0\n\t" \
  "s_addc_u32 s61, s59, 0\n\t" \
  "s_mov_b32 m0, 10240\n\t" \
  "s_nop 0\n\t" \
  "global_load_lds_dwordx4 v2, s[60:61] sc1\n\t" \
  "global_load_lds_dwordx4 v2, s[60:61] offset:1024 sc1\n\t" \
  "global_load_lds_dwordx4 v2, s[60:61] offset:2048 sc1\n\t" \
  "global_load_lds_dwordx4 v2, s[60:61] offset:3072 sc1\n\t" \
  "113:\n\t" \
  "s_barrier\n\t" \
  "s_cmp_eq_u32 s62, 0\n\t" \
  "s_cbranch_scc0 117f\n\t" \
  "s_mov_b32 s40, 0x40000\n\t" \
  "115:\n\t" \
  "buffer_load_dwordx4 v[152:155], v6, s[28:31], 0 offen sc1\n\t" \
  "s_waitcnt vmcnt(0)\n\t" \
  "v_cmp_ne_u64 vcc, s[64:65], v[152:153]\n\t" \
  "v_cmp_ne_u64 s[36:37], s[64:65], v[154:155]\n\t" \
  "s_or_b64 vcc, vcc, s[36:37]\n\t" \
  "s_cbranch_scc0 116f\n\t" \
  "s_sleep 1\n\t" \
  "s_sub_u32 s40, s40, 1\n\t" \
  "s_cmp_lg_u32 s40, 0\n\t" \
  "s_cbranch_scc1 115b\n\t" \
  "116:\n\t" \
  "s_add_u32 s50, s10, 0\n\t" \
  "s_and_b32 s50, s50, 3\n\t" \
  "s_lshr_b32 s54, s5, 3\n\t" \
  "s_lshl2_add_u32 s50, s54, s50\n\t" \
  "s_mul_i32 s50, s50, 10240\n\t" \
  "s_add_u32 s58, s48, s50\n\t" \
  "s_addc_u32 s59, s49, 0\n\t" \
  "s_add_u32 s60, s58, 0\n\t" \
  "s_addc_u32 s61, s59, 0\n\t" \
  "s_mov_b32 m0, 0\n\t" \
  "s_nop 0\n\t" \
  "global_load_lds_dwordx4 v2, s[60:61] sc1\n\t" \
  "global_load_lds_dwordx4 v2, s[60:61] offset:1024 sc1\n\t" \
  "global_load_lds_dwordx4 v2, s[60:61] offset:2048 sc1\n\t" \
  "global_load_lds_dwordx4 v2, s[60:61] offset:3072 sc1\n\t" \
  "s_add_u32 s60, s58, 4096\n\t" \
  "s_addc_u32 s61, s59, 0\n\t" \
  "s_mov_b32 m0, 4096\n\t" \
  "s_nop 0\n\t" \
  "global_load_lds_dwordx4 v2, s[60:61] sc1\n\t" \
  "global_load_lds_dwordx4 v2, s[60:61] offset:1024 sc1\n\t" \
  "global_load_lds_dwordx4 v2, s[60:61] offset:2048 sc1\n\t" \
  "global_load_lds_dwordx4 v2, s[60:61] offset:3072 sc1\n\t" \
  "s_add_u32 s60, s58, 8192\n\t" \
  "s_addc_u32 s61, s59, 0\n\t" \
  "s_mov_b32 m0, 8192\n\t" \
  "s_nop 0\n\t" \
  "global_load_lds_dwordx4 v2, s[60:61] sc1\n\t" \
  "global_load_lds_dwordx4 v2, s[60:61] offset:1024 sc1\n\t" \
  "s_add_u32 s50, s10, 1\n\t" \
  "s_and_b32 s50, s50, 3\n\t" \
  "s_lshr_b32 s54, s5, 3\n\t" \
  "s_lshl2_add_u32 s50, s54, s50\n\t" \
  "s_mul_i32 s50, s50, 10240\n\t" \
  "s_add_u32 s58, s48, s50\n\t" \
  "s_addc_u32 s59, s49, 0\n\t" \
  "s_add_u32 s60, s58, 0\n\t" \
  "s_addc_u32 s61, s59, 0\n\t" \
  "s_mov_b32 m0, 10240\n\t" \
  "s_nop 0\n\t" \
  "global_load_lds_dwordx4 v2, s[60:61] sc1\n\t" \
  "global_load_lds_dwordx4 v2, s[60:61] offset:1024 sc1\n\t" \
  "global_load_lds_dwordx4 v2, s[60:61] offset:2048 sc1\n\t" \
  "global_load_lds_dwordx4 v2, s[60:61] offset:3072 sc1\n\t" \
  "117:\n\t" \
  "s_waitcnt vmcnt(0)\n\t" \
    \
  "120:\n\t" \
  "s_setprio 0\n\t" \
  "s_barrier\n\t" \
  "s_lshr_b32 s50, s3, 2\n\t" \
  "s_and_b32 s51, s3, 3\n\t" \
  "s_lshl2_add_u32 s52, s2, s51\n\t" \
  "s_lshl3_add_u32 s42, s52, 8192\n\t" \
  "s_lshl3_add_u32 s53, s6, s3\n\t" \
  "s_mul_i32 s43, s53, 51200\n\t" \
  "s_add_u32 s43, s43, 393216\n\t" \
  "s_lshr_b32 s54, s2, 5\n\t" \
  "s_mul_i32 s44, s54, 2944\n\t" \
  "s_add_u32 s44, s44, 27648\n\t" \
  "s_and_b32 s54, s3, 1\n\t" \
  "s_lshl1_add_u32 s54, s5, s54\n\t" \
  "s_mul_i32 s45, s54, 81920\n\t" \
  "s_lshl1_add_u32 s54, s50, s4\n\t" \
  "s_lshl2_add_u32 s54, s54, s6\n\t" \
  "s_bfe_u32 s55, s3, 0x10001\n\t" \
  "s_lshl1_add_u32 s54, s54, s55\n\t" \
  "s_mul_i32 s54, s54, 2560\n\t" \
  "s_add_u32 s45, s45, s54\n\t" \
  "s_add_u32 s45, s45, 2031616\n\t" \
  "s_lshl1_add_u32 s54, s5, s4\n\t" \
  "s_lshl3_add_u32 s54, s54, s3\n\t" \
  "s_mul_i32 s46, s54, 10240\n\t" \
  "s_add_u32 s46, s46, 2031616\n\t" \
  "s_xor_b32 s47, s3, 4\n\t" \
  "s_mul_i32 s47, s47, 20480\n\t" \
  "s_mul_i32 s53, s53, 50\n\t" \
  "v_add_u32 v8, s53, v1\n\t" \
  "v_and_b32 v8, 0x3ff, v8\n\t" \
  "s_and_b32 s54, s2, 230\n\t" \
  "s_lshl_b32 s55, s55, 0\n\t" \
  "s_or_b32 s54, s54, s55\n\t" \
  "s_and_b32 s56, s3, 1\n\t" \
  "s_lshl4_add_u32 s54, s56, s54\n\t" \
  "s_lshl2_add_u32 s54, s54, s4\n\t" \
  "v_and_b32 v9, 2, v1\n\t" \
  "v_lshlrev_b32 v9, 4, v9\n\t" \
  "v_and_b32 v10, 1, v1\n\t" \
  "v_lshl_add_u32 v9, v10, 1, v9\n\t" \
  "v_add_u32 v9, s54, v9\n\t" \
  "v_cmp_gt_u32 vcc, 56, v1\n\t" \
  "v_mov_b32 v10, s52\n\t" \
  "s_nop 0\n\t" \
  "v_cndmask_b32 v9, v10, v9, vcc\n\t" \
  "v_cmp_gt_u32 vcc, 50, v1\n\t" \
  "s_nop 1\n\t" \
  "v_cndmask_b32 v8, v9, v8, vcc\n\t" \
  "v_lshlrev_b32 v8, 3, v8\n\t" \
  "v_add_u32 v147, 8192, v8\n\t" \
  "v_min_u32 v146, 22, v1\n\t" \
  "v_lshlrev_b32 v146, 7, v146\n\t" \
  "v_mov_b32 v0, v2\n\t" \
  "v_lshlrev_b32 v1, 3, v1\n\t" \
  "v_add_u32 v14, s45, v0\n\t" \
  "v_add_u32 v15, s45, v1\n\t" \
  "s_add_u32 s53, s51, 4\n\t" \
  "s_mul_i32 s53, s53, 10240\n\t" \
  "v_add_u32 v199, s53, v2\n\t" \
  "v_mov_b32 v200, -1\n\t" \
  "v_mov_b32 v201, -1\n\t" \
  "v_mov_b32 v202, -1\n\t" \
  "v_mov_b32 v203, -1\n\t" \
  "v_subrev_u32 v206, 40960, v14\n\t" \
  "v_subrev_u32 v207, 40960, v15\n\t" \
  "s_mov_b32 s57, s52\n\t" \
  "s_lshl_b32 s50, s4, 10\n\t" \
  "s_add_u32 s50, s50, 4096\n\t" \
  "v_add_u32 v156, s50, v2\n\t" \
  "s_cmp_eq_u32 s51, 0\n\t" \
  "s_cbranch_scc1 122f\n\t" \
  "s_add_u32 s50, s10, s51\n\t" \
  "s_and_b32 s50, s50, 3\n\t" \
  "s_lshr_b32 s54, s5, 3\n\t" \
  "s_lshl2_add_u32 s50, s54, s50\n\t" \
  "s_mul_i32 s50, s50, 10240\n\t" \
  "s_add_u32 s58, s48, s50\n\t" \
  "s_addc_u32 s59, s49, 0\n\t" \
  "s_mul_i32 s53, s51, 10240\n\t" \
  "s_cmp_eq_u32 s51, 1\n\t" \
  "s_cbranch_scc1 121f\n\t" \
  "s_add_u32 s60, s58, 0\n\t" \
  "s_addc_u32 s61, s59, 0\n\t" \
  "s_add_u32 m0, s53, 0\n\t" \
  "s_nop 0\n\t" \
  "global_load_lds_dwordx4 v2, s[60:61] sc1\n\t" \
  "global_load_lds_dwordx4 v2, s[60:61] offset:1024 sc1\n\t" \
  "global_load_lds_dwordx4 v2, s[60:61] offset:2048 sc1\n\t" \
  "global_load_lds_dwordx4 v2, s[60:61] offset:3072 sc1\n\t" \
  "121:\n\t" \
  "s_add_u32 s60, s58, 4096\n\t" \
  "s_addc_u32 s61, s59, 0\n\t" \
  "s_add_u32 m0, s53, 4096\n\t" \
  "s_nop 0\n\t" \
  "global_load_lds_dwordx4 v2, s[60:61] sc1\n\t" \
  "global_load_lds_dwordx4 v2, s[60:61] offset:1024 sc1\n\t" \
  "global_load_lds_dwordx4 v2, s[60:61] offset:2048 sc1\n\t" \
  "global_load_lds_dwordx4 v2, s[60:61] offset:3072 sc1\n\t" \
  "s_add_u32 s60, s58, 8192\n\t" \
  "s_addc_u32 s61, s59, 0\n\t" \
  "s_add_u32 m0, s53, 8192\n\t" \
  "s_nop 0\n\t" \
  "global_load_lds_dwordx4 v2, s[60:61] sc1\n\t" \
  "global_load_lds_dwordx4 v2, s[60:61] offset:1024 sc1\n\t" \
  "122:\n\t" \
  "buffer_load_dwordx4 v[152:155], v156, s[28:31], 0 offen sc1\n\t" \
  "s_waitcnt vmcnt(0)\n\t" \
  "v_cmp_ne_u64 vcc, s[64:65], v[152:153]\n\t" \
  "v_cmp_ne_u64 s[36:37], s[64:65], v[154:155]\n\t" \
  "s_or_b64 vcc, vcc, s[36:37]\n\t" \
  "s_cselect_b32 s62, 0, 1\n\t" \
  "s_cbranch_scc1 125f\n\t" \
  "s_add_u32 s52, s51, 8\n\t" \
  "s_add_u32 s50, s10, s52\n\t" \
  "s_mul_i32 s54, s50, 43\n\t" \
  "s_lshr_b32 s54, s54, 9\n\t" \
  "s_mul_i32 s54, s54, 12\n\t" \
  "s_sub_u32 s50, s50, s54\n\t" \
  "s_lshr_b32 s54, s5, 3\n\t" \
  "s_lshl2_add_u32 s50, s54, s50\n\t" \
  "s_add_u32 s50, s50, 4\n\t" \
  "s_and_b32 s50, s50, 15\n\t" \
  "s_mul_i32 s50, s50, 10240\n\t" \
  "s_add_u32 s58, s48, s50\n\t" \
  "s_addc_u32 s59, s49, 0\n\t" \
  "s_mul_i32 s53, s52, 10240\n\t" \
  "s_add_u32 s60, s58, 0\n\t" \
  "s_addc_u32 s61, s59, 0\n\t" \
  "s_add_u32 m0, s53, 0\n\t" \
  "s_nop 0\n\t" \
  "global_load_lds_dwordx4 v2, s[60:61] sc1\n\t" \
  "global_load_lds_dwordx4 v2, s[60:61] offset:1024 sc1\n\t" \
  "global_load_lds_dwordx4 v2, s[60:61] offset:2048 sc1\n\t" \
  "global_load_lds_dwordx4 v2, s[60:61] offset:3072 sc1\n\t" \
  "s_add_u32 s60, s58, 4096\n\t" \
  "s_addc_u32 s61, s59, 0\n\t" \
  "s_add_u32 m0, s53, 4096\n\t" \
  "s_nop 0\n\t" \
  "global_load_lds_dwordx4 v2, s[60:61] sc1\n\t" \
  "global_load_lds_dwordx4 v2, s[60:61] offset:1024 sc1\n\t" \
  "global_load_lds_dwordx4 v2, s[60:61] offset:2048 sc1\n\t" \
  "global_load_lds_dwordx4 v2, s[60:61] offset:3072 sc1\n\t" \
  "s_add_u32 s60, s58, 8192\n\t" \
  "s_addc_u32 s61, s59, 0\n\t" \
  "s_add_u32 m0, s53, 8192\n\t" \
  "s_nop 0\n\t" \
  "global_load_lds_dwordx4 v2, s[60:61] sc1\n\t" \
  "global_load_lds_dwordx4 v2, s[60:61] offset:1024 sc1\n\t" \
  "125:\n\t" \
  "s_barrier\n\t" \
  "ds_read_b128 v[100:103], v199 offset:0\n\t" \
  "ds_read_b128 v[104:107], v199 offset:1024\n\t" \
  "ds_read_b64 v[108:109], v199 offset:2048\n\t" \
  "ds_read2st64_b64 v[84:87], v111 offset0:4 offset1:5\n\t" \
  "ds_read_b128 v[80:83], v110 offset:0\n\t" \
  "s_mov_b64 exec, s[34:35]\n\t" \
  "ds_read_b128 v[92:95], v110 offset:3072\n\t" \
  "s_mov_b64 exec, -1\n\t" \
  "s_cmp_eq_u32 s62, 0\n\t" \
  "s_cbranch_scc0 126f\n\t" \
  "s_mov_b32 s40, 0x40000\n\t" \
  "123:\n\t" \
  "buffer_load_dwordx4 v[152:155], v156, s[28:31], 0 offen sc1\n\t" \
  "s_waitcnt vmcnt(0)\n\t" \
  "v_cmp_ne_u64 vcc, s[64:65], v[152:153]\n\t" \
  "v_cmp_ne_u64 s[36:37], s[64:65], v[154:155]\n\t" \
  "s_or_b64 vcc, vcc, s[36:37]\n\t" \
  "s_cbranch_scc0 124f\n\t" \
  "s_sleep 2\n\t" \
  "s_sub_u32 s40, s40, 1\n\t" \
  "s_cmp_lg_u32 s40, 0\n\t" \
  "s_cbranch_scc1 123b\n\t" \
  "124:\n\t" \
  "s_add_u32 s52, s51, 8\n\t" \
  "s_add_u32 s50, s10, s52\n\t" \
  "s_mul_i32 s54, s50, 43\n\t" \
  "s_lshr_b32 s54, s54, 9\n\t" \
  "s_mul_i32 s54, s54, 12\n\t" \
  "s_sub_u32 s50, s50, s54\n\t" \
  "s_lshr_b32 s54, s5, 3\n\t" \
  "s_lshl2_add_u32 s50, s54, s50\n\t" \
  "s_add_u32 s50, s50, 4\n\t" \
  "s_and_b32 s50, s50, 15\n\t" \
  "s_mul_i32 s50, s50, 10240\n\t" \
  "s_add_u32 s58, s48, s50\n\t" \
  "s_addc_u32 s59, s49, 0\n\t" \
  "s_mul_i32 s53, s52, 10240\n\t" \
  "s_add_u32 s60, s58, 0\n\t" \
  "s_addc_u32 s61, s59, 0\n\t" \
  "s_add_u32 m0, s53, 0\n\t" \
  "s_nop 0\n\t" \
  "global_load_lds_dwordx4 v2, s[60:61] sc1\n\t" \
  "global_load_lds_dwordx4 v2, s[60:61] offset:1024 sc1\n\t" \
  "global_load_lds_dwordx4 v2, s[60:61] offset:2048 sc1\n\t" \
  "global_load_lds_dwordx4 v2, s[60:61] offset:3072 sc1\n\t" \
  "s_add_u32 s60, s58, 4096\n\t" \
  "s_addc_u32 s61, s59, 0\n\t" \
  "s_add_u32 m0, s53, 4096\n\t" \
  "s_nop 0\n\t" \
  "global_load_lds_dwordx4 v2, s[60:61] sc1\n\t" \
  "global_load_lds_dwordx4 v2, s[60:61] offset:1024 sc1\n\t" \
  "global_load_lds_dwordx4 v2, s[60:61] offset:2048 sc1\n\t" \
  "global_load_lds_dwordx4 v2, s[60:61] offset:3072 sc1\n\t" \
  "s_add_u32 s60, s58, 8192\n\t" \
  "s_addc_u32 s61, s59, 0\n\t" \
  "s_add_u32 m0, s53, 8192\n\t" \
  "s_nop 0\n\t" \
  "global_load_lds_dwordx4 v2, s[60:61] sc1\n\t" \
  "global_load_lds_dwordx4 v2, s[60:61] offset:1024 sc1\n\t" \
  "126:\n\t" \
  "s_add_u32 s52, s51, 12\n\t" \
  "s_add_u32 s50, s10, s52\n\t" \
  "s_mul_i32 s54, s50, 43\n\t" \
  "s_lshr_b32 s54, s54, 9\n\t" \
  "s_mul_i32 s54, s54, 12\n\t" \
  "s_sub_u32 s50, s50, s54\n\t" \
  "s_lshr_b32 s54, s5, 3\n\t" \
  "s_lshl2_add_u32 s50, s54, s50\n\t" \
  "s_add_u32 s50, s50, 4\n\t" \
  "s_and_b32 s50, s50, 15\n\t" \
  "s_mul_i32 s50, s50, 10240\n\t" \
  "s_add_u32 s58, s48, s50\n\t" \
  "s_addc_u32 s59, s49, 0\n\t" \
  "s_mul_i32 s53, s52, 10240\n\t" \
  "s_add_u32 s60, s58, 0\n\t" \
  "s_addc_u32 s61, s59, 0\n\t" \
  "s_add_u32 m0, s53, 0\n\t" \
  "s_nop 0\n\t" \
  "global_load_lds_dwordx4 v2, s[60:61] sc1\n\t" \
  "global_load_lds_dwordx4 v2, s[60:61] offset:1024 sc1\n\t" \
  "global_load_lds_dwordx4 v2, s[60:61] offset:2048 sc1\n\t" \
  "global_load_lds_dwordx4 v2, s[60:61] offset:3072 sc1\n\t" \
  "s_add_u32 s60, s58, 4096\n\t" \
  "s_addc_u32 s61, s59, 0\n\t" \
  "s_add_u32 m0, s53, 4096\n\t" \
  "s_nop 0\n\t" \
  "global_load_lds_dwordx4 v2, s[60:61] sc1\n\t" \
  "global_load_lds_dwordx4 v2, s[60:61] offset:1024 sc1\n\t" \
  "global_load_lds_dwordx4 v2, s[60:61] offset:2048 sc1\n\t" \
  "global_load_lds_dwordx4 v2, s[60:61] offset:3072 sc1\n\t" \
  "s_add_u32 s60, s58, 8192\n\t" \
  "s_addc_u32 s61, s59, 0\n\t" \
  "s_add_u32 m0, s53, 8192\n\t" \
  "s_nop 0\n\t" \
  "global_load_lds_dwordx4 v2, s[60:61] sc1\n\t" \
  "global_load_lds_dwordx4 v2, s[60:61] offset:1024 sc1\n\t" \
  "s_waitcnt lgkmcnt(0)\n\t" \
  "s_add_u32 s52, s51, 4\n\t" \
  "s_add_u32 s50, s10, s52\n\t" \
  "s_mul_i32 s54, s50, 43\n\t" \
  "s_lshr_b32 s54, s54, 9\n\t" \
  "s_mul_i32 s54, s54, 12\n\t" \
  "s_sub_u32 s50, s50, s54\n\t" \
  "s_lshr_b32 s54, s5, 3\n\t" \
  "s_lshl2_add_u32 s50, s54, s50\n\t" \
  "s_add_u32 s50, s50, 4\n\t" \
  "s_and_b32 s50, s50, 15\n\t" \
  "s_mul_i32 s50, s50, 10240\n\t" \
  "s_add_u32 s58, s48, s50\n\t" \
  "s_addc_u32 s59, s49, 0\n\t" \
  "s_mul_i32 s53, s52, 10240\n\t" \
  "s_add_u32 s60, s58, 0\n\t" \
  "s_addc_u32 s61, s59, 0\n\t" \
  "s_add_u32 m0, s53, 0\n\t" \
  "s_nop 0\n\t" \
  "global_load_lds_dwordx4 v2, s[60:61] sc1\n\t" \
  "global_load_lds_dwordx4 v2, s[60:61] offset:1024 sc1\n\t" \
  "global_load_lds_dwordx4 v2, s[60:61] offset:2048 sc1\n\t" \
  "global_load_lds_dwordx4 v2, s[60:61] offset:3072 sc1\n\t" \
  "s_add_u32 s60, s58, 4096\n\t" \
  "s_addc_u32 s61, s59, 0\n\t" \
  "s_add_u32 m0, s53, 4096\n\t" \
  "s_nop 0\n\t" \
  "global_load_lds_dwordx4 v2, s[60:61] sc1\n\t" \
  "global_load_lds_dwordx4 v2, s[60:61] offset:1024 sc1\n\t" \
  "global_load_lds_dwordx4 v2, s[60:61] offset:2048 sc1\n\t" \
  "global_load_lds_dwordx4 v2, s[60:61] offset:3072 sc1\n\t" \
  "s_add_u32 s60, s58, 8192\n\t" \
  "s_addc_u32 s61, s59, 0\n\t" \
  "s_add_u32 m0, s53, 8192\n\t" \
  "s_nop 0\n\t" \
  "global_load_lds_dwordx4 v2, s[60:61] sc1\n\t" \
  "global_load_lds_dwordx4 v2, s[60:61] offset:1024 sc1\n\t" \
  "buffer_load_dword v145, v145, s[28:31], 0 offen sc1\n\t" \
  "v_mfma_f32_32x32x4_2b_bf16 v[16:47], v[84:85], v[108:109], 4.0 cbsz:1 abid:0\n\t" \
  "s_lshl_b32 s50, s57, 10\n\t" \
  "s_add_u32 s50, s50, 393216\n\t" \
  "v_add_u32 v210, s50, v2\n\t" \
  "s_mul_i32 s50, s57, 52429\n\t" \
  "s_lshr_b32 s50, s50, 19\n\t" \
  "s_mul_i32 s51, s50, 10\n\t" \
  "s_sub_u32 s51, s57, s51\n\t" \
  "s_mul_i32 s52, s50, 52429\n\t" \
  "s_lshr_b32 s52, s52, 18\n\t" \
  "s_mul_i32 s53, s52, 5\n\t" \
  "s_sub_u32 s53, s50, s53\n\t" \
  "s_lshr_b32 s50, s52, 3\n\t" \
  "s_mul_i32 s50, s50, 320\n\t" \
  "s_lshl_b32 s53, s53, 6\n\t" \
  "s_add_u32 s53, s53, s50\n\t" \
  "s_bitcmp1_b32 s52, 1\n\t" \
  "s_cselect_b64 s[66:67], s[26:27], s[24:25]\n\t" \
  "s_and_b32 s50, s52, 1\n\t" \
  "s_lshl_b32 s50, s50, 4\n\t" \
  "s_bfe_u32 s52, s52, 0x10002\n\t" \
  "s_lshl1_add_u32 s50, s52, s50\n\t" \
  "s_and_b32 s52, s51, 6\n\t" \
  "s_add_u32 s52, s52, s51\n\t" \
  "s_mul_i32 s52, s52, 20\n\t" \
  "s_and_b32 s54, s51, 1\n\t" \
  "s_mul_i32 s54, s54, 160\n\t" \
  "s_add_u32 s54, s54, 16\n\t" \
  "s_cmp_lt_u32 s51, 8\n\t" \
  "s_cselect_b32 s52, s52, s54\n\t" \
  "s_cselect_b32 s54, 2, 1\n\t" \
  "s_cselect_b32 s55, 1, 10\n\t" \
  "s_cselect_b32 s56, 4, 40\n\t" \
  "s_mul_i32 s50, s50, 20\n\t" \
  "s_add_u32 s50, s50, s52\n\t" \
  "s_mul_i32 s50, s50, 1280\n\t" \
  "s_add_u32 s50, s50, s53\n\t" \
  "s_add_u32 s66, s66, s50\n\t" \
  "s_addc_u32 s67, s67, 0\n\t" \
  "s_mul_i32 s54, s54, 2560\n\t" \
  "s_mul_i32 s68, s55, 2560\n\t" \
  "s_mul_i32 s69, s56, 2560\n\t" \
  "v_lshlrev_b32 v208, 1, v3\n\t" \
  "v_mad_u32_u24 v208, v4, s54, v208\n\t" \
  "v_mfma_f32_32x32x16_bf16 v[16:31], v[80:83], v[100:103], v[16:31]\n\t" \
  "v_mfma_f32_32x32x16_bf16 v[32:47], v[80:83], v[104:107], v[32:47]\n\t" \
  "ds_read_b128 v[80:83], v110 offset:1024\n\t" \
  "s_add_u32 s50, s57, 1024\n\t" \
  "s_cmp_lt_u32 s50, 1600\n\t" \
  "s_cselect_b32 s57, s50, s57\n\t" \
  "s_lshl_b32 s50, s57, 10\n\t" \
  "s_add_u32 s50, s50, 393216\n\t" \
  "v_add_u32 v211, s50, v2\n\t" \
  "s_mul_i32 s50, s57, 52429\n\t" \
  "s_lshr_b32 s50, s50, 19\n\t" \
  "s_mul_i32 s51, s50, 10\n\t" \
  "s_sub_u32 s51, s57, s51\n\t" \
  "s_mul_i32 s52, s50, 52429\n\t" \
  "s_lshr_b32 s52, s52, 18\n\t" \
  "s_mul_i32 s53, s52, 5\n\t" \
  "s_sub_u32 s53, s50, s53\n\t" \
  "s_lshr_b32 s50, s52, 3\n\t" \
  "s_mul_i32 s50, s50, 320\n\t" \
  "s_lshl_b32 s53, s53, 6\n\t" \
  "s_add_u32 s53, s53, s50\n\t" \
  "s_bitcmp1_b32 s52, 1\n\t" \
  "s_cselect_b64 s[70:71], s[26:27], s[24:25]\n\t" \
  "s_and_b32 s50, s52, 1\n\t" \
  "s_lshl_b32 s50, s50, 4\n\t" \
  "s_bfe_u32 s52, s52, 0x10002\n\t" \
  "s_lshl1_add_u32 s50, s52, s50\n\t" \
  "s_and_b32 s52, s51, 6\n\t" \
  "s_add_u32 s52, s52, s51\n\t" \
  "s_mul_i32 s52, s52, 20\n\t" \
  "s_and_b32 s54, s51, 1\n\t" \
  "s_mul_i32 s54, s54, 160\n\t" \
  "s_add_u32 s54, s54, 16\n\t" \
  "s_cmp_lt_u32 s51, 8\n\t" \
  "s_cselect_b32 s52, s52, s54\n\t" \
  "s_cselect_b32 s54, 2, 1\n\t" \
  "s_cselect_b32 s55, 1, 10\n\t" \
  "s_cselect_b32 s56, 4, 40\n\t" \
  "s_mul_i32 s50, s50, 20\n\t" \
  "s_add_u32 s50, s50, s52\n\t" \
  "s_mul_i32 s50, s50, 1280\n\t" \
  "s_add_u32 s50, s50, s53\n\t" \
  "s_add_u32 s70, s70, s50\n\t" \
  "s_addc_u32 s71, s71, 0\n\t" \
  "s_mul_i32 s54, s54, 2560\n\t" \
  "s_mul_i32 s72, s55, 2560\n\t" \
  "s_mul_i32 s73, s56, 2560\n\t" \
  "v_lshlrev_b32 v209, 1, v3\n\t" \
  "v_mad_u32_u24 v209, v4, s54, v209\n\t" \
    \
    \
  "v_exp_f32 v16, v16\n\t" \
  "v_exp_f32 v17, v17\n\t" \
  "v_smfmac_f32_32x32x32_bf16 v[128:143], v[88:91], v[72:79], v144\n\t" \
  "s_mov_b64 exec, s[34:35]\n\t" \
  "ds_read_b128 v[88:91], v110 offset:3712\n\t" \
  "ds_read_b128 v[96:99], v110 offset:6400\n\t" \
  "s_mov_b64 exec, -1\n\t" \
  "v_exp_f32 v18, v18\n\t" \
  "v_exp_f32 v19, v19\n\t" \
  "v_exp_f32 v20, v20\n\t" \
  "v_exp_f32 v21, v21\n\t" \
  "v_mfma_f32_32x32x4_2b_bf16 v[48:79], v[84:85], v[108:109], 4.0 cbsz:1 abid:1\n\t" \
  "v_exp_f32 v22, v22\n\t" \
  "v_exp_f32 v23, v23\n\t" \
  "v_exp_f32 v24, v24\n\t" \
  "v_exp_f32 v25, v25\n\t" \
  "v_exp_f32 v26, v26\n\t" \
  "v_exp_f32 v27, v27\n\t" \
  "v_exp_f32 v28, v28\n\t" \
  "v_exp_f32 v29, v29\n\t" \
  "v_smfmac_f32_32x32x32_bf16 v[112:127], v[92:95], v[16:23], v144\n\t" \
  "s_waitcnt lgkmcnt(0)\n\t" \
  "v_exp_f32 v30, v30\n\t" \
  "v_exp_f32 v31, v31\n\t" \
  "v_exp_f32 v32, v32\n\t" \
  "v_exp_f32 v33, v33\n\t" \
  "v_smfmac_f32_32x32x32_bf16 v[112:127], v[88:91], v[24:31], v144\n\t" \
  "v_exp_f32 v34, v34\n\t" \
  "v_exp_f32 v35, v35\n\t" \
  "v_exp_f32 v36, v36\n\t" \
  "v_exp_f32 v37, v37\n\t" \
  "v_mfma_f32_32x32x16_bf16 v[48:63], v[80:83], v[100:103], v[48:63]\n\t" \
  "v_exp_f32 v38, v38\n\t" \
  "v_exp_f32 v39, v39\n\t" \
  "v_exp_f32 v40, v40\n\t" \
  "v_exp_f32 v41, v41\n\t" \
  "v_mfma_f32_32x32x16_bf16 v[64:79], v[80:83], v[104:107], v[64:79]\n\t" \
  "ds_read_b128 v[80:83], v110 offset:4352\n\t" \
  "v_exp_f32 v42, v42\n\t" \
  "v_exp_f32 v43, v43\n\t" \
  "v_exp_f32 v44, v44\n\t" \
  "v_exp_f32 v45, v45\n\t" \
  "v_smfmac_f32_32x32x32_bf16 v[128:143], v[92:95], v[32:39], v144\n\t" \
  "s_waitcnt vmcnt(0)\n\t" \
  "s_barrier\n\t" \
  "s_setprio 2\n\t" \
  "v_exp_f32 v46, v46\n\t" \
  "v_exp_f32 v47, v47\n\t" \
    \
  "v_exp_f32 v48, v48\n\t" \
  "v_exp_f32 v49, v49\n\t" \
  "v_smfmac_f32_32x32x32_bf16 v[128:143], v[88:91], v[40:47], v144\n\t" \
  "s_mov_b64 exec, s[34:35]\n\t" \
  "ds_read_b128 v[88:91], v110 offset:7040\n\t" \
  "ds_read_b128 v[92:95], v110 offset:7680\n\t" \
  "s_mov_b64 exec, -1\n\t" \
  "v_exp_f32 v50, v50\n\t" \
  "v_exp_f32 v51, v51\n\t" \
  "v_exp_f32 v52, v52\n\t" \
  "v_exp_f32 v53, v53\n\t" \
  "v_mfma_f32_32x32x4_2b_bf16 v[16:47], v[86:87], v[108:109], 4.0 cbsz:1 abid:0\n\t" \
  "v_exp_f32 v54, v54\n\t" \
  "v_exp_f32 v55, v55\n\t" \
  "v_exp_f32 v56, v56\n\t" \
  "v_exp_f32 v57, v57\n\t" \
  "v_exp_f32 v58, v58\n\t" \
  "v_exp_f32 v59, v59\n\t" \
  "v_exp_f32 v60, v60\n\t" \
  "v_exp_f32 v61, v61\n\t" \
  "v_smfmac_f32_32x32x32_bf16 v[112:127], v[96:99], v[48:55], v144\n\t" \
  "s_waitcnt lgkmcnt(0)\n\t" \
  "global_load_ushort v168, v208, s[66:67]\n\t" \
  "v_exp_f32 v62, v62\n\t" \
  "v_exp_f32 v63, v63\n\t" \
  "v_exp_f32 v64, v64\n\t" \
  "v_exp_f32 v65, v65\n\t" \
  "v_smfmac_f32_32x32x32_bf16 v[112:127], v[88:91], v[56:63], v144\n\t" \
  "global_load_ushort v176, v208, s[66:67] offset:1280\n\t" \
  "v_exp_f32 v66, v66\n\t" \
  "v_exp_f32 v67, v67\n\t" \
  "v_exp_f32 v68, v68\n\t" \
  "v_exp_f32 v69, v69\n\t" \
  "v_mfma_f32_32x32x16_bf16 v[16:31], v[80:83], v[100:103], v[16:31]\n\t" \
  "s_add_u32 s60, s66, s68\n\t" \
  "s_addc_u32 s61, s67, 0\n\t" \
  "global_load_ushort v169, v208, s[60:61]\n\t" \
  "v_exp_f32 v70, v70\n\t" \
  "v_exp_f32 v71, v71\n\t" \
  "v_exp_f32 v72, v72\n\t" \
  "v_exp_f32 v73, v73\n\t" \
  "v_mfma_f32_32x32x16_bf16 v[32:47], v[80:83], v[104:107], v[32:47]\n\t" \
  "ds_read_b128 v[80:83], v110 offset:5376\n\t" \
  "v_exp_f32 v74, v74\n\t" \
  "v_exp_f32 v75, v75\n\t" \
  "v_exp_f32 v76, v76\n\t" \
  "v_exp_f32 v77, v77\n\t" \
  "v_smfmac_f32_32x32x32_bf16 v[128:143], v[96:99], v[64:71], v144\n\t" \
  "global_load_ushort v177, v208, s[60:61] offset:1280\n\t" \
  "v_exp_f32 v78, v78\n\t" \
  "v_exp_f32 v79, v79\n\t" \
    \
  "v_exp_f32 v16, v16\n\t" \
  "v_exp_f32 v17, v17\n\t" \
  "v_smfmac_f32_32x32x32_bf16 v[128:143], v[88:91], v[72:79], v144\n\t" \
  "s_mov_b64 exec, s[34:35]\n\t" \
  "ds_read_b128 v[88:91], v110 offset:8320\n\t" \
  "ds_read_b128 v[96:99], v110 offset:8960\n\t" \
  "s_mov_b64 exec, -1\n\t" \
  "v_exp_f32 v18, v18\n\t" \
  "v_exp_f32 v19, v19\n\t" \
  "v_exp_f32 v20, v20\n\t" \
  "v_exp_f32 v21, v21\n\t" \
  "v_mfma_f32_32x32x4_2b_bf16 v[48:79], v[86:87], v[108:109], 4.0 cbsz:1 abid:1\n\t" \
  "ds_read2st64_b64 v[84:87], v111 offset0:24 offset1:25\n\t" \
  "v_exp_f32 v22, v22\n\t" \
  "v_exp_f32 v23, v23\n\t" \
  "v_exp_f32 v24, v24\n\t" \
  "v_exp_f32 v25, v25\n\t" \
  "v_exp_f32 v26, v26\n\t" \
  "v_exp_f32 v27, v27\n\t" \
  "v_exp_f32 v28, v28\n\t" \
  "v_exp_f32 v29, v29\n\t" \
  "v_smfmac_f32_32x32x32_bf16 v[112:127], v[92:95], v[16:23], v144\n\t" \
  "s_waitcnt lgkmcnt(0)\n\t" \
  "s_add_u32 s66, s66, s69\n\t" \
  "s_addc_u32 s67, s67, 0\n\t" \
  "global_load_ushort v170, v208, s[66:67]\n\t" \
  "v_exp_f32 v30, v30\n\t" \
  "v_exp_f32 v31, v31\n\t" \
  "v_exp_f32 v32, v32\n\t" \
  "v_exp_f32 v33, v33\n\t" \
  "v_smfmac_f32_32x32x32_bf16 v[112:127], v[88:91], v[24:31], v144\n\t" \
  "global_load_ushort v178, v208, s[66:67] offset:1280\n\t" \
  "v_exp_f32 v34, v34\n\t" \
  "v_exp_f32 v35, v35\n\t" \
  "v_exp_f32 v36, v36\n\t" \
  "v_exp_f32 v37, v37\n\t" \
  "v_mfma_f32_32x32x16_bf16 v[48:63], v[80:83], v[100:103], v[48:63]\n\t" \
  "s_add_u32 s60, s66, s68\n\t" \
  "s_addc_u32 s61, s67, 0\n\t" \
  "global_load_ushort v171, v208, s[60:61]\n\t" \
  "v_exp_f32 v38, v38\n\t" \
  "v_exp_f32 v39, v39\n\t" \
  "v_exp_f32 v40, v40\n\t" \
  "v_exp_f32 v41, v41\n\t" \
  "v_mfma_f32_32x32x16_bf16 v[64:79], v[80:83], v[104:107], v[64:79]\n\t" \
  "ds_read_b128 v[80:83], v110 offset:10240\n\t" \
  "v_exp_f32 v42, v42\n\t" \
  "v_exp_f32 v43, v43\n\t" \
  "v_exp_f32 v44, v44\n\t" \
  "v_exp_f32 v45, v45\n\t" \
  "v_smfmac_f32_32x32x32_bf16 v[128:143], v[92:95], v[32:39], v144\n\t" \
  "global_load_ushort v179, v208, s[60:61] offset:1280\n\t" \
  "v_exp_f32 v46, v46\n\t" \
  "v_exp_f32 v47, v47\n\t" \
    \
  "v_exp_f32 v48, v48\n\t" \
  "v_exp_f32 v49, v49\n\t" \
  "v_smfmac_f32_32x32x32_bf16 v[128:143], v[88:91], v[40:47], v144\n\t" \
  "s_mov_b64 exec, s[34:35]\n\t" \
  "ds_read_b128 v[88:91], v110 offset:9600\n\t" \
  "ds_read_b128 v[92:95], v110 offset:13312\n\t" \
  "s_mov_b64 exec, -1\n\t" \
  "v_exp_f32 v50, v50\n\t" \
  "v_exp_f32 v51, v51\n\t" \
  "v_exp_f32 v52, v52\n\t" \
  "v_exp_f32 v53, v53\n\t" \
  "v_mfma_f32_32x32x4_2b_bf16 v[16:47], v[84:85], v[108:109], 4.0 cbsz:1 abid:0\n\t" \
  "v_exp_f32 v54, v54\n\t" \
  "v_exp_f32 v55, v55\n\t" \
  "v_exp_f32 v56, v56\n\t" \
  "v_exp_f32 v57, v57\n\t" \
  "v_exp_f32 v58, v58\n\t" \
  "v_exp_f32 v59, v59\n\t" \
  "v_exp_f32 v60, v60\n\t" \
  "v_exp_f32 v61, v61\n\t" \
  "v_smfmac_f32_32x32x32_bf16 v[112:127], v[96:99], v[48:55], v144\n\t" \
  "s_waitcnt lgkmcnt(0)\n\t" \
  "global_load_ushort v172, v209, s[70:71]\n\t" \
  "v_exp_f32 v62, v62\n\t" \
  "v_exp_f32 v63, v63\n\t" \
  "v_exp_f32 v64, v64\n\t" \
  "v_exp_f32 v65, v65\n\t" \
  "v_smfmac_f32_32x32x32_bf16 v[112:127], v[88:91], v[56:63], v144\n\t" \
  "global_load_ushort v180, v209, s[70:71] offset:1280\n\t" \
  "v_exp_f32 v66, v66\n\t" \
  "v_exp_f32 v67, v67\n\t" \
  "v_exp_f32 v68, v68\n\t" \
  "v_exp_f32 v69, v69\n\t" \
  "v_mfma_f32_32x32x16_bf16 v[16:31], v[80:83], v[100:103], v[16:31]\n\t" \
  "s_add_u32 s60, s70, s72\n\t" \
  "s_addc_u32 s61, s71, 0\n\t" \
  "global_load_ushort v173, v209, s[60:61]\n\t" \
  "v_exp_f32 v70, v70\n\t" \
  "v_exp_f32 v71, v71\n\t" \
  "v_exp_f32 v72, v72\n\t" \
  "v_exp_f32 v73, v73\n\t" \
  "v_mfma_f32_32x32x16_bf16 v[32:47], v[80:83], v[104:107], v[32:47]\n\t" \
  "ds_read_b128 v[80:83], v110 offset:11264\n\t" \
  "v_exp_f32 v74, v74\n\t" \
  "v_exp_f32 v75, v75\n\t" \
  "v_exp_f32 v76, v76\n\t" \
  "v_exp_f32 v77, v77\n\t" \
  "v_smfmac_f32_32x32x32_bf16 v[128:143], v[96:99], v[64:71], v144\n\t" \
  "global_load_ushort v181, v209, s[60:61] offset:1280\n\t" \
  "v_exp_f32 v78, v78\n\t" \
  "v_exp_f32 v79, v79\n\t" \
    \
  "v_exp_f32 v16, v16\n\t" \
  "v_exp_f32 v17, v17\n\t" \
  "v_smfmac_f32_32x32x32_bf16 v[128:143], v[88:91], v[72:79], v144\n\t" \
  "s_mov_b64 exec, s[34:35]\n\t" \
  "ds_read_b128 v[88:91], v110 offset:13952\n\t" \
  "ds_read_b128 v[96:99], v110 offset:16640\n\t" \
  "s_mov_b64 exec, -1\n\t" \
  "v_exp_f32 v18, v18\n\t" \
  "v_exp_f32 v19, v19\n\t" \
  "v_exp_f32 v20, v20\n\t" \
  "v_exp_f32 v21, v21\n\t" \
  "v_mfma_f32_32x32x4_2b_bf16 v[48:79], v[84:85], v[108:109], 4.0 cbsz:1 abid:1\n\t" \
  "v_exp_f32 v22, v22\n\t" \
  "v_exp_f32 v23, v23\n\t" \
  "v_exp_f32 v24, v24\n\t" \
  "v_exp_f32 v25, v25\n\t" \
  "v_exp_f32 v26, v26\n\t" \
  "v_exp_f32 v27, v27\n\t" \
  "v_exp_f32 v28, v28\n\t" \
  "v_exp_f32 v29, v29\n\t" \
  "v_smfmac_f32_32x32x32_bf16 v[112:127], v[92:95], v[16:23], v144\n\t" \
  "s_waitcnt lgkmcnt(0)\n\t" \
  "s_add_u32 s70, s70, s73\n\t" \
  "s_addc_u32 s71, s71, 0\n\t" \
  "global_load_ushort v174, v209, s[70:71]\n\t" \
  "v_exp_f32 v30, v30\n\t" \
  "v_exp_f32 v31, v31\n\t" \
  "v_exp_f32 v32, v32\n\t" \
  "v_exp_f32 v33, v33\n\t" \
  "v_smfmac_f32_32x32x32_bf16 v[112:127], v[88:91], v[24:31], v144\n\t" \
  "global_load_ushort v182, v209, s[70:71] offset:1280\n\t" \
  "v_exp_f32 v34, v34\n\t" \
  "v_exp_f32 v35, v35\n\t" \
  "v_exp_f32 v36, v36\n\t" \
  "v_exp_f32 v37, v37\n\t" \
  "v_mfma_f32_32x32x16_bf16 v[48:63], v[80:83], v[100:103], v[48:63]\n\t" \
  "s_add_u32 s60, s70, s72\n\t" \
  "s_addc_u32 s61, s71, 0\n\t" \
  "global_load_ushort v175, v209, s[60:61]\n\t" \
  "v_exp_f32 v38, v38\n\t" \
  "v_exp_f32 v39, v39\n\t" \
  "v_exp_f32 v40, v40\n\t" \
  "v_exp_f32 v41, v41\n\t" \
  "v_mfma_f32_32x32x16_bf16 v[64:79], v[80:83], v[104:107], v[64:79]\n\t" \
  "ds_read_b128 v[80:83], v110 offset:14592\n\t" \
  "v_exp_f32 v42, v42\n\t" \
  "v_exp_f32 v43, v43\n\t" \
  "v_exp_f32 v44, v44\n\t" \
  "v_exp_f32 v45, v45\n\t" \
  "v_smfmac_f32_32x32x32_bf16 v[128:143], v[92:95], v[32:39], v144\n\t" \
  "global_load_ushort v183, v209, s[60:61] offset:1280\n\t" \
  "v_exp_f32 v46, v46\n\t" \
  "v_exp_f32 v47, v47\n\t" \
    \
  "v_exp_f32 v48, v48\n\t" \
  "v_exp_f32 v49, v49\n\t" \
  "v_smfmac_f32_32x32x32_bf16 v[128:143], v[88:91], v[40:47], v144\n\t" \
  "s_mov_b64 exec, s[34:35]\n\t" \
  "ds_read_b128 v[88:91], v110 offset:17280\n\t" \
  "ds_read_b128 v[92:95], v110 offset:17920\n\t" \
  "s_mov_b64 exec, -1\n\t" \
  "v_exp_f32 v50, v50\n\t" \
  "v_exp_f32 v51, v51\n\t" \
  "v_exp_f32 v52, v52\n\t" \
  "v_exp_f32 v53, v53\n\t" \
  "v_mfma_f32_32x32x4_2b_bf16 v[16:47], v[86:87], v[108:109], 4.0 cbsz:1 abid:0\n\t" \
  "v_exp_f32 v54, v54\n\t" \
  "v_exp_f32 v55, v55\n\t" \
  "v_exp_f32 v56, v56\n\t" \
  "v_exp_f32 v57, v57\n\t" \
  "v_exp_f32 v58, v58\n\t" \
  "v_exp_f32 v59, v59\n\t" \
  "v_exp_f32 v60, v60\n\t" \
  "v_exp_f32 v61, v61\n\t" \
  "v_smfmac_f32_32x32x32_bf16 v[112:127], v[96:99], v[48:55], v144\n\t" \
  "s_waitcnt lgkmcnt(0)\n\t" \
  "buffer_store_dwordx4 v[200:203], v14, s[28:31], 0 offen sc1\n\t" \
  "v_exp_f32 v62, v62\n\t" \
  "v_exp_f32 v63, v63\n\t" \
  "v_exp_f32 v64, v64\n\t" \
  "v_exp_f32 v65, v65\n\t" \
  "v_smfmac_f32_32x32x32_bf16 v[112:127], v[88:91], v[56:63], v144\n\t" \
  "buffer_store_dwordx4 v[200:203], v14, s[28:31], 0 offen offset:1024 sc1\n\t" \
  "v_exp_f32 v66, v66\n\t" \
  "v_exp_f32 v67, v67\n\t" \
  "v_exp_f32 v68, v68\n\t" \
  "v_exp_f32 v69, v69\n\t" \
  "v_mfma_f32_32x32x16_bf16 v[16:31], v[80:83], v[100:103], v[16:31]\n\t" \
  "buffer_store_dwordx2 v[200:201], v15, s[28:31], 0 offen offset:2048 sc1\n\t" \
  "v_exp_f32 v70, v70\n\t" \
  "v_exp_f32 v71, v71\n\t" \
  "v_exp_f32 v72, v72\n\t" \
  "v_exp_f32 v73, v73\n\t" \
  "v_mfma_f32_32x32x16_bf16 v[32:47], v[80:83], v[104:107], v[32:47]\n\t" \
  "ds_read_b128 v[80:83], v110 offset:15616\n\t" \
  "v_exp_f32 v74, v74\n\t" \
  "v_exp_f32 v75, v75\n\t" \
  "v_exp_f32 v76, v76\n\t" \
  "v_exp_f32 v77, v77\n\t" \
  "v_smfmac_f32_32x32x32_bf16 v[128:143], v[96:99], v[64:71], v144\n\t" \
  "buffer_store_dwordx4 v[200:203], v206, s[28:31], 0 offen sc1\n\t" \
  "v_exp_f32 v78, v78\n\t" \
  "v_exp_f32 v79, v79\n\t" \
    \
  "v_exp_f32 v16, v16\n\t" \
  "v_exp_f32 v17, v17\n\t" \
  "v_smfmac_f32_32x32x32_bf16 v[128:143], v[88:91], v[72:79], v144\n\t" \
  "s_mov_b64 exec, s[34:35]\n\t" \
  "ds_read_b128 v[88:91], v110 offset:18560\n\t" \
  "ds_read_b128 v[96:99], v110 offset:19200\n\t" \
  "s_mov_b64 exec, -1\n\t" \
  "v_exp_f32 v18, v18\n\t" \
  "v_exp_f32 v19, v19\n\t" \
  "v_exp_f32 v20, v20\n\t" \
  "v_exp_f32 v21, v21\n\t" \
  "v_mfma_f32_32x32x4_2b_bf16 v[48:79], v[86:87], v[108:109], 4.0 cbsz:1 abid:1\n\t" \
  "ds_read2st64_b64 v[84:87], v111 offset0:44 offset1:45\n\t" \
  "v_exp_f32 v22, v22\n\t" \
  "v_exp_f32 v23, v23\n\t" \
  "v_exp_f32 v24, v24\n\t" \
  "v_exp_f32 v25, v25\n\t" \
  "v_exp_f32 v26, v26\n\t" \
  "v_exp_f32 v27, v27\n\t" \
  "v_exp_f32 v28, v28\n\t" \
  "v_exp_f32 v29, v29\n\t" \
  "v_smfmac_f32_32x32x32_bf16 v[112:127], v[92:95], v[16:23], v144\n\t" \
  "s_waitcnt lgkmcnt(0)\n\t" \
  "buffer_store_dwordx4 v[200:203], v206, s[28:31], 0 offen offset:1024 sc1\n\t" \
  "v_exp_f32 v30, v30\n\t" \
  "v_exp_f32 v31, v31\n\t" \
  "v_exp_f32 v32, v32\n\t" \
  "v_exp_f32 v33, v33\n\t" \
  "v_smfmac_f32_32x32x32_bf16 v[112:127], v[88:91], v[24:31], v144\n\t" \
  "buffer_store_dwordx2 v[200:201], v207, s[28:31], 0 offen offset:2048 sc1\n\t" \
  "v_exp_f32 v34, v34\n\t" \
  "v_exp_f32 v35, v35\n\t" \
  "v_exp_f32 v36, v36\n\t" \
  "v_exp_f32 v37, v37\n\t" \
  "v_mfma_f32_32x32x16_bf16 v[48:63], v[80:83], v[100:103], v[48:63]\n\t" \
  "v_exp_f32 v38, v38\n\t" \
  "v_exp_f32 v39, v39\n\t" \
  "v_exp_f32 v40, v40\n\t" \
  "v_exp_f32 v41, v41\n\t" \
  "v_mfma_f32_32x32x16_bf16 v[64:79], v[80:83], v[104:107], v[64:79]\n\t" \
  "ds_read_b128 v[80:83], v110 offset:20480\n\t" \
  "v_exp_f32 v42, v42\n\t" \
  "v_exp_f32 v43, v43\n\t" \
  "v_exp_f32 v44, v44\n\t" \
  "v_exp_f32 v45, v45\n\t" \
  "v_smfmac_f32_32x32x32_bf16 v[128:143], v[92:95], v[32:39], v144\n\t" \
  "v_exp_f32 v46, v46\n\t" \
  "v_exp_f32 v47, v47\n\t" \
    \
  "v_exp_f32 v48, v48\n\t" \
  "v_exp_f32 v49, v49\n\t" \
  "v_smfmac_f32_32x32x32_bf16 v[128:143], v[88:91], v[40:47], v144\n\t" \
  "s_mov_b64 exec, s[34:35]\n\t" \
  "ds_read_b128 v[88:91], v110 offset:19840\n\t" \
  "ds_read_b128 v[92:95], v110 offset:23552\n\t" \
  "s_mov_b64 exec, -1\n\t" \
  "v_exp_f32 v50, v50\n\t" \
  "v_exp_f32 v51, v51\n\t" \
  "v_exp_f32 v52, v52\n\t" \
  "v_exp_f32 v53, v53\n\t" \
  "v_mfma_f32_32x32x4_2b_bf16 v[16:47], v[84:85], v[108:109], 4.0 cbsz:1 abid:0\n\t" \
  "v_exp_f32 v54, v54\n\t" \
  "v_exp_f32 v55, v55\n\t" \
  "v_exp_f32 v56, v56\n\t" \
  "v_exp_f32 v57, v57\n\t" \
  "v_exp_f32 v58, v58\n\t" \
  "v_exp_f32 v59, v59\n\t" \
  "v_exp_f32 v60, v60\n\t" \
  "v_exp_f32 v61, v61\n\t" \
  "v_smfmac_f32_32x32x32_bf16 v[112:127], v[96:99], v[48:55], v144\n\t" \
  "s_waitcnt lgkmcnt(0)\n\t" \
  "v_exp_f32 v62, v62\n\t" \
  "v_exp_f32 v63, v63\n\t" \
  "v_exp_f32 v64, v64\n\t" \
  "v_exp_f32 v65, v65\n\t" \
  "v_smfmac_f32_32x32x32_bf16 v[112:127], v[88:91], v[56:63], v144\n\t" \
  "v_exp_f32 v66, v66\n\t" \
  "v_exp_f32 v67, v67\n\t" \
  "v_exp_f32 v68, v68\n\t" \
  "v_exp_f32 v69, v69\n\t" \
  "v_mfma_f32_32x32x16_bf16 v[16:31], v[80:83], v[100:103], v[16:31]\n\t" \
  "v_exp_f32 v70, v70\n\t" \
  "v_exp_f32 v71, v71\n\t" \
  "v_exp_f32 v72, v72\n\t" \
  "v_exp_f32 v73, v73\n\t" \
  "v_mfma_f32_32x32x16_bf16 v[32:47], v[80:83], v[104:107], v[32:47]\n\t" \
  "ds_read_b128 v[80:83], v110 offset:21504\n\t" \
  "v_exp_f32 v74, v74\n\t" \
  "v_exp_f32 v75, v75\n\t" \
  "v_exp_f32 v76, v76\n\t" \
  "v_exp_f32 v77, v77\n\t" \
  "v_smfmac_f32_32x32x32_bf16 v[128:143], v[96:99], v[64:71], v144\n\t" \
  "v_exp_f32 v78, v78\n\t" \
  "v_exp_f32 v79, v79\n\t" \
  "s_branch 2b\n\t" \
    \
  "100:\n\t" \
  "v_xor_b32 v9, 64, v8\n\t" \
  "v_add_u32 v9, 9216, v9\n\t" \
  "v_add_u32 v10, 18432, v8\n\t" \
  "v_add_u32 v11, 18432, v9\n\t" \
  "s_bfe_u32 s6, s2, 0x20003\n\t" \
  "s_bfe_u32 s10, s2, 0x40003\n\t" \
  "v_and_b32 v3, 31, v0\n\t" \
  "v_bfe_u32 v4, v0, 5, 1\n\t" \
  "s_sub_u32 s57, s3, 5\n\t" \
  "s_lshl_b32 s50, s57, 9\n\t" \
  "v_add_u32 v5, s50, v1\n\t" \
  "v_add_u32 v6, 0, v5\n\t" \
  "v_mul_u32_u24 v12, 3277, v6\n\t" \
  "v_lshrrev_b32 v12, 16, v12\n\t" \
  "v_mad_i32_i24 v6, v12, -16, v6\n\t" \
  "v_mad_i32_i24 v6, v12, -4, v6\n\t" \
  "v_bfe_u32 v13, v12, 3, 1\n\t" \
  "v_mad_i32_i24 v6, v13, -2, v6\n\t" \
  "v_add_u32 v13, 20, v6\n\t" \
  "v_min_u32 v6, v6, v13\n\t" \
  "v_mul_u32_u24 v12, 1280, v12\n\t" \
  "v_lshl_add_u32 v148, v6, 4, v12\n\t" \
  "v_add_u32 v6, 64, v5\n\t" \
  "v_mul_u32_u24 v12, 3277, v6\n\t" \
  "v_lshrrev_b32 v12, 16, v12\n\t" \
  "v_mad_i32_i24 v6, v12, -16, v6\n\t" \
  "v_mad_i32_i24 v6, v12, -4, v6\n\t" \
  "v_bfe_u32 v13, v12, 3, 1\n\t" \
  "v_mad_i32_i24 v6, v13, -2, v6\n\t" \
  "v_add_u32 v13, 20, v6\n\t" \
  "v_min_u32 v6, v6, v13\n\t" \
  "v_mul_u32_u24 v12, 1280, v12\n\t" \
  "v_lshl_add_u32 v149, v6, 4, v12\n\t" \
  "v_subrev_u32 v149, 1024, v149\n\t" \
  "v_add_u32 v6, 128, v5\n\t" \
  "v_mul_u32_u24 v12, 3277, v6\n\t" \
  "v_lshrrev_b32 v12, 16, v12\n\t" \
  "v_mad_i32_i24 v6, v12, -16, v6\n\t" \
  "v_mad_i32_i24 v6, v12, -4, v6\n\t" \
  "v_bfe_u32 v13, v12, 3, 1\n\t" \
  "v_mad_i32_i24 v6, v13, -2, v6\n\t" \
  "v_add_u32 v13, 20, v6\n\t" \
  "v_min_u32 v6, v6, v13\n\t" \
  "v_mul_u32_u24 v12, 1280, v12\n\t" \
  "v_lshl_add_u32 v150, v6, 4, v12\n\t" \
  "v_subrev_u32 v150, 2048, v150\n\t" \
  "v_add_u32 v6, 192, v5\n\t" \
  "v_mul_u32_u24 v12, 3277, v6\n\t" \
  "v_lshrrev_b32 v12, 16, v12\n\t" \
  "v_mad_i32_i24 v6, v12, -16, v6\n\t" \
  "v_mad_i32_i24 v6, v12, -4, v6\n\t" \
  "v_bfe_u32 v13, v12, 3, 1\n\t" \
  "v_mad_i32_i24 v6, v13, -2, v6\n\t" \
  "v_add_u32 v13, 20, v6\n\t" \
  "v_min_u32 v6, v6, v13\n\t" \
  "v_mul_u32_u24 v12, 1280, v12\n\t" \
  "v_lshl_add_u32 v151, v6, 4, v12\n\t" \
  "v_subrev_u32 v151, 3072, v151\n\t" \
  "v_add_u32 v6, 256, v5\n\t" \
  "v_mul_u32_u24 v12, 3277, v6\n\t" \
  "v_lshrrev_b32 v12, 16, v12\n\t" \
  "v_mad_i32_i24 v6, v12, -16, v6\n\t" \
  "v_mad_i32_i24 v6, v12, -4, v6\n\t" \
  "v_bfe_u32 v13, v12, 3, 1\n\t" \
  "v_mad_i32_i24 v6, v13, -2, v6\n\t" \
  "v_add_u32 v13, 20, v6\n\t" \
  "v_min_u32 v6, v6, v13\n\t" \
  "v_mul_u32_u24 v12, 1280, v12\n\t" \
  "v_lshl_add_u32 v152, v6, 4, v12\n\t" \
  "v_add_u32 v6, 320, v5\n\t" \
  "v_mul_u32_u24 v12, 3277, v6\n\t" \
  "v_lshrrev_b32 v12, 16, v12\n\t" \
  "v_mad_i32_i24 v6, v12, -16, v6\n\t" \
  "v_mad_i32_i24 v6, v12, -4, v6\n\t" \
  "v_bfe_u32 v13, v12, 3, 1\n\t" \
  "v_mad_i32_i24 v6, v13, -2, v6\n\t" \
  "v_add_u32 v13, 20, v6\n\t" \
  "v_min_u32 v6, v6, v13\n\t" \
  "v_mul_u32_u24 v12, 1280, v12\n\t" \
  "v_lshl_add_u32 v153, v6, 4, v12\n\t" \
  "v_subrev_u32 v153, 1024, v153\n\t" \
  "v_add_u32 v6, 384, v5\n\t" \
  "v_mul_u32_u24 v12, 3277, v6\n\t" \
  "v_lshrrev_b32 v12, 16, v12\n\t" \
  "v_mad_i32_i24 v6, v12, -16, v6\n\t" \
  "v_mad_i32_i24 v6, v12, -4, v6\n\t" \
  "v_bfe_u32 v13, v12, 3, 1\n\t" \
  "v_mad_i32_i24 v6, v13, -2, v6\n\t" \
  "v_add_u32 v13, 20, v6\n\t" \
  "v_min_u32 v6, v6, v13\n\t" \
  "v_mul_u32_u24 v12, 1280, v12\n\t" \
  "v_lshl_add_u32 v154, v6, 4, v12\n\t" \
  "v_subrev_u32 v154, 2048, v154\n\t" \
  "v_add_u32 v6, 448, v5\n\t" \
  "v_mul_u32_u24 v12, 3277, v6\n\t" \
  "v_lshrrev_b32 v12, 16, v12\n\t" \
  "v_mad_i32_i24 v6, v12, -16, v6\n\t" \
  "v_mad_i32_i24 v6, v12, -4, v6\n\t" \
  "v_bfe_u32 v13, v12, 3, 1\n\t" \
  "v_mad_i32_i24 v6, v13, -2, v6\n\t" \
  "v_add_u32 v13, 20, v6\n\t" \
  "v_min_u32 v6, v6, v13\n\t" \
  "v_mul_u32_u24 v12, 1280, v12\n\t" \
  "v_lshl_add_u32 v155, v6, 4, v12\n\t" \
  "v_subrev_u32 v155, 3072, v155\n\t" \
  "s_cmp_eq_u32 s57, 2\n\t" \
  "s_cselect_b64 vcc, -1, 0\n\t" \
  "v_cndmask_b32 v152, v152, v8, vcc\n\t" \
  "v_cndmask_b32 v153, v153, v9, vcc\n\t" \
  "v_cndmask_b32 v154, v154, v10, vcc\n\t" \
  "v_cndmask_b32 v155, v155, v11, vcc\n\t" \
  "s_waitcnt lgkmcnt(0)\n\t" \
  "s_load_dwordx2 s[64:65], s[14:15], 0x0\n\t" \
  "s_add_u32 s12, s12, s52\n\t" \
  "s_addc_u32 s13, s13, 0\n\t" \
  "s_mul_i32 s50, s6, 320\n\t" \
  "s_add_u32 s18, s18, s50\n\t" \
  "s_addc_u32 s19, s19, 0\n\t" \
  "s_xor_b32 s50, s7, 1\n\t" \
  "s_mul_i32 s56, s50, 40960\n\t" \
  "s_add_u32 s12, s12, s56\n\t" \
  "s_addc_u32 s13, s13, 0\n\t" \
  "s_add_u32 s62, s56, 20480\n\t" \
  "s_lshl_b32 s8, s57, 13\n\t" \
  "s_add_u32 s8, s8, 102400\n\t" \
  "s_mov_b32 s7, 0\n\t" \
  "s_mov_b32 s40, 0\n\t" \
  "s_sleep 10\n\t" \
  "105:\n\t" \
  "s_mul_i32 s50, s9, 81920\n\t" \
  "s_add_u32 s58, s18, s50\n\t" \
  "s_addc_u32 s59, s19, 0\n\t" \
  "s_add_u32 m0, s8, s7\n\t" \
  "s_lshl_b32 s50, s9, 7\n\t" \
  "global_load_lds_dwordx4 v148, s[58:59]\n\t" \
  "global_load_lds_dwordx4 v149, s[58:59] offset:1024\n\t" \
  "global_load_lds_dwordx4 v150, s[58:59] offset:2048\n\t" \
  "global_load_lds_dwordx4 v151, s[58:59] offset:3072\n\t" \
  "s_add_u32 s60, s12, s50\n\t" \
  "s_addc_u32 s61, s13, 0\n\t" \
  "s_lshl_b32 s50, s40, 12\n\t" \
  "s_add_u32 s50, s50, s62\n\t" \
  "s_add_u32 s51, m0, 4096\n\t" \
  "s_cmp_eq_u32 s3, 7\n\t" \
  "s_cselect_b32 m0, s50, s51\n\t" \
  "s_cselect_b64 s[58:59], s[60:61], s[58:59]\n\t" \
  "global_load_lds_dwordx4 v152, s[58:59]\n\t" \
  "global_load_lds_dwordx4 v153, s[58:59] offset:1024\n\t" \
  "global_load_lds_dwordx4 v154, s[58:59] offset:2048\n\t" \
  "global_load_lds_dwordx4 v155, s[58:59] offset:3072\n\t" \
  "s_addk_i32 s9, 1\n\t" \
  "s_cmp_eq_u32 s9, 10\n\t" \
  "s_cselect_b32 s9, 0, s9\n\t" \
  "s_addk_i32 s7, 20480\n\t" \
  "s_cmp_eq_u32 s7, 61440\n\t" \
  "s_cselect_b32 s7, 0, s7\n\t" \
  "s_addk_i32 s40, 1\n\t" \
  "s_cmp_eq_u32 s40, 1\n\t" \
  "s_cbranch_scc1 105b\n\t" \
  "s_cmp_eq_u32 s40, 2\n\t" \
  "s_cbranch_scc0 106f\n\t" \
  "v_mov_b64 v[72:73], 0\n\t" \
  "v_mov_b64 v[74:75], 0\n\t" \
  "v_mov_b64 v[76:77], 0\n\t" \
  "v_mov_b64 v[78:79], 0\n\t" \
  "v_and_b32 v10, 16, v1\n\t" \
  "v_cmp_ne_u32 vcc, 0, v10\n\t" \
  "v_mov_b32 v8, 0x0c0a0200\n\t" \
  "v_mov_b32 v10, 0x3a083412\n\t" \
  "v_cndmask_b32 v8, v8, v10, vcc\n\t" \
  "v_mov_b32 v9, 0x0604100e\n\t" \
  "v_mov_b32 v10, 0x38363e3c\n\t" \
  "v_cndmask_b32 v9, v9, v10, vcc\n\t" \
  "v_and_b32 v10, 14, v1\n\t" \
  "v_lshlrev_b32 v10, 2, v10\n\t" \
  "v_lshrrev_b64 v[8:9], v10, v[8:9]\n\t" \
  "v_and_b32 v8, 0xff, v8\n\t" \
  "v_cmp_gt_u32 vcc, 20, v8\n\t" \
  "v_and_b32 v10, 1, v1\n\t" \
  "v_add_u32 v8, v8, v10\n\t" \
  "v_cndmask_b32_e64 v10, -12, 20, vcc\n\t" \
  "v_cmp_lt_u32 vcc, 31, v1\n\t" \
  "s_nop 1\n\t" \
  "v_cndmask_b32 v10, 0, v10, vcc\n\t" \
  "v_add_u32 v110, v8, v10\n\t" \
  "v_mfma_f32_32x32x16_bf16 v[112:127], v[72:75], v[72:75], 0\n\t" \
  "v_lshlrev_b32 v110, 4, v110\n\t" \
  "v_lshlrev_b32 v111, 3, v1\n\t" \
  "v_and_b32 v8, 27, v1\n\t" \
  "v_cmp_eq_u32 vcc, 18, v8\n\t" \
  "v_mov_b32 v9, 0x3f803f80\n\t" \
  "v_mov_b32 v144, 0xdddddddd\n\t" \
  "v_cndmask_b32 v88, 0, v9, vcc\n\t" \
  "v_mov_b32 v89, v88\n\t" \
  "v_mfma_f32_32x32x16_bf16 v[128:143], v[72:75], v[72:75], 0\n\t" \
  "v_mov_b64 v[90:91], v[88:89]\n\t" \
  "v_mov_b64 v[92:93], v[88:89]\n\t" \
  "v_mov_b64 v[94:95], v[88:89]\n\t" \
  "v_mov_b64 v[96:97], v[88:89]\n\t" \
  "v_mov_b64 v[98:99], v[88:89]\n\t" \
  "s_mov_b32 s34, 0x0033ffff\n\t" \
  "s_mov_b32 s35, 0x0033ffff\n\t" \
  "s_mov_b32 s28, s14\n\t" \
  "s_and_b32 s29, s15, 0xffff\n\t" \
  "s_mov_b32 s30, 7274496\n\t" \
  "s_mov_b32 s31, 0x20000\n\t" \
  "s_cmp_lt_u32 s3, 4\n\t" \
  "s_cselect_b32 s41, 1, 0\n\t" \
  "s_lshl_b32 s54, s4, 5\n\t" \
  "s_add_u32 s54, s54, s5\n\t" \
  "s_and_b32 s53, s3, 1\n\t" \
  "s_lshl1_add_u32 s54, s54, s53\n\t" \
  "s_lshl_b32 s54, s54, 7\n\t" \
  "s_add_u32 s54, s54, 16384\n\t" \
  "v_lshl_add_u32 v145, v3, 2, s54\n\t" \
  "s_mul_i32 s54, s4, 163840\n\t" \
  "s_add_u32 s54, s54, 65536\n\t" \
  "s_add_u32 s48, s14, s54\n\t" \
  "s_addc_u32 s49, s15, 0\n\t" \
  "s_waitcnt lgkmcnt(0)\n\t" \
  "s_add_u32 s64, s64, 1\n\t" \
  "s_addc_u32 s65, s65, 0\n\t" \
  "106:\n\t" \
  "s_cmp_lt_u32 s40, 10\n\t" \
  "s_cbranch_scc0 108f\n\t" \
  "s_waitcnt vmcnt(8)\n\t" \
  "s_barrier\n\t" \
  "s_branch 105b\n\t" \
  "108:\n\t" \
  "s_waitcnt vmcnt(8)\n\t" \
  "s_barrier\n\t" \
  "s_waitcnt vmcnt(0)\n\t" \
  "s_barrier\n\t" \
  "s_branch 120b\n\t" \
  "99:\n\t"

#define KERNEL_CLOB "v1","v2","v3","v4","v5","v6","v8","v9","v10","v11","v12","v13","v14","v15","v16","v17","v18","v19","v20","v21","v22","v23","v24","v25","v26","v27","v28","v29","v30","v31","v32","v33","v34","v35","v36","v37","v38","v39","v40","v41","v42","v43","v44","v45","v46","v47","v48","v49","v50","v51","v52","v53","v54","v55","v56","v57","v58","v59","v60","v61","v62","v63","v64","v65","v66","v67","v68","v69","v70","v71","v72","v73","v74","v75","v76","v77","v78","v79","v80","v81","v82","v83","v84","v85","v86","v87","v88","v89","v90","v91","v92","v93","v94","v95","v96","v97","v98","v99","v100","v101","v102","v103","v104","v105","v106","v107","v108","v109","v110","v111","v112","v113","v114","v115","v116","v117","v118","v119","v120","v121","v122","v123","v124","v125","v126","v127","v128","v129","v130","v131","v132","v133","v134","v135","v136","v137","v138","v139","v140","v141","v142","v143","v144","v145","v146","v147","v148","v149","v150","v151","v152","v153","v154","v155","v156","v157","v158","v159","v160","v161","v162","v163","v164","v165","v166","v167","v168","v169","v170","v171","v172","v173","v174","v175","v176","v177","v178","v179","v180","v181","v182","v183","v184","v185","v186","v187","v188","v189","v190","v191","v192","v193","v194","v195","v196","v197","v198","v199","v200","v201","v202","v203","v204","v205","v206","v207","v208","v209","v210","v211","v212","v213","v214","v215","v216","v217","v218","v219","v220","v221","v222","v223","v224","v225","v226","v227","v228","v229","v230","v231","v232","v233","v234","v235","v236","v237","v238","v239","v240","v241","v242","v243","v244","v245","v246","v247","v248","v249","v250","v251","v252","v253","v254","v255","s3","s4","s5","s6","s7","s8","s9","s10","s11","s12","s13","s14","s15","s16","s17","s18","s19","s20","s21","s22","s23","s24","s25","s26","s27","s28","s29","s30","s31","s34","s35","s36","s37","s38","s39","s40","s41","s42","s43","s44","s45","s46","s47","s48","s49","s50","s51","s52","s53","s54","s55","s56","s57","s58","s59","s60","s61","s62","s63","s64","s65","s66","s67","s68","s69","s70","s71","s72","s73"

typedef unsigned short u16;
struct Params { const u16* x; char* ws; const u16* gw; u16* out; const u16* wo[2]; const u16* wq[2]; const u16* wk[2]; const u16* wv[2]; };

__global__ void __launch_bounds__(512, 2) gated_mix__grouped_kv_mqa__grouped_kv_mqa__prompt_kernel(Params p) {
  __shared__ __attribute__((aligned(1024))) char smem[163840];
  unsigned tid = threadIdx.x;
  asm volatile(KERNEL_ASM
      : "+{v0}"(tid)
      : "{s[0:1]}"(__builtin_amdgcn_kernarg_segment_ptr()), "{s2}"(blockIdx.x), "s"((unsigned)(uintptr_t)(__attribute__((address_space(3))) char*)smem)
      : KERNEL_CLOB, "vcc", "scc", "memory");
}

extern "C" void kernel_launch(void* const* d_in, const int* in_sizes, int n_in, void* d_out, int out_size, void* d_ws, size_t ws_size, hipStream_t stream) {
  Params p;
  p.x = (const u16*)d_in[0];
  p.wq[0] = (const u16*)d_in[1]; p.wk[0] = (const u16*)d_in[2]; p.wv[0] = (const u16*)d_in[3]; p.wo[0] = (const u16*)d_in[4];
  p.wq[1] = (const u16*)d_in[6]; p.wk[1] = (const u16*)d_in[7]; p.wv[1] = (const u16*)d_in[8]; p.wo[1] = (const u16*)d_in[9];
  p.gw = (const u16*)d_in[10]; p.out = (u16*)d_out; p.ws = (char*)d_ws;
  gated_mix__grouped_kv_mqa__grouped_kv_mqa__prompt_kernel<<<256, 512, 0, stream>>>(p);
}
```
